```python
import torch

def _softmax(x, axis=-1):
    return torch.softmax(x, dim=axis)
def _gelu(x):
    return torch.nn.functional.gelu(x, approximate="tanh")
def _silu(x):
    return torch.nn.functional.silu(x)
def _sigmoid(x):
    return torch.sigmoid(x)
def _layernorm(x, axis=-1, eps=1e-5):
    m = x.mean(dim=axis, keepdim=True)
    v = x.var(dim=axis, keepdim=True, unbiased=False)
    return (x - m) / torch.sqrt(v + eps)
def _scatter_add(tgt, idx, src, axis=0):
    return tgt.clone().index_add_(axis, idx, src)
def _segment_sum(src, seg, n):
    out = torch.zeros((n,) + src.shape[1:], dtype=src.dtype)
    return out.index_add_(0, seg, src)
def _pad(x, pw):
    flat = tuple(v for pair in reversed(pw) for v in pair)
    return torch.nn.functional.pad(x, flat)

kernel_name = 'sequential_preLN__ssm_conv_s4__mixer_s_mlp__prompt'

def setup_inputs():

    _w = (lambda a: (lambda t: t.to(torch.bfloat16) if t.is_floating_point() else t)(torch.as_tensor(a)))
    def _A_setup():
        B, L, D, N = 1, 2048, 576, 128
        A_re = -(lambda _x: torch.exp(torch.as_tensor(_x)))((lambda *s: torch.randn(*s, dtype=torch.bfloat16) * 3.2)(D, N))
        A_im = torch.pi * (lambda *s: torch.randn(*s, dtype=torch.bfloat16) * 0.05)(D, N)
        B_re = (lambda *s: torch.randn(*s, dtype=torch.bfloat16) * 0.1)(D, N)
        B_im = (lambda *s: torch.randn(*s, dtype=torch.bfloat16) * 0.1)(D, N)
        C_re = (lambda *s: torch.randn(*s, dtype=torch.bfloat16) * 0.1)(D, N)
        C_im = (lambda *s: torch.randn(*s, dtype=torch.bfloat16) * 0.1)(D, N)
        log_dt = (lambda *s: torch.randn(*s, dtype=torch.bfloat16) * 0.05)(D) * 0.5 - 4.0
        Dskip = (lambda *s: torch.randn(*s, dtype=torch.bfloat16) * 0.05)(D)
        return {
            "u": _w((lambda *s: torch.randn(*s, dtype=torch.bfloat16) * 0.05)(B, L, D)),
            "A_re": _w(A_re),
            "A_im": _w(A_im),
            "B_re": _w(B_re),
            "B_im": _w(B_im),
            "C_re": _w(C_re),
            "C_im": _w(C_im),
            "log_dt": _w(log_dt),
            "Dskip": _w(Dskip),
        }
    _A_in = _A_setup()

    def _B_setup():
        B, S, D, F = 1, 2048, 576, 4608
        return {
            "x":  _w((lambda *s: torch.randn(*s, dtype=torch.bfloat16) * 0.05)(B, S, D)),
            "w1": _w((lambda *s: torch.randn(*s, dtype=torch.bfloat16) * 3.2)(D, D) * (D ** -0.5)),
            "w2": _w((lambda *s: torch.randn(*s, dtype=torch.bfloat16) * 3.2)(D, D) * (D ** -0.5)),
            "w3": _w((lambda *s: torch.randn(*s, dtype=torch.bfloat16) * 3.2)(D, F) * (D ** -0.5)),
            "w4": _w((lambda *s: torch.randn(*s, dtype=torch.bfloat16) * 1.6)(F, D) * (F ** -0.5)),
        }
    _B_in = _B_setup()

    return {
        "A_u": _A_in["u"],
        "A_A_re": _A_in["A_re"],
        "A_A_im": _A_in["A_im"],
        "A_B_re": _A_in["B_re"],
        "A_B_im": _A_in["B_im"],
        "A_C_re": _A_in["C_re"],
        "A_C_im": _A_in["C_im"],
        "A_log_dt": _A_in["log_dt"],
        "A_Dskip": _A_in["Dskip"],
        "B_x": _B_in["x"],
        "B_w1": _B_in["w1"],
        "B_w2": _B_in["w2"],
        "B_w3": _B_in["w3"],
        "B_w4": _B_in["w4"],
    }

def reference(A_u, A_A_re, A_A_im, A_B_re, A_B_im, A_C_re, A_C_im, A_log_dt, A_Dskip, B_x, B_w1, B_w2, B_w3, B_w4):
    def _A_ref(u, A_re, A_im, B_re, B_im, C_re, C_im, log_dt, Dskip):
        Bsz, L, D = u.shape
        dt = torch.exp(log_dt)[:, None]
        A = A_re + 1j * A_im
        Bc = B_re + 1j * B_im
        Cc = C_re + 1j * C_im
        logdA = dt * A
        dB = dt * Bc
        l = (lambda x, dt: x.to(dt))(torch.arange(L), A_re.dtype)
        P = torch.exp(logdA[:, :, None] * l[None, None, :])
        K = torch.einsum("dn,dnl,dn->dl", Cc, P, dB).real
        L2 = 2 * L
        Kp = _pad(K, ((0, 0), (0, L)))
        up = (lambda x, *d: x.permute(*d))(u, 0, 2, 1)
        up = _pad(up, ((0, 0), (0, 0), (0, L)))
        Kf = (lambda x, axes=(-2,-1): torch.fft.rfft2(x.float(), dim=axes))(Kp[:, None, :], axes=(-2, -1))
        Uf = (lambda x, axes=(-2,-1): torch.fft.rfft2(x.float(), dim=axes))(up[:, :, None, :], axes=(-2, -1))
        Yf = Uf * Kf[None, :, :, :]
        y = (lambda x, s, axes=(-2,-1): torch.fft.irfft2(x, s=s, dim=axes).to(torch.bfloat16))(Yf, (1, L2), axes=(-2, -1))[:, :, 0, :L]
        y = (lambda x, *d: x.permute(*d))(y, 0, 2, 1) + u * Dskip[None, None, :]
        return y

    def _B_ref(x, w1, w2, w3, w4):
        B, S, D = x.shape
        h = _gelu(_layernorm(x, axis=-1) @ w1)
        c = D // 4
        g0 = _pad(h[:, :, 0*c:1*c], ((0, 0), (1, 0), (0, 0)))[:, :S, :]
        g1 = _pad(h[:, :, 1*c:2*c], ((0, 0), (0, 1), (0, 0)))[:, 1:, :]
        g2 = _pad(h[:, :, 2*c:3*c], ((0, 0), (2, 0), (0, 0)))[:, :S, :]
        g3 = _pad(h[:, :, 3*c:   ], ((0, 0), (0, 2), (0, 0)))[:, 2:, :]
        hs = torch.concatenate([g0, g1, g2, g3], axis=-1) @ w2
        x1 = x + hs
        h2 = _gelu(_layernorm(x1, axis=-1) @ w3) @ w4
        return x1 + h2

    x = A_u
    h = x + _A_ref(_layernorm(x, axis=-1), A_A_re, A_A_im, A_B_re, A_B_im, A_C_re, A_C_im, A_log_dt, A_Dskip)
    h2 = h + _B_ref(_layernorm(h, axis=-1), B_w1, B_w2, B_w3, B_w4)
    return h2
```

```cpp
#include <hip/hip_runtime.h>
#include <hip/hip_bf16.h>
#include <stdint.h>

typedef unsigned short u16;
typedef short bf16x8 __attribute__((ext_vector_type(8)));
typedef short s16x4 __attribute__((ext_vector_type(4)));
typedef float f32x2 __attribute__((ext_vector_type(2)));
typedef float f32x4 __attribute__((ext_vector_type(4)));
typedef float f32x16 __attribute__((ext_vector_type(16)));
typedef unsigned u32x2 __attribute__((ext_vector_type(2)));
typedef unsigned u32x4 __attribute__((ext_vector_type(4)));
typedef __bf16 bf16x2_t __attribute__((ext_vector_type(2)));
#define LAS __attribute__((address_space(3)))

constexpr int L = 2048, D = 576, NS = 128, F = 4608;
constexpr int RSTR = 2112;
constexpr int WSUB = D * D;
constexpr float LN_EPS = 1e-5f;

constexpr size_t WS_UT = 0;
constexpr size_t WS_XT = WS_UT + (size_t)D * L * 2;
constexpr size_t WS_HT = WS_XT + (size_t)D * L * 2;
constexpr size_t WS_RK = WS_HT + (size_t)D * L * 2;
constexpr size_t WS_WF = WS_RK + (size_t)D * RSTR * 2;
constexpr size_t WS_CS = WS_WF + (size_t)18 * WSUB * 2;
constexpr size_t WS_ST2 = WS_CS + (size_t)2 * (D + F) * 4;
constexpr size_t WS_G = WS_ST2 + (size_t)L * 8;
constexpr size_t WS_X1 = WS_G + (size_t)(L + 4) * D * 2;
constexpr size_t WS_HR = WS_X1 + (size_t)L * D * 2;
constexpr size_t WS_PST = WS_HR + (size_t)L * D * 2;
constexpr size_t WS_PART = WS_PST + (size_t)L * 8 * 8;
constexpr size_t WS_END = WS_PART + (size_t)32 * 8 * 72 * 512 * 2;

__device__ __forceinline__ unsigned pk_bf16(float lo, float hi) {
    f32x2 v = {lo, hi};
    return __builtin_bit_cast(unsigned, __builtin_convertvector(v, bf16x2_t));
}
__device__ __forceinline__ float bf_lo(unsigned p) { return __uint_as_float(p << 16); }
__device__ __forceinline__ float bf_hi(unsigned p) { return __uint_as_float(p & 0xffff0000u); }
__device__ __forceinline__ float bf2f(u16 x) { return __uint_as_float((unsigned)x << 16); }
__device__ __forceinline__ float rbf(float x) { return bf_lo(pk_bf16(x, x)); }
__device__ __forceinline__ float dot2bf(unsigned a, unsigned b, float c) {
    return __builtin_amdgcn_fdot2_f32_bf16(__builtin_bit_cast(bf16x2_t, a), __builtin_bit_cast(bf16x2_t, b), c, false);
}
__device__ __forceinline__ float gelu_tanh(float x) {
    const float c0 = -2.0f * 0.7978845608028654f * 1.4426950408889634f;
    const float c1 = c0 * 0.044715f;
    float e = __builtin_amdgcn_exp2f(x * (c0 + c1 * x * x));
    return x * __builtin_amdgcn_rcpf(1.0f + e);
}
__device__ __forceinline__ s16x4 tr_read(const LAS char* p) {
    return __builtin_amdgcn_ds_read_tr16_b64_v4i16((LAS s16x4*)p);
}
__device__ __forceinline__ bf16x8 cat8(s16x4 a, s16x4 b) {
    return __builtin_shufflevector(a, b, 0, 1, 2, 3, 4, 5, 6, 7);
}
__device__ __forceinline__ void glds16(const void* g, LAS char* l) {
    __builtin_amdgcn_global_load_lds((const unsigned*)g, (LAS unsigned*)l, 16, 0, 0);
}
__device__ __forceinline__ int fsw(int row) { return ((row >> 1) & 1) | (((row >> 3) & 1) << 1); }
#define MFMA16(a, b, c) __builtin_amdgcn_mfma_f32_16x16x32_bf16((a), (b), (c), 0, 0, 0)
#define MFMA32(a, b, c) __builtin_amdgcn_mfma_f32_32x32x16_bf16((a), (b), (c), 0, 0, 0)

struct KAArgs {
    const u16* x; const u16 *are, *aim, *bre, *bim, *cre, *cim, *logdt;
    const u16 *w1, *w2, *w3, *w4;
    char* ws;
};

constexpr int KA_LN = 64, KA_KG = 288, KA_PREP = 324;
constexpr int KA_LDS = 32 * 1184;

__device__ __forceinline__ void ka_ln(const KAArgs& a, int job, LAS char* lds) {
    const int tid = threadIdx.x, w = tid >> 6, lane = tid & 63;
    const int row0 = job * 32, rl = w * 8 + (lane >> 3), c8 = lane & 7;
    const u32x4* src = (const u32x4*)(a.x + (size_t)(row0 + rl) * D);
    u32x4 raw[9];
#pragma unroll
    for (int i = 0; i < 9; ++i) raw[i] = src[c8 + 8 * i];
    float s = 0.f;
#pragma unroll
    for (int i = 0; i < 9; ++i)
#pragma unroll
        for (int k = 0; k < 4; ++k) s += bf_lo(raw[i][k]) + bf_hi(raw[i][k]);
    s += __shfl_xor(s, 1); s += __shfl_xor(s, 2); s += __shfl_xor(s, 4);
    const float mean = s * (1.0f / D);
    float q = 0.f;
#pragma unroll
    for (int i = 0; i < 9; ++i)
#pragma unroll
        for (int k = 0; k < 4; ++k) { float d0 = bf_lo(raw[i][k]) - mean, d1 = bf_hi(raw[i][k]) - mean; q += d0 * d0 + d1 * d1; }
    q += __shfl_xor(q, 1); q += __shfl_xor(q, 2); q += __shfl_xor(q, 4);
    const float mb = rbf(mean), sb = rbf(sqrtf(rbf(rbf(q * (1.0f / D)) + LN_EPS))), rs = 1.0f / sb;
    u16* xT = (u16*)(a.ws + WS_XT); u16* uT = (u16*)(a.ws + WS_UT);
    const int g = lane >> 4, i16 = lane & 15, qq = i16 >> 2, p = lane & 3;
#pragma unroll
    for (int pass = 0; pass < 2; ++pass) {
#pragma unroll
        for (int i = 0; i < 9; ++i) {
            u32x4 v = raw[i];
            if (pass == 1) {
#pragma unroll
                for (int k = 0; k < 4; ++k) {
                    unsigned dd = pk_bf16(bf_lo(raw[i][k]) - mb, bf_hi(raw[i][k]) - mb);
                    v[k] = pk_bf16(bf_lo(dd) * rs, bf_hi(dd) * rs);
                }
            }
            *(LAS u32x4*)(lds + rl * 1184 + (c8 + 8 * i) * 16) = v;
        }
        __syncthreads();
        u16* dst = pass == 0 ? xT : uT;
        for (int cb = w; cb < 36; cb += 4) {
            s16x4 lo = tr_read(lds + (8 * g + qq) * 1184 + cb * 32 + p * 8);
            s16x4 hi = tr_read(lds + (8 * g + 4 + qq) * 1184 + cb * 32 + p * 8);
            *(bf16x8*)(dst + (size_t)(cb * 16 + i16) * L + row0 + 8 * g) = cat8(lo, hi);
        }
        __syncthreads();
    }
}

__device__ __forceinline__ void ka_kgen(const KAArgs& a, int job, LAS char* lds) {
    const int tid = threadIdx.x, w = tid >> 6, lane = tid & 63;
    LAS f32x4* par = (LAS f32x4*)lds;
    LAS float* T = (LAS float*)(lds + 4096);
    {
        const int ch = job * 2 + (tid >> 7), n = tid & 127, o = ch * NS + n;
        const float dt = rbf(expf(bf2f(a.logdt[ch])));
        const float are = dt * bf2f(a.are[o]), aim = dt * bf2f(a.aim[o]);
        const float br = dt * bf2f(a.bre[o]), bi = dt * bf2f(a.bim[o]);
        const float cr = bf2f(a.cre[o]), ci = bf2f(a.cim[o]);
        f32x4 v = {are * 1.4426950408889634f, aim * 0.15915494309189535f, cr * br - ci * bi, cr * bi + ci * br};
        par[tid] = v;
    }
    __syncthreads();
    const int chl = w >> 1, nh = w & 1, r = lane & 31, h = lane >> 5;
    const float xE = 8.0f * r;
    int base;
    if (r < 8) base = r;
    else if (r < 12) base = 256 + 2 * (r - 8);
    else if (r < 16) base = 512 + 4 * (r & 1) + 256 * ((r - 12) >> 1);
    else if (r < 20) base = 1024 + 256 * (r - 16);
    else if (r == 20) base = 2048;
    else base = 0;
    const float xX = (float)base;
    f32x16 acc = {};
#pragma unroll 2
    for (int t = 0; t < 8; ++t) {
        u32x4 eh, el, xh, xl;
#pragma unroll
        for (int c = 0; c < 4; ++c) {
            const f32x4 pr = par[chl * 128 + nh * 64 + t * 8 + h * 4 + c];
            float e1 = __builtin_amdgcn_exp2f(pr[0] * xE), p1 = __builtin_amdgcn_fractf(pr[1] * xE);
            float er = e1 * __builtin_amdgcn_cosf(p1), ei = e1 * __builtin_amdgcn_sinf(p1);
            float e2 = __builtin_amdgcn_exp2f(pr[0] * xX), p2 = __builtin_amdgcn_fractf(pr[1] * xX);
            float fr = e2 * __builtin_amdgcn_cosf(p2), fi = e2 * __builtin_amdgcn_sinf(p2);
            float xr = fr * pr[2] - fi * pr[3], xi = -(fr * pr[3] + fi * pr[2]);
            unsigned a1 = pk_bf16(er, ei);
            eh[c] = a1; el[c] = pk_bf16(er - bf_lo(a1), ei - bf_hi(a1));
            unsigned b1 = pk_bf16(xr, xi);
            xh[c] = b1; xl[c] = pk_bf16(xr - bf_lo(b1), xi - bf_hi(b1));
        }
        bf16x8 EH = __builtin_bit_cast(bf16x8, eh), EL = __builtin_bit_cast(bf16x8, el);
        bf16x8 XH = __builtin_bit_cast(bf16x8, xh), XL = __builtin_bit_cast(bf16x8, xl);
        acc = MFMA32(EH, XH, acc);
        acc = MFMA32(EH, XL, acc);
        acc = MFMA32(EL, XH, acc);
    }
#pragma unroll
    for (int g = 0; g < 16; ++g) T[w * 1056 + r * 33 + (g & 3) + 8 * (g >> 2) + 4 * h] = acc[g];
    __syncthreads();
    {
        const int c2 = tid >> 7, t7 = tid & 127;
        u16* R = (u16*)(a.ws + WS_RK) + (size_t)(job * 2 + c2) * RSTR;
        const LAS float* T0 = T + (c2 * 2) * 1056; const LAS float* T1 = T0 + 1056;
#pragma unroll
        for (int half = 0; half < 2; ++half) {
            const int p0 = (t7 + half * 128) * 8;
            float v[8];
#pragma unroll
            for (int e = 0; e < 8; ++e) {
                const int l = 2047 - (p0 + e);
                const int lr = (int)rbf((float)l);
                int i, j;
                if (lr < 256) { i = lr & 7; j = lr >> 3; }
                else if (lr < 512) { i = 8 + ((lr & 7) >> 1); j = (lr - 256) >> 3; }
                else if (lr < 1024) { i = 12 + ((lr >> 2) & 1) + 2 * ((lr - 512) >> 8); j = ((lr - 512) & 255) >> 3; }
                else if (lr < 2048) { i = 16 + ((lr - 1024) >> 8); j = ((lr - 1024) & 255) >> 3; }
                else { i = 20; j = 0; }
                v[e] = T0[i * 33 + j] + T1[i * 33 + j];
            }
            u32x4 o = {pk_bf16(v[0], v[1]), pk_bf16(v[2], v[3]), pk_bf16(v[4], v[5]), pk_bf16(v[6], v[7])};
            *(u32x4*)(R + p0) = o;
        }
        if (t7 < 8) *(u32x4*)(R + 2048 + t7 * 8) = (u32x4){0u, 0u, 0u, 0u};
    }
}

__device__ __forceinline__ void ka_prep(const KAArgs& a, int job, LAS char* lds) {
    const int tid = threadIdx.x, w = __builtin_amdgcn_readfirstlane(tid >> 6), lane = tid & 63;
    const int sub = job / 18, rem = job % 18, kh = rem / 9, ns = rem % 9;
    const u16* src; int ld;
    if (sub == 0) { src = a.w1; ld = D; }
    else if (sub == 1) { src = a.w2; ld = D; }
    else if (sub < 10) { src = a.w3 + (sub - 2) * D; ld = F; }
    else { src = a.w4 + (size_t)(sub - 10) * D * D; ld = D; }
    src += (size_t)(kh * 288) * ld + ns * 64;
#pragma unroll
    for (int i = 0; i < 9; ++i) {
        const int ins = w * 9 + i, row = ins * 8 + (lane >> 3), c = (lane & 7) ^ (fsw(row) << 1);
        glds16(src + (size_t)row * ld + c * 8, lds + ins * 1024);
    }
    asm volatile("s_waitcnt vmcnt(0)" ::: "memory");
    __syncthreads();
    const int g = lane >> 4, i16 = lane & 15, q = i16 >> 2, p = lane & 3;
    const int f = ((q >> 1) & 1) | ((g & 1) << 1);
    const LAS char* rb = lds + (8 * g + q) * 128 + (((2 * w + (p >> 1)) ^ (f << 1)) * 16) + 8 * (p & 1);
    u16* dst = (u16*)(a.ws + WS_WF) + (size_t)sub * WSUB + ((size_t)(kh * 9) * 36 + ns * 4 + w) * 512 + lane * 8;
    float cs = 0.f;
#pragma unroll
    for (int s = 0; s < 9; ++s) {
        bf16x8 v = cat8(tr_read(rb + s * 4096), tr_read(rb + s * 4096 + 512));
        *(bf16x8*)(dst + (size_t)s * 36 * 512) = v;
        u32x4 u = __builtin_bit_cast(u32x4, v);
#pragma unroll
        for (int k = 0; k < 4; ++k) cs += bf_lo(u[k]) + bf_hi(u[k]);
    }
    cs += __shfl_xor(cs, 16); cs += __shfl_xor(cs, 32);
    if (lane < 16 && sub != 1 && sub < 10) {
        float* csp = (float*)(a.ws + WS_CS) + kh * (D + F) + (sub == 0 ? 0 : D + (sub - 2) * D) + ns * 64 + w * 16 + lane;
        *csp = cs;
    }
}

__global__ __launch_bounds__(256) void ka_kernel(KAArgs a) {
    extern __shared__ __attribute__((aligned(16))) char smem[];
    LAS char* lds = (LAS char*)smem;
    const int b = blockIdx.x;
    if (b < KA_LN) {
        if (b == 0) {
            u32x4* gz = (u32x4*)(a.ws + WS_G);
            if (threadIdx.x < 144) { gz[threadIdx.x] = (u32x4){0u, 0u, 0u, 0u}; gz[(size_t)(L + 2) * 72 + threadIdx.x] = (u32x4){0u, 0u, 0u, 0u}; }
        }
        ka_ln(a, b, lds);
    } else if (b < KA_LN + KA_KG) ka_kgen(a, b - KA_LN, lds);
    else ka_prep(a, b - KA_LN - KA_KG, lds);
}

constexpr int KB_RC = 4160;
constexpr int KB_U = 4 * KB_RC;
constexpr int KB_RED = KB_U + 2288 * 2;
constexpr int KB_LDS = KB_RED + 4 * 6 * 1024;

__global__ __launch_bounds__(256) void kb_kernel(char* __restrict__ ws, const u16* __restrict__ dskip) {
    extern __shared__ __attribute__((aligned(16))) char smem[];
    LAS char* lds = (LAS char*)smem;
    const int d = blockIdx.x, tid = threadIdx.x, w = __builtin_amdgcn_readfirstlane(tid >> 6), lane = tid & 63;
    const u16* R = (const u16*)(ws + WS_RK) + (size_t)d * RSTR;
    const u16* uT = (const u16*)(ws + WS_UT) + (size_t)d * L;
    const u16* xT = (const u16*)(ws + WS_XT) + (size_t)d * L;
    u16* hT = (u16*)(ws + WS_HT) + (size_t)d * L;
    const int p = lane & 15, g = lane >> 4;
    const u32x4 uv = *(const u32x4*)(uT + tid * 8);
    u32x4 r0 = *(const u32x4*)(R + tid * 8);
    u32x2 r1 = *(const u32x2*)(R + tid * 8 + 8);
    u32x4 r2 = {}; u32x2 r3 = {};
    if (tid < 4) { r2 = *(const u32x4*)(R + (256 + tid) * 8); r3 = *(const u32x2*)(R + (256 + tid) * 8 + 8); }
    u32x2 xv[2];
#pragma unroll
    for (int i = 0; i < 2; ++i) xv[i] = *(const u32x2*)(xT + 256 * (2 * w + i) + 16 * p + 4 * g);
    const float dsk = bf2f(dskip[d]);
    auto put = [&](int m, u32x4 A, u32x2 B) {
        u32x4 c1 = {__builtin_amdgcn_alignbit(A[1], A[0], 16), __builtin_amdgcn_alignbit(A[2], A[1], 16), __builtin_amdgcn_alignbit(A[3], A[2], 16), __builtin_amdgcn_alignbit(B[0], A[3], 16)};
        u32x4 c2 = {A[1], A[2], A[3], B[0]};
        u32x4 c3 = {c1[1], c1[2], c1[3], __builtin_amdgcn_alignbit(B[1], B[0], 16)};
        *(LAS u32x4*)(lds + 0 * KB_RC + m * 16) = A;
        *(LAS u32x4*)(lds + 1 * KB_RC + m * 16) = c1;
        *(LAS u32x4*)(lds + 2 * KB_RC + m * 16) = c2;
        *(LAS u32x4*)(lds + 3 * KB_RC + m * 16) = c3;
    };
    put(tid, r0, r1);
    if (tid < 4) put(256 + tid, r2, r3);
    *(LAS u32x4*)(lds + KB_U + 480 + tid * 16) = uv;
    if (tid < 30) *(LAS u32x4*)(lds + KB_U + tid * 16) = (u32x4){0u, 0u, 0u, 0u};
    __syncthreads();
    f32x4 acc[8];
#pragma unroll
    for (int c = 0; c < 8; ++c) acc[c] = (f32x4){0.f, 0.f, 0.f, 0.f};
    const int cpy = (3 - p) & 3;
#pragma unroll
    for (int rr = 0; rr < 2; ++rr) {
        const int r = 2 * w + rr;
        const LAS char* ab = lds + cpy * KB_RC + 2 * (2031 - 32 * r + 8 * g - p - cpy);
        const LAS char* fb = lds + KB_U + 2 * (240 - 32 * r + 16 * p - 16 + 8 * g);
        bf16x8 A[8], Fm[8];
#pragma unroll
        for (int s = 0; s < 8; ++s) {
            const s16x4 lo = *(const volatile LAS s16x4*)(ab - 512 * s);
            const s16x4 hi = *(const volatile LAS s16x4*)(ab - 512 * s + 8);
            A[s] = cat8(lo, hi);
        }
#pragma unroll
        for (int e = 0; e < 8; ++e) Fm[e] = *(const LAS bf16x8*)(fb + 512 * e);
#pragma unroll
        for (int c = 0; c < 8; ++c)
#pragma unroll
            for (int s = 0; s <= c; ++s) acc[c] = MFMA16(A[s], Fm[c - s], acc[c]);
    }
    {
        LAS f32x4* red = (LAS f32x4*)(lds + KB_RED);
#pragma unroll
        for (int c = 0; c < 8; ++c) {
            const int ow = c >> 1;
            if (ow != w) {
                const int slot = (c & 1) * 3 + (w > ow ? w - 1 : w);
                red[(ow * 6 + slot) * 64 + lane] = acc[c];
            }
        }
        __syncthreads();
#pragma unroll
        for (int i = 0; i < 2; ++i) {
            f32x4 y = {};
#pragma unroll
            for (int c = 0; c < 8; ++c) if ((c >> 1) == w && (c & 1) == i) y = acc[c];
#pragma unroll
            for (int k = 0; k < 3; ++k) y += red[(w * 6 + i * 3 + k) * 64 + lane];
            const int l0 = 256 * (2 * w + i) + 16 * p + 4 * g;
            const u32x2 uu = *(const LAS u32x2*)(lds + KB_U + 480 + 2 * l0);
            float o[4];
#pragma unroll
            for (int k = 0; k < 4; ++k) {
                const float u = (k & 1) ? bf_hi(uu[k >> 1]) : bf_lo(uu[k >> 1]);
                const float x = (k & 1) ? bf_hi(xv[i][k >> 1]) : bf_lo(xv[i][k >> 1]);
                o[k] = x + rbf(rbf(y[k]) + rbf(u * dsk));
            }
            u32x2 ov = {pk_bf16(o[0], o[1]), pk_bf16(o[2], o[3])};
            *(u32x2*)(hT + l0) = ov;
        }
    }
}

__device__ __forceinline__ int nb0_of(int j) { return (9 * j + 1) >> 1; }

constexpr int KC_LDS = 73728 + 512;
__global__ __launch_bounds__(320) void kc_kernel(char* __restrict__ ws) {
    extern __shared__ __attribute__((aligned(16))) char smem[];
    LAS char* lds = (LAS char*)smem;
    const int tid = threadIdx.x, w = __builtin_amdgcn_readfirstlane(tid >> 6), lane = tid & 63;
    const int j = blockIdx.x & 7, tok0 = (blockIdx.x >> 3) * 64;
    const int nb = nb0_of(j) + w; const bool act = nb < nb0_of(j + 1);
    const int nbc = act ? nb : nb0_of(j);
    const u16* hT = (const u16*)(ws + WS_HT);
    for (int i = w; i < 72; i += 5) {
        const int row = i * 8 + (lane >> 3), c = (lane & 7) ^ (fsw(row) << 1);
        glds16(hT + (size_t)row * L + tok0 + c * 8, lds + i * 1024);
    }
    const bf16x8* wf = (const bf16x8*)(ws + WS_WF) + nbc * 64 + lane;
    bf16x8 W[18];
#pragma unroll
    for (int s = 0; s < 18; ++s) W[s] = wf[s * 36 * 64];
    const int g = lane >> 4, i16 = lane & 15, q = i16 >> 2, p = lane & 3;
    const float* csp = (const float*)(ws + WS_CS) + nbc * 16 + 4 * g;
    const f32x4 cs = *(const f32x4*)csp + *(const f32x4*)(csp + (D + F));
    asm volatile("s_waitcnt vmcnt(0)" ::: "memory");
    __syncthreads();
    const int f = ((q >> 1) & 1) | ((g & 1) << 1);
    const LAS char* hb[4];
#pragma unroll
    for (int mt = 0; mt < 4; ++mt) hb[mt] = lds + (8 * g + q) * 128 + (((2 * mt + (p >> 1)) ^ (f << 1)) * 16) + 8 * (p & 1);
    const LAS char* sbp = lds + (8 * g + q) * 128 + (((2 * (w & 3) + (p >> 1)) ^ (f << 1)) * 16) + 8 * (p & 1);
    f32x4 acc[4] = {};
    float s1 = 0.f, s2 = 0.f;
#pragma unroll
    for (int s = 0; s < 18; ++s) {
#pragma unroll
        for (int mt = 0; mt < 4; ++mt) {
            bf16x8 H = cat8(tr_read(hb[mt] + s * 4096), tr_read(hb[mt] + s * 4096 + 512));
            acc[mt] = MFMA16(W[s], H, acc[mt]);
        }
        u32x4 hs = __builtin_bit_cast(u32x4, cat8(tr_read(sbp + s * 4096), tr_read(sbp + s * 4096 + 512)));
#pragma unroll
        for (int k = 0; k < 4; ++k) { s1 = dot2bf(hs[k], 0x3f803f80u, s1); s2 = dot2bf(hs[k], hs[k], s2); }
    }
    s1 += __shfl_xor(s1, 16); s1 += __shfl_xor(s1, 32);
    s2 += __shfl_xor(s2, 16); s2 += __shfl_xor(s2, 32);
    LAS f32x2* st = (LAS f32x2*)(lds + 73728);
    if (w < 4 && lane < 16) {
        const float mean = s1 * (1.0f / D), var = fmaxf(s2 * (1.0f / D) - mean * mean, 0.f);
        const float mb = rbf(mean), sb = rbf(sqrtf(rbf(rbf(var) + LN_EPS))), rs = 1.0f / sb;
        const float m2 = rbf((mean - mb) * rs), v2 = rbf(var * rs * rs), sb2 = rbf(sqrtf(rbf(v2 + LN_EPS))), rs2 = 1.0f / sb2;
        st[w * 16 + lane] = (f32x2){rs * rs2, (mb * rs + m2) * rs2};
        if (j == 0) ((f32x2*)(ws + WS_ST2))[tok0 + w * 16 + lane] = (f32x2){mb, rs};
    }
    __syncthreads();
    if (act) {
        u16* gp = (u16*)(ws + WS_G) + (size_t)(2 + tok0 + i16) * D + nb * 16 + 4 * g;
#pragma unroll
        for (int mt = 0; mt < 4; ++mt) {
            const f32x2 ab = st[mt * 16 + i16];
            float o[4];
#pragma unroll
            for (int k = 0; k < 4; ++k) o[k] = gelu_tanh(ab[0] * acc[mt][k] - ab[1] * cs[k]);
            *(u32x2*)(gp + (size_t)mt * 16 * D) = (u32x2){pk_bf16(o[0], o[1]), pk_bf16(o[2], o[3])};
        }
    }
}

constexpr int KD_LDS = 73728 + 10240 + 5 * 64 * 8;
__global__ __launch_bounds__(320) void kd_kernel(char* __restrict__ ws) {
    extern __shared__ __attribute__((aligned(16))) char smem[];
    LAS char* lds = (LAS char*)smem;
    const int tid = threadIdx.x, w = __builtin_amdgcn_readfirstlane(tid >> 6), lane = tid & 63;
    const int j = blockIdx.x & 7, tok0 = (blockIdx.x >> 3) * 64;
    const int nbs = nb0_of(j), nbe = nb0_of(j + 1);
    const int nb = nbs + w; const bool act = nb < nbe;
    const int nbc = act ? nb : nbs;
    const u16* G = (const u16*)(ws + WS_G);
    for (int i = w; i < 72; i += 5) {
        const int P = i * 64 + lane, m = P / 72, cp = P % 72, c = cp ^ ((m >> 1) & 7);
        const int sg = c / 18, off = (sg == 0) ? -1 : (sg == 1) ? 1 : (sg == 2) ? -2 : 2;
        glds16(G + (size_t)(2 + tok0 + m + off) * D + c * 8, lds + i * 1024);
    }
    const u16* hT = (const u16*)(ws + WS_HT);
    for (int i = w; i < 2 * (nbe - nbs); i += 5) {
        const int row = i * 8 + (lane >> 3), c = (lane & 7) ^ (fsw(row) << 1);
        glds16(hT + (size_t)(nbs * 16 + row) * L + tok0 + c * 8, lds + 73728 + i * 1024);
    }
    const bf16x8* wf = (const bf16x8*)(ws + WS_WF) + (size_t)WSUB / 8 + nbc * 64 + lane;
    bf16x8 W[18];
#pragma unroll
    for (int s = 0; s < 18; ++s) W[s] = wf[s * 36 * 64];
    const int g = lane >> 4, i16 = lane & 15, q = i16 >> 2, p = lane & 3;
    f32x2 st[4];
#pragma unroll
    for (int mt = 0; mt < 4; ++mt) st[mt] = ((const f32x2*)(ws + WS_ST2))[tok0 + mt * 16 + i16];
    asm volatile("s_waitcnt vmcnt(0)" ::: "memory");
    __syncthreads();
    const int z = (i16 >> 1) & 7;
    const LAS char* gb0 = lds + i16 * 1152 + ((g) ^ z) * 16;
    const LAS char* gb1 = lds + i16 * 1152 + ((4 + g) ^ z) * 16;
    f32x4 acc[4] = {};
#pragma unroll
    for (int s = 0; s < 18; ++s) {
#pragma unroll
        for (int mt = 0; mt < 4; ++mt) {
            bf16x8 A = *(const LAS bf16x8*)(((s & 1) ? gb1 : gb0) + mt * 16 * 1152 + (s >> 1) * 128);
            acc[mt] = MFMA16(W[s], A, acc[mt]);
        }
    }
    LAS f32x2* ps = (LAS f32x2*)(lds + 73728 + 10240);
    const int wl = act ? w : 0;
    const int hrow = 16 * wl + 4 * g + q;
    const LAS char* hb = lds + 73728 + hrow * 128 + 8 * (p & 1);
#pragma unroll
    for (int mt = 0; mt < 4; ++mt) {
        const s16x4 hv = tr_read(hb + (((2 * mt + (p >> 1)) ^ (fsw(hrow) << 1)) * 16));
        const u32x2 hu = __builtin_bit_cast(u32x2, hv);
        float o[4], s1 = 0.f, s2 = 0.f;
#pragma unroll
        for (int k = 0; k < 4; ++k) {
            const float h = (k & 1) ? bf_hi(hu[k >> 1]) : bf_lo(hu[k >> 1]);
            const float xb = rbf(rbf(h - st[mt][0]) * st[mt][1]);
            o[k] = rbf(xb + rbf(acc[mt][k]));
            s1 += o[k]; s2 += o[k] * o[k];
        }
        s1 += __shfl_xor(s1, 16); s1 += __shfl_xor(s1, 32);
        s2 += __shfl_xor(s2, 16); s2 += __shfl_xor(s2, 32);
        if (!act) { s1 = 0.f; s2 = 0.f; }
        if (lane < 16) ps[w * 64 + mt * 16 + lane] = (f32x2){s1, s2};
        if (act) {
            const size_t o_ = (size_t)(tok0 + mt * 16 + i16) * D + nb * 16 + 4 * g;
            *(u32x2*)((u16*)(ws + WS_X1) + o_) = (u32x2){pk_bf16(o[0], o[1]), pk_bf16(o[2], o[3])};
            *(u32x2*)((u16*)(ws + WS_HR) + o_) = hu;
        }
    }
    __syncthreads();
    if (tid < 64) {
        f32x2 t = ps[tid];
#pragma unroll
        for (int k = 1; k < 5; ++k) t += ps[k * 64 + tid];
        ((f32x2*)(ws + WS_PST))[(size_t)(tok0 + tid) * 8 + j] = t;
    }
}

constexpr int KE_HID = 73728, KE_ST = 2 * 73728, KE_LDS = KE_ST + 512;
constexpr int RING = 3;

template <int NB>
__device__ __forceinline__ void ke_body(char* __restrict__ ws, LAS char* lds, int j, int panel, int nbs, int lane) {
    const int g = lane >> 4, i16 = lane & 15;
    const bf16x8* w3 = (const bf16x8*)(ws + WS_WF) + (size_t)(2 + j) * (WSUB / 8) + nbs * 64 + lane;
    const bf16x8* w4 = (const bf16x8*)(ws + WS_WF) + (size_t)(10 + j) * (WSUB / 8) + nbs * 64 + lane;
    bf16x8 W[RING][NB];
#pragma unroll
    for (int s = 0; s < RING; ++s)
#pragma unroll
        for (int b = 0; b < NB; ++b) W[s][b] = w3[(s * 36 + b) * 64];
    f32x4 cs[NB];
    {
        const float* csp = (const float*)(ws + WS_CS) + D + j * D + nbs * 16 + 4 * g;
#pragma unroll
        for (int b = 0; b < NB; ++b) cs[b] = *(const f32x4*)(csp + b * 16) + *(const f32x4*)(csp + b * 16 + (D + F));
    }
    asm volatile("s_waitcnt vmcnt(0)" ::: "memory");
    __syncthreads();
    const int z = (i16 >> 1) & 7;
    const int o0 = i16 * 1152 + (g ^ z) * 16, o1 = i16 * 1152 + ((4 + g) ^ z) * 16;
    f32x4 acc[NB][4];
#pragma unroll
    for (int b = 0; b < NB; ++b)
#pragma unroll
        for (int mt = 0; mt < 4; ++mt) acc[b][mt] = (f32x4){0.f, 0.f, 0.f, 0.f};
#pragma unroll
    for (int s = 0; s < 18; ++s) {
        bf16x8 A[4];
#pragma unroll
        for (int mt = 0; mt < 4; ++mt) A[mt] = *(const LAS bf16x8*)(lds + ((s & 1) ? o1 : o0) + mt * 16 * 1152 + (s >> 1) * 128);
#pragma unroll
        for (int b = 0; b < NB; ++b) {
#pragma unroll
            for (int mt = 0; mt < 4; ++mt) acc[b][mt] = MFMA16(W[s % RING][b], A[mt], acc[b][mt]);
            W[s % RING][b] = (s + RING < 18) ? w3[((s + RING) * 36 + b) * 64] : w4[((s + RING - 18) * 36 + b) * 64];
        }
    }
    {
        const LAS f32x2* st = (const LAS f32x2*)(lds + KE_ST);
#pragma unroll
        for (int mt = 0; mt < 4; ++mt) {
            const f32x2 ab = st[mt * 16 + i16];
#pragma unroll
            for (int b = 0; b < NB; ++b) {
                float o[4];
#pragma unroll
                for (int k = 0; k < 4; ++k) o[k] = gelu_tanh(ab[0] * acc[b][mt][k] - ab[1] * cs[b][k]);
                const int ch = 2 * (nbs + b) + (g >> 1);
                *(LAS u32x2*)(lds + KE_HID + (mt * 16 + i16) * 1152 + ((ch ^ z) * 16) + 8 * (g & 1)) = (u32x2){pk_bf16(o[0], o[1]), pk_bf16(o[2], o[3])};
                acc[b][mt] = (f32x4){0.f, 0.f, 0.f, 0.f};
            }
        }
    }
    __syncthreads();
#pragma unroll
    for (int s = 0; s < 18; ++s) {
        bf16x8 A[4];
#pragma unroll
        for (int mt = 0; mt < 4; ++mt) A[mt] = *(const LAS bf16x8*)(lds + KE_HID + ((s & 1) ? o1 : o0) + mt * 16 * 1152 + (s >> 1) * 128);
#pragma unroll
        for (int b = 0; b < NB; ++b) {
#pragma unroll
            for (int mt = 0; mt < 4; ++mt) acc[b][mt] = MFMA16(W[s % RING][b], A[mt], acc[b][mt]);
            if (s + RING < 18) W[s % RING][b] = w4[((s + RING) * 36 + b) * 64];
        }
    }
    u32x4* pp = (u32x4*)(ws + WS_PART) + ((size_t)(panel * 8 + j) * 72 + nbs * 2) * 64 + lane;
#pragma unroll
    for (int b = 0; b < NB; ++b)
#pragma unroll
        for (int m2 = 0; m2 < 2; ++m2) {
            const f32x4 a0 = acc[b][2 * m2], a1 = acc[b][2 * m2 + 1];
            pp[(b * 2 + m2) * 64] = (u32x4){pk_bf16(a0[0], a0[1]), pk_bf16(a0[2], a0[3]), pk_bf16(a1[0], a1[1]), pk_bf16(a1[2], a1[3])};
        }
}

__global__ __launch_bounds__(512) void ke_kernel(char* __restrict__ ws) {
    extern __shared__ __attribute__((aligned(16))) char smem[];
    LAS char* lds = (LAS char*)smem;
    const int tid = threadIdx.x, w = __builtin_amdgcn_readfirstlane(tid >> 6), lane = tid & 63;
    const int j = blockIdx.x & 7, panel = blockIdx.x >> 3, tok0 = panel * 64;
    const u16* X1 = (const u16*)(ws + WS_X1);
#pragma unroll
    for (int ii = 0; ii < 9; ++ii) {
        const int i = w * 9 + ii, P = i * 64 + lane, m = P / 72, cp = P % 72, c = cp ^ ((m >> 1) & 7);
        glds16(X1 + (size_t)(tok0 + m) * D + c * 8, lds + i * 1024);
    }
    if (tid < 64) {
        const f32x4* pst = (const f32x4*)(ws + WS_PST) + (size_t)(tok0 + tid) * 4;
        float s1 = 0.f, s2 = 0.f;
#pragma unroll
        for (int k = 0; k < 4; ++k) { const f32x4 v = pst[k]; s1 += v[0] + v[2]; s2 += v[1] + v[3]; }
        const float mean = s1 * (1.0f / D), var = fmaxf(s2 * (1.0f / D) - mean * mean, 0.f);
        const float mb = rbf(mean), sb = rbf(sqrtf(rbf(rbf(var) + LN_EPS))), rs = 1.0f / sb;
        ((LAS f32x2*)(lds + KE_ST))[tid] = (f32x2){rs, mb * rs};
    }
    if (w < 4) ke_body<5>(ws, lds, j, panel, 9 * w, lane);
    else ke_body<4>(ws, lds, j, panel, 9 * (w - 4) + 5, lane);
}

__global__ __launch_bounds__(576) void kf_kernel(const char* __restrict__ ws, u16* __restrict__ out) {
    const int tid = threadIdx.x, w = tid >> 6, lane = tid & 63;
    const int jj = blockIdx.x & 7, panel = blockIdx.x >> 3;
    const int pr = jj * 9 + w, nb = pr >> 1, m2 = pr & 1;
    const u32x4* pp = (const u32x4*)(ws + WS_PART) + ((size_t)(panel * 8) * 72 + pr) * 64 + lane;
    u32x4 v[8];
#pragma unroll
    for (int k = 0; k < 8; ++k) v[k] = pp[(size_t)k * 72 * 64];
    const int g = lane >> 4, i16 = lane & 15;
    u32x2 xa[2], ha[2];
    size_t off[2];
#pragma unroll
    for (int t = 0; t < 2; ++t) {
        off[t] = (size_t)(panel * 64 + m2 * 32 + t * 16 + i16) * D + nb * 16 + 4 * g;
        xa[t] = *(const u32x2*)((const u16*)(ws + WS_X1) + off[t]);
        ha[t] = *(const u32x2*)((const u16*)(ws + WS_HR) + off[t]);
    }
    float s[8] = {};
#pragma unroll
    for (int k = 0; k < 8; ++k)
#pragma unroll
        for (int e = 0; e < 4; ++e) { s[2 * e] += bf_lo(v[k][e]); s[2 * e + 1] += bf_hi(v[k][e]); }
#pragma unroll
    for (int t = 0; t < 2; ++t) {
        float o[4];
#pragma unroll
        for (int k = 0; k < 4; ++k) {
            const float x1 = (k & 1) ? bf_hi(xa[t][k >> 1]) : bf_lo(xa[t][k >> 1]);
            const float h = (k & 1) ? bf_hi(ha[t][k >> 1]) : bf_lo(ha[t][k >> 1]);
            o[k] = h + rbf(x1 + rbf(s[4 * t + k]));
        }
        *(u32x2*)(out + off[t]) = (u32x2){pk_bf16(o[0], o[1]), pk_bf16(o[2], o[3])};
    }
}

extern "C" void kernel_launch(void* const* d_in, const int* in_sizes, int n_in,
                              void* d_out, int out_size, void* d_ws, size_t ws_size,
                              hipStream_t stream) {
    char* ws = (char*)d_ws;
    KAArgs a;
    a.x = (const u16*)d_in[0];
    a.are = (const u16*)d_in[1]; a.aim = (const u16*)d_in[2];
    a.bre = (const u16*)d_in[3]; a.bim = (const u16*)d_in[4];
    a.cre = (const u16*)d_in[5]; a.cim = (const u16*)d_in[6];
    a.logdt = (const u16*)d_in[7];
    a.w1 = (const u16*)d_in[10]; a.w2 = (const u16*)d_in[11];
    a.w3 = (const u16*)d_in[12]; a.w4 = (const u16*)d_in[13];
    a.ws = ws;
    const u16* dskip = (const u16*)d_in[8];
    (void)hipFuncSetAttribute((const void*)kc_kernel, hipFuncAttributeMaxDynamicSharedMemorySize, KC_LDS);
    (void)hipFuncSetAttribute((const void*)kd_kernel, hipFuncAttributeMaxDynamicSharedMemorySize, KD_LDS);
    (void)hipFuncSetAttribute((const void*)ke_kernel, hipFuncAttributeMaxDynamicSharedMemorySize, KE_LDS);
    ka_kernel<<<KA_LN + KA_KG + KA_PREP, 256, KA_LDS, stream>>>(a);
    kb_kernel<<<D, 256, KB_LDS, stream>>>(ws, dskip);
    kc_kernel<<<256, 320, KC_LDS, stream>>>(ws);
    kd_kernel<<<256, 320, KD_LDS, stream>>>(ws);
    ke_kernel<<<256, 512, KE_LDS, stream>>>(ws);
    kf_kernel<<<256, 576, 0, stream>>>(ws, (u16*)d_out);
}
```

```cpp
#include <hip/hip_runtime.h>
#include <hip/hip_bf16.h>
#include <stdint.h>

typedef unsigned short u16;
typedef short bf16x8 __attribute__((ext_vector_type(8)));
typedef short s16x4 __attribute__((ext_vector_type(4)));
typedef float f32x2 __attribute__((ext_vector_type(2)));
typedef float f32x4 __attribute__((ext_vector_type(4)));
typedef float f32x16 __attribute__((ext_vector_type(16)));
typedef unsigned u32x2 __attribute__((ext_vector_type(2)));
typedef unsigned u32x4 __attribute__((ext_vector_type(4)));
typedef __bf16 bf16x2_t __attribute__((ext_vector_type(2)));
#define LAS __attribute__((address_space(3)))
#if defined(__HIP_DEVICE_COMPILE__)
#define NOPK __attribute__((target("no-packed-fp32-ops")))
#else
#define NOPK
#endif

constexpr int L = 2048, D = 576, NS = 128, F = 4608;
constexpr int RSTR = 2112;
constexpr int WSUB = D * D;
constexpr float LN_EPS = 1e-5f;

constexpr size_t WS_UT = 0;
constexpr size_t WS_XT = WS_UT + (size_t)D * L * 2;
constexpr size_t WS_HT = WS_XT + (size_t)D * L * 2;
constexpr size_t WS_RK = WS_HT + (size_t)D * L * 2;
constexpr size_t WS_WF = WS_RK + (size_t)D * RSTR * 2;
constexpr size_t WS_CS = WS_WF + (size_t)18 * WSUB * 2;
constexpr size_t WS_ST2 = WS_CS + (size_t)2 * (D + F) * 4;
constexpr size_t WS_G = WS_ST2 + (size_t)L * 8;
constexpr size_t WS_X1 = WS_G + (size_t)(L + 4) * D * 2;
constexpr size_t WS_HR = WS_X1 + (size_t)L * D * 2;
constexpr size_t WS_PST = WS_HR + (size_t)L * D * 2;
constexpr size_t WS_PART = WS_PST + (size_t)L * 8 * 8;
constexpr size_t WS_END = WS_PART + (size_t)32 * 8 * 72 * 512 * 2;

#define STAMP(ws, kern, i) do {} while (0)

__device__ __forceinline__ unsigned pk_bf16(float lo, float hi) {
    f32x2 v = {lo, hi};
    return __builtin_bit_cast(unsigned, __builtin_convertvector(v, bf16x2_t));
}
__device__ __forceinline__ float bf_lo(unsigned p) { return __uint_as_float(p << 16); }
__device__ __forceinline__ float bf_hi(unsigned p) { return __uint_as_float(p & 0xffff0000u); }
__device__ __forceinline__ float bf2f(u16 x) { return __uint_as_float((unsigned)x << 16); }
__device__ __forceinline__ float rbf(float x) { return bf_lo(pk_bf16(x, x)); }
__device__ __forceinline__ float dot2bf(unsigned a, unsigned b, float c) {
    return __builtin_amdgcn_fdot2_f32_bf16(__builtin_bit_cast(bf16x2_t, a), __builtin_bit_cast(bf16x2_t, b), c, false);
}
__device__ __forceinline__ float gelu_tanh(float x) {
    const float c0 = -2.0f * 0.7978845608028654f * 1.4426950408889634f;
    const float c1 = c0 * 0.044715f;
    float e = __builtin_amdgcn_exp2f(x * (c0 + c1 * x * x));
    return x * __builtin_amdgcn_rcpf(1.0f + e);
}
__device__ __forceinline__ s16x4 tr_read(const LAS char* p) {
    return __builtin_amdgcn_ds_read_tr16_b64_v4i16((LAS s16x4*)p);
}
__device__ __forceinline__ bf16x8 cat8(s16x4 a, s16x4 b) {
    return __builtin_shufflevector(a, b, 0, 1, 2, 3, 4, 5, 6, 7);
}
__device__ __forceinline__ void glds16(const void* g, LAS char* l) {
    __builtin_amdgcn_global_load_lds((const unsigned*)g, (LAS unsigned*)l, 16, 0, 0);
}
__device__ __forceinline__ f32x4 sel4(bool c, f32x4 a, f32x4 b) {
    f32x4 r;
#pragma unroll
    for (int k = 0; k < 4; ++k) { float x = a[k], y = b[k]; asm volatile("" : "+v"(x), "+v"(y)); r[k] = c ? x : y; }
    return r;
}
__device__ __forceinline__ float rsum_g(float x) {
    const unsigned u = __float_as_uint(x);
    const auto a = __builtin_amdgcn_permlane16_swap(u, u, false, false);
    const float y = __uint_as_float(a[0]) + __uint_as_float(a[1]);
    const unsigned v = __float_as_uint(y);
    const auto b = __builtin_amdgcn_permlane32_swap(v, v, false, false);
    return __uint_as_float(b[0]) + __uint_as_float(b[1]);
}
__device__ __forceinline__ float ln_sd(float var) { return rbf(__builtin_amdgcn_sqrtf(rbf(rbf(var) + LN_EPS))); }
__device__ __forceinline__ int fsw(int row) { return ((row >> 1) & 1) | (((row >> 3) & 1) << 1); }
#define MFMA16(a, b, c) __builtin_amdgcn_mfma_f32_16x16x32_bf16((a), (b), (c), 0, 0, 0)
#define MFMA32(a, b, c) __builtin_amdgcn_mfma_f32_32x32x16_bf16((a), (b), (c), 0, 0, 0)

typedef __amdgpu_buffer_rsrc_t rsrc_t;
__device__ __forceinline__ rsrc_t mk_rsrc(const void* p, unsigned bytes) { return __builtin_amdgcn_make_buffer_rsrc((void*)p, 0, bytes, 0x00020000); }
__device__ __forceinline__ bf16x8 bld16(rsrc_t r, int voff, int soff) {
    return __builtin_bit_cast(bf16x8, __builtin_amdgcn_raw_buffer_load_b128(r, voff, soff, 0));
}
__device__ __forceinline__ f32x4 bldf4(rsrc_t r, int voff, int soff) {
    return __builtin_bit_cast(f32x4, __builtin_amdgcn_raw_buffer_load_b128(r, voff, soff, 0));
}
__device__ __forceinline__ void blds16(rsrc_t r, LAS char* l, int voff, int soff) {
    __builtin_amdgcn_raw_ptr_buffer_load_lds(r, (LAS void*)l, 16, voff, soff, 0, 0);
}

struct KAArgs {
    const u16* x; const u16 *are, *aim, *bre, *bim, *cre, *cim, *logdt;
    const u16 *w1, *w2, *w3, *w4;
    char* ws;
};

constexpr int KA_LN = 64, KA_KG = 576, KA_PREP = 324;
constexpr int KA_LDS = 32 * 1184;

__device__ __forceinline__ void ka_ln(const KAArgs& a, int job, LAS char* lds) {
    const int tid = threadIdx.x, w = tid >> 6, lane = tid & 63;
    const int row0 = job * 32, rl = w * 8 + (lane >> 3), c8 = lane & 7;
    const u32x4* src = (const u32x4*)(a.x + (size_t)(row0 + rl) * D);
    u32x4 raw[9];
#pragma unroll
    for (int i = 0; i < 9; ++i) raw[i] = src[c8 + 8 * i];
    float s1 = 0.f, s2 = 0.f;
#pragma unroll
    for (int i = 0; i < 9; ++i) {
        *(LAS u32x4*)(lds + rl * 1184 + (c8 + 8 * i) * 16) = raw[i];
#pragma unroll
        for (int k = 0; k < 4; ++k) { s1 = dot2bf(raw[i][k], 0x3f803f80u, s1); s2 = dot2bf(raw[i][k], raw[i][k], s2); }
    }
    s1 += __shfl_xor(s1, 1); s2 += __shfl_xor(s2, 1);
    s1 += __shfl_xor(s1, 2); s2 += __shfl_xor(s2, 2);
    s1 += __shfl_xor(s1, 4); s2 += __shfl_xor(s2, 4);
    const float mean = s1 * (1.0f / D), var = fmaxf(s2 * (1.0f / D) - mean * mean, 0.f);
    const float mb = rbf(mean), sb = ln_sd(var), rs = __builtin_amdgcn_rcpf(sb);
    u16* xT = (u16*)(a.ws + WS_XT); u16* uT = (u16*)(a.ws + WS_UT);
    const int g = lane >> 4, i16 = lane & 15, qq = i16 >> 2, p = lane & 3;
    const LAS char* tb = lds + (8 * g + qq) * 1184 + p * 8;
    STAMP(a.ws, 0, 2);
    __syncthreads();
#pragma unroll
    for (int k = 0; k < 9; ++k) {
        const int cb = w + 4 * k;
        *(bf16x8*)(xT + (size_t)(cb * 16 + i16) * L + row0 + 8 * g) = cat8(tr_read(tb + cb * 32), tr_read(tb + 4 * 1184 + cb * 32));
    }
    __syncthreads();
    STAMP(a.ws, 0, 3);
#pragma unroll
    for (int i = 0; i < 9; ++i) {
        u32x4 v;
#pragma unroll
        for (int k = 0; k < 4; ++k) {
            const unsigned dd = pk_bf16(bf_lo(raw[i][k]) - mb, bf_hi(raw[i][k]) - mb);
            v[k] = pk_bf16(bf_lo(dd) * rs, bf_hi(dd) * rs);
        }
        *(LAS u32x4*)(lds + rl * 1184 + (c8 + 8 * i) * 16) = v;
    }
    __syncthreads();
#pragma unroll
    for (int k = 0; k < 9; ++k) {
        const int cb = w + 4 * k;
        *(bf16x8*)(uT + (size_t)(cb * 16 + i16) * L + row0 + 8 * g) = cat8(tr_read(tb + cb * 32), tr_read(tb + 4 * 1184 + cb * 32));
    }
}

__device__ __forceinline__ void split_pair(float re, float im, unsigned mlo, unsigned mhi, unsigned& hi, unsigned& lo) {
    hi = pk_bf16(re, im);
    lo = pk_bf16(dot2bf(hi, mlo, re), dot2bf(hi, mhi, im));
}
constexpr int KG_TAB = 720;
__device__ __forceinline__ void ka_kgen(const KAArgs& a, int ch, LAS char* lds) {
    const int tid = threadIdx.x, w = tid >> 6, lane = tid & 63;
    LAS f32x4* par = (LAS f32x4*)lds;
    LAS float* T = (LAS float*)(lds + 2048);
    if (tid < 128) {
        const int o = ch * NS + tid;
        const float dt = rbf(expf(bf2f(a.logdt[ch])));
        const float are = dt * bf2f(a.are[o]), aim = dt * bf2f(a.aim[o]);
        const float br = dt * bf2f(a.bre[o]), bi = dt * bf2f(a.bim[o]);
        const float cr = bf2f(a.cre[o]), ci = bf2f(a.cim[o]);
        par[tid] = (f32x4){are * 1.4426950408889634f, aim * 0.15915494309189535f, cr * br - ci * bi, cr * bi + ci * br};
    }
    __syncthreads();
    STAMP(a.ws, 0, 2);
    const int r = lane & 31, h = lane >> 5;
    const float xE = 8.0f * r;
    int base, idx0, istr;
    if (r < 8) { base = r; idx0 = r; istr = 8; }
    else if (r < 12) { base = 256 + 2 * (r - 8); idx0 = 256 + (r - 8); istr = 4; }
    else if (r < 16) { base = 512 + 4 * (r & 1) + 256 * ((r - 12) >> 1); idx0 = 384 + (r & 1) + 64 * ((r - 12) >> 1); istr = 2; }
    else if (r < 20) { base = 1024 + 256 * (r - 16); idx0 = 512 + 32 * (r - 16); istr = 1; }
    else if (r == 20) { base = 2048; idx0 = 640; istr = 1; }
    else { base = 0; idx0 = 680; istr = 1; }
    const float xX = (float)base;
    f32x16 acc = {};
    unsigned mlo = 0x0000bf80u, mhi = 0xbf800000u;
    asm volatile("" : "+s"(mlo), "+s"(mhi));
#pragma unroll
    for (int t = 0; t < 4; ++t) {
        u32x4 eh, el, xh, xl;
#pragma unroll
        for (int c = 0; c < 4; ++c) {
            const f32x4 pr = par[w * 32 + t * 8 + h * 4 + c];
            const float e1 = __builtin_amdgcn_exp2f(pr[0] * xE), p1 = __builtin_amdgcn_fractf(pr[1] * xE);
            const float e2 = __builtin_amdgcn_exp2f(pr[0] * xX), p2 = __builtin_amdgcn_fractf(pr[1] * xX);
            const float fr = e2 * __builtin_amdgcn_cosf(p2), fi = e2 * __builtin_amdgcn_sinf(p2);
            unsigned h1, l1, h2, l2;
            split_pair(e1 * __builtin_amdgcn_cosf(p1), e1 * __builtin_amdgcn_sinf(p1), mlo, mhi, h1, l1);
            split_pair(fr * pr[2] - fi * pr[3], -(fr * pr[3] + fi * pr[2]), mlo, mhi, h2, l2);
            eh[c] = h1; el[c] = l1; xh[c] = h2; xl[c] = l2;
        }
        const bf16x8 EH = __builtin_bit_cast(bf16x8, eh), EL = __builtin_bit_cast(bf16x8, el);
        const bf16x8 XH = __builtin_bit_cast(bf16x8, xh), XL = __builtin_bit_cast(bf16x8, xl);
        acc = MFMA32(EH, XH, acc);
        acc = MFMA32(EH, XL, acc);
        acc = MFMA32(EL, XH, acc);
    }
    STAMP(a.ws, 0, 3);
    {
        LAS float* tw = T + w * KG_TAB + idx0 + 4 * h * istr;
#pragma unroll
        for (int q = 0; q < 16; ++q) tw[((q & 3) + 8 * (q >> 2)) * istr] = acc[q];
    }
    __syncthreads();
    u16* R = (u16*)(a.ws + WS_RK) + (size_t)ch * RSTR;
    float v[8];
#pragma unroll
    for (int e = 0; e < 8; ++e) {
        const int l = 2047 - (tid * 8 + e);
        const int idx = l < 128 ? l : (int)(pk_bf16((float)l, 0.f) & 0xffffu) - 17024;
        v[e] = (T[idx] + T[KG_TAB + idx]) + (T[2 * KG_TAB + idx] + T[3 * KG_TAB + idx]);
    }
    *(u32x4*)(R + tid * 8) = (u32x4){pk_bf16(v[0], v[1]), pk_bf16(v[2], v[3]), pk_bf16(v[4], v[5]), pk_bf16(v[6], v[7])};
    if (tid < 8) *(u32x4*)(R + 2048 + tid * 8) = (u32x4){0u, 0u, 0u, 0u};
}

__device__ __forceinline__ void ka_prep(const KAArgs& a, int job, LAS char* lds) {
    const int tid = threadIdx.x, w = __builtin_amdgcn_readfirstlane(tid >> 6), lane = tid & 63;
    const int sub = job / 18, rem = job % 18, kh = rem / 9, ns = rem % 9;
    const u16* src; int ld;
    if (sub == 0) { src = a.w1; ld = D; }
    else if (sub == 1) { src = a.w2; ld = D; }
    else if ((sub & 1) == 0) { src = a.w3 + ((sub - 2) >> 1) * D; ld = F; }
    else { src = a.w4 + (size_t)((sub - 2) >> 1) * D * D; ld = D; }
    src += (size_t)(kh * 288) * ld + ns * 64;
#pragma unroll
    for (int i = 0; i < 9; ++i) {
        const int ins = w * 9 + i, row = ins * 8 + (lane >> 3), c = (lane & 7) ^ (fsw(row) << 1);
        glds16(src + (size_t)row * ld + c * 8, lds + ins * 1024);
    }
    asm volatile("s_waitcnt vmcnt(0)" ::: "memory");
    __syncthreads();
    STAMP(a.ws, 0, 2);
    const int g = lane >> 4, i16 = lane & 15, q = i16 >> 2, p = lane & 3;
    const int f = ((q >> 1) & 1) | ((g & 1) << 1);
    const LAS char* rb = lds + (8 * g + q) * 128 + (((2 * w + (p >> 1)) ^ (f << 1)) * 16) + 8 * (p & 1);
    u16* dst = (u16*)(a.ws + WS_WF) + (size_t)sub * WSUB + ((size_t)(kh * 9) * 36 + ns * 4 + w) * 512 + lane * 8;
    float cs = 0.f;
#pragma unroll
    for (int s = 0; s < 9; ++s) {
        bf16x8 v = cat8(tr_read(rb + s * 4096), tr_read(rb + s * 4096 + 512));
        *(bf16x8*)(dst + (size_t)s * 36 * 512) = v;
        u32x4 u = __builtin_bit_cast(u32x4, v);
#pragma unroll
        for (int k = 0; k < 4; ++k) cs += bf_lo(u[k]) + bf_hi(u[k]);
    }
    cs = rsum_g(cs);
    if (lane < 16 && (sub & 1) == 0) {
        float* csp = (float*)(a.ws + WS_CS) + kh * (D + F) + (sub == 0 ? 0 : D + ((sub - 2) >> 1) * D) + ns * 64 + w * 16 + lane;
        *csp = cs;
    }
}

NOPK __global__ __launch_bounds__(256) void ka_kernel(KAArgs a) {
    extern __shared__ __attribute__((aligned(16))) char smem[];
    LAS char* lds = (LAS char*)smem;
    const int b = blockIdx.x;
    STAMP(a.ws, 0, 0);
    if (b < KA_PREP) ka_prep(a, b, lds);
    else if (b < KA_PREP + KA_LN) {
        if (b == KA_PREP) {
            u32x4* gz = (u32x4*)(a.ws + WS_G);
            if (threadIdx.x < 144) { gz[threadIdx.x] = (u32x4){0u, 0u, 0u, 0u}; gz[(size_t)(L + 2) * 72 + threadIdx.x] = (u32x4){0u, 0u, 0u, 0u}; }
        }
        ka_ln(a, b - KA_PREP, lds);
    } else ka_kgen(a, b - KA_PREP - KA_LN, lds);
    STAMP(a.ws, 0, 1);
}

constexpr int KB_RC = 4160;
constexpr int KB_U = 4 * KB_RC;
constexpr int KB_RED = KB_U + 2288 * 2;
constexpr int KB_LDS = KB_RED + 4 * 6 * 1024;

__device__ __forceinline__ void kb_put(LAS char* lds, int m, u32x4 A, u32x2 B) {
    u32x4 c1 = {__builtin_amdgcn_alignbit(A[1], A[0], 16), __builtin_amdgcn_alignbit(A[2], A[1], 16), __builtin_amdgcn_alignbit(A[3], A[2], 16), __builtin_amdgcn_alignbit(B[0], A[3], 16)};
    u32x4 c2 = {A[1], A[2], A[3], B[0]};
    u32x4 c3 = {c1[1], c1[2], c1[3], __builtin_amdgcn_alignbit(B[1], B[0], 16)};
    *(LAS u32x4*)(lds + 0 * KB_RC + m * 16) = A;
    *(LAS u32x4*)(lds + 1 * KB_RC + m * 16) = c1;
    *(LAS u32x4*)(lds + 2 * KB_RC + m * 16) = c2;
    *(LAS u32x4*)(lds + 3 * KB_RC + m * 16) = c3;
}

NOPK __global__ __launch_bounds__(256) void kb_kernel(char* __restrict__ ws, const u16* __restrict__ dskip) {
    extern __shared__ __attribute__((aligned(16))) char smem[];
    LAS char* lds = (LAS char*)smem;
    const int d = blockIdx.x, tid = threadIdx.x, w = __builtin_amdgcn_readfirstlane(tid >> 6), lane = tid & 63;
    const u16* R = (const u16*)(ws + WS_RK) + (size_t)d * RSTR;
    const u16* uT = (const u16*)(ws + WS_UT) + (size_t)d * L;
    const u16* xT = (const u16*)(ws + WS_XT) + (size_t)d * L;
    u16* hT = (u16*)(ws + WS_HT) + (size_t)d * L;
    const int p = lane & 15, g = lane >> 4;
    STAMP(ws, 1, 0);
    const u32x4 uv = *(const u32x4*)(uT + tid * 8);
    u32x4 r0 = *(const u32x4*)(R + tid * 8);
    u32x2 r1 = *(const u32x2*)(R + tid * 8 + 8);
    u32x4 r2 = {}; u32x2 r3 = {};
    if (tid < 4) { r2 = *(const u32x4*)(R + (256 + tid) * 8); r3 = *(const u32x2*)(R + (256 + tid) * 8 + 8); }
    u32x2 xv[2];
#pragma unroll
    for (int i = 0; i < 2; ++i) xv[i] = *(const u32x2*)(xT + 256 * (2 * w + i) + 16 * p + 4 * g);
    const float dsk = bf2f(dskip[d]);
    kb_put(lds, tid, r0, r1);
    if (tid < 4) kb_put(lds, 256 + tid, r2, r3);
    *(LAS u32x4*)(lds + KB_U + 480 + tid * 16) = uv;
    if (tid < 30) *(LAS u32x4*)(lds + KB_U + tid * 16) = (u32x4){0u, 0u, 0u, 0u};
    __syncthreads();
    STAMP(ws, 1, 1);
    f32x4 acc[8];
#pragma unroll
    for (int c = 0; c < 8; ++c) acc[c] = (f32x4){0.f, 0.f, 0.f, 0.f};
    const int cpy = (3 - p) & 3;
#pragma unroll
    for (int rr = 0; rr < 2; ++rr) {
        const int r = 2 * w + rr;
        const LAS char* ab = lds + cpy * KB_RC + 2 * (2031 - 32 * r + 8 * g - p - cpy);
        const LAS char* fb = lds + KB_U + 2 * (240 - 32 * r + 16 * p - 16 + 8 * g);
        bf16x8 A[8], Fm[8];
#pragma unroll
        for (int s = 0; s < 8; ++s) {
            const s16x4 lo = *(const volatile LAS s16x4*)(ab - 512 * s);
            const s16x4 hi = *(const volatile LAS s16x4*)(ab - 512 * s + 8);
            A[s] = cat8(lo, hi);
        }
#pragma unroll
        for (int e = 0; e < 8; ++e) Fm[e] = *(const LAS bf16x8*)(fb + 512 * e);
#pragma unroll
        for (int c = 0; c < 8; ++c)
#pragma unroll
            for (int s = 0; s <= c; ++s) acc[c] = MFMA16(A[s], Fm[c - s], acc[c]);
    }
    STAMP(ws, 1, 2);
    {
        LAS f32x4* red = (LAS f32x4*)(lds + KB_RED);
#pragma unroll
        for (int c = 0; c < 8; ++c) {
            const int ow = c >> 1;
            if (ow != w) {
                const int slot = (c & 1) * 3 + (w > ow ? w - 1 : w);
                red[(ow * 6 + slot) * 64 + lane] = acc[c];
            }
        }
        __syncthreads();
#pragma unroll
        for (int i = 0; i < 2; ++i) {
            f32x4 y;
#pragma unroll
            for (int k = 0; k < 4; ++k) {
                const float y01 = (w & 1) ? acc[2 + i][k] : acc[i][k], y23 = (w & 1) ? acc[6 + i][k] : acc[4 + i][k];
                y[k] = (w & 2) ? y23 : y01;
            }
#pragma unroll
            for (int k = 0; k < 3; ++k) y += red[(w * 6 + i * 3 + k) * 64 + lane];
            const int l0 = 256 * (2 * w + i) + 16 * p + 4 * g;
            const u32x2 uu = *(const LAS u32x2*)(lds + KB_U + 480 + 2 * l0);
            float o[4];
#pragma unroll
            for (int k = 0; k < 4; ++k) {
                const float u = (k & 1) ? bf_hi(uu[k >> 1]) : bf_lo(uu[k >> 1]);
                const float x = (k & 1) ? bf_hi(xv[i][k >> 1]) : bf_lo(xv[i][k >> 1]);
                o[k] = x + rbf(rbf(y[k]) + rbf(u * dsk));
            }
            u32x2 ov = {pk_bf16(o[0], o[1]), pk_bf16(o[2], o[3])};
            *(u32x2*)(hT + l0) = ov;
        }
    }
    STAMP(ws, 1, 3);
}

__device__ __forceinline__ int nb0_of(int j) { return (9 * j + 1) >> 1; }

constexpr int KC_XB = 73728, KC_ST = KC_XB + 20 * 1024, KC_LDS = KC_ST + 512;
__device__ __forceinline__ void phase_c(char* __restrict__ ws, LAS char* lds, int panel, int j) {
    const int tid = threadIdx.x, w = __builtin_amdgcn_readfirstlane(tid >> 6), lane = tid & 63;
    const int tok0 = panel * 64, nbs = nb0_of(j), nblk = nb0_of(j + 1) - nbs;
    const int g = lane >> 4, i16 = lane & 15, q = i16 >> 2, p = lane & 3;
    {
        const rsrc_t rh = mk_rsrc(ws + WS_HT, D * L * 2);
        const int fl = (lane >> 4) & 1;
        const int v0 = (lane >> 3) * 4096 + (((lane & 7) ^ (fl << 1)) * 16), v1 = (lane >> 3) * 4096 + (((lane & 7) ^ ((fl | 2) << 1)) * 16);
#pragma unroll
        for (int k = 0; k < 6; ++k) { const int idx = 6 * w + k; blds16(rh, lds + idx * 1024, (k & 1) ? v1 : v0, idx * 32768 + tok0 * 2); }
    }
    const int b = w >> 1, kh = w & 1;
    const bool act = w < 10 && b < nblk;
    const int nb = nbs + (act ? b : 0);
    bf16x8 W[9];
    f32x4 cs;
    if (w < 10) {
        const rsrc_t rw = mk_rsrc(ws + WS_WF, WSUB * 2);
#pragma unroll
        for (int i = 0; i < 9; ++i) W[i] = bld16(rw, lane * 16, ((kh * 9 + i) * 36 + nb) * 1024);
        const rsrc_t rc = mk_rsrc(ws + WS_CS, 2 * (D + F) * 4);
        cs = bldf4(rc, g * 16, nb * 64) + bldf4(rc, g * 16, nb * 64 + (D + F) * 4);
    }
    asm volatile("s_waitcnt vmcnt(0)" ::: "memory");
    __builtin_amdgcn_s_barrier();
    STAMP(ws, 2, 1);
    const int f = ((q >> 1) & 1) | ((g & 1) << 1);
    const int lo_ = (8 * g + q) * 128 + 8 * (p & 1);
    f32x4 acc[4] = {};
    LAS f32x2* st = (LAS f32x2*)(lds + KC_ST);
    LAS f32x4* xb = (LAS f32x4*)(lds + KC_XB);
    if (w < 10) {
        const LAS char* hb[4];
#pragma unroll
        for (int mt = 0; mt < 4; ++mt) hb[mt] = lds + kh * 9 * 4096 + lo_ + (((2 * mt + (p >> 1)) ^ (f << 1)) * 16);
        bf16x8 H[2][3][4];
#pragma unroll
        for (int c = 0; c < 4; ++c) {
            if (c < 3) {
#pragma unroll
                for (int i = 0; i < 3; ++i)
#pragma unroll
                    for (int mt = 0; mt < 4; ++mt) H[c & 1][i][mt] = cat8(tr_read(hb[mt] + (3 * c + i) * 4096), tr_read(hb[mt] + (3 * c + i) * 4096 + 512));
            }
            __builtin_amdgcn_sched_barrier(0);
            if (c > 0) {
#pragma unroll
                for (int i = 0; i < 3; ++i)
#pragma unroll
                    for (int mt = 0; mt < 4; ++mt) acc[mt] = MFMA16(W[3 * (c - 1) + i], H[(c - 1) & 1][i][mt], acc[mt]);
            }
            __builtin_amdgcn_sched_barrier(0);
        }
        xb[((w * 2 + 0) * 64) + lane] = sel4(kh, acc[0], acc[2]);
        xb[((w * 2 + 1) * 64) + lane] = sel4(kh, acc[1], acc[3]);
    } else {
        const bf16x8 ones = {0x3f80, 0x3f80, 0x3f80, 0x3f80, 0x3f80, 0x3f80, 0x3f80, 0x3f80};
        const int m0 = 2 * (w - 10);
        const LAS char* h0 = lds + lo_ + (((2 * m0 + (p >> 1)) ^ (f << 1)) * 16);
        const LAS char* h1 = lds + lo_ + (((2 * m0 + 2 + (p >> 1)) ^ (f << 1)) * 16);
        bf16x8 H[2][6][2];
#pragma unroll
        for (int c = 0; c < 4; ++c) {
            if (c < 3) {
#pragma unroll
                for (int i = 0; i < 6; ++i) {
                    H[c & 1][i][0] = cat8(tr_read(h0 + (6 * c + i) * 4096), tr_read(h0 + (6 * c + i) * 4096 + 512));
                    H[c & 1][i][1] = cat8(tr_read(h1 + (6 * c + i) * 4096), tr_read(h1 + (6 * c + i) * 4096 + 512));
                }
            }
            __builtin_amdgcn_sched_barrier(0);
            if (c > 0) {
#pragma unroll
                for (int i = 0; i < 6; ++i) {
                    const bf16x8 H0 = H[(c - 1) & 1][i][0], H1 = H[(c - 1) & 1][i][1];
                    acc[0] = MFMA16(ones, H0, acc[0]); acc[1] = MFMA16(H0, H0, acc[1]);
                    acc[2] = MFMA16(ones, H1, acc[2]); acc[3] = MFMA16(H1, H1, acc[3]);
                }
            }
            __builtin_amdgcn_sched_barrier(0);
        }
        if (g == q) {
#pragma unroll
            for (int t = 0; t < 2; ++t) {
                const f32x4 d = acc[2 * t + 1];
                const float s2 = p == 0 ? d[0] : p == 1 ? d[1] : p == 2 ? d[2] : d[3];
                const float mean = acc[2 * t][0] * (1.0f / D), var = fmaxf(s2 * (1.0f / D) - mean * mean, 0.f);
                const float mb = rbf(mean), rs = __builtin_amdgcn_rcpf(ln_sd(var));
                const float m2 = rbf((mean - mb) * rs), rs2 = __builtin_amdgcn_rcpf(ln_sd(var * rs * rs));
                st[(m0 + t) * 16 + i16] = (f32x2){rs * rs2, (mb * rs + m2) * rs2};
                if (j == 0) ((f32x2*)(ws + WS_ST2))[tok0 + (m0 + t) * 16 + i16] = (f32x2){mb, rs};
            }
        }
    }
    asm volatile("s_waitcnt lgkmcnt(0)" ::: "memory");
    __builtin_amdgcn_s_barrier();
    STAMP(ws, 2, 2);
    if (act) {
        u16* gp = (u16*)(ws + WS_G) + (size_t)(2 + tok0 + kh * 32 + i16) * D + nb * 16 + 4 * g;
#pragma unroll
        for (int t = 0; t < 2; ++t) {
            const f32x4 a = sel4(kh, acc[2 + t], acc[t]) + xb[((w ^ 1) * 2 + t) * 64 + lane];
            const f32x2 ab = st[(2 * kh + t) * 16 + i16];
            float o[4];
#pragma unroll
            for (int k = 0; k < 4; ++k) o[k] = gelu_tanh(ab[0] * a[k] - ab[1] * cs[k]);
            *(u32x2*)(gp + (size_t)t * 16 * D) = (u32x2){pk_bf16(o[0], o[1]), pk_bf16(o[2], o[3])};
        }
    }
}

NOPK __global__ __launch_bounds__(768) void kc_kernel(char* __restrict__ ws) {
    extern __shared__ __attribute__((aligned(16))) char smem[];
    STAMP(ws, 2, 0);
    phase_c(ws, (LAS char*)smem, (blockIdx.x & 7) * 4 + (blockIdx.x >> 6), (blockIdx.x >> 3) & 7);
    STAMP(ws, 2, 3);
}

constexpr int KD_HT = 73728, KD_XB = KD_HT + 10240, KD_PS = KD_XB + 20 * 1024, KD_LDS = KD_PS + 5 * 64 * 8;
__device__ __forceinline__ void phase_d(char* __restrict__ ws, LAS char* lds, int panel, int j) {
    const int tid = threadIdx.x, w = __builtin_amdgcn_readfirstlane(tid >> 6), lane = tid & 63;
    const int tok0 = panel * 64, nbs = nb0_of(j), nblk = nb0_of(j + 1) - nbs;
    const int g = lane >> 4, i16 = lane & 15, q = i16 >> 2, p = lane & 3;
    {
        const rsrc_t rg = mk_rsrc(ws + WS_G, (L + 4) * D * 2);
        const int c7 = (lane & 7) ^ ((lane >> 3) & 6), vb = (lane >> 3) * 1152 + c7 * 16;
#pragma unroll
        for (int k = 0; k < 6; ++k) {
            const int idx = 6 * w + k, cg = idx >> 3, c = cg * 8 + c7;
            const int o2 = (0x4031 >> (((c * 57) >> 10) * 4)) & 15;
            blds16(rg, lds + idx * 1024, vb + o2 * 1152, (tok0 + (idx & 7) * 8) * 1152 + cg * 128);
        }
        if (w < 2 * nblk) {
            const rsrc_t rh = mk_rsrc(ws + WS_HT, D * L * 2);
            const int fl = ((lane >> 4) & 1) | ((w & 1) << 1);
            blds16(rh, lds + KD_HT + w * 1024, (lane >> 3) * 4096 + (((lane & 7) ^ (fl << 1)) * 16), (nbs * 16 + w * 8) * 4096 + tok0 * 2);
        }
    }
    const int b = w >> 1, kh = w & 1;
    const bool act = w < 10 && b < nblk;
    const int nb = nbs + (act ? b : 0);
    bf16x8 W[9];
    f32x2 st[2];
    if (w < 10) {
        const rsrc_t rw = mk_rsrc(ws + WS_WF + WSUB * 2, WSUB * 2);
#pragma unroll
        for (int i = 0; i < 9; ++i) W[i] = bld16(rw, lane * 16, ((kh * 9 + i) * 36 + nb) * 1024);
#pragma unroll
        for (int t = 0; t < 2; ++t) st[t] = ((const f32x2*)(ws + WS_ST2))[tok0 + kh * 32 + t * 16 + i16];
    }
    asm volatile("s_waitcnt vmcnt(0)" ::: "memory");
    __builtin_amdgcn_s_barrier();
    STAMP(ws, 3, 1);
    f32x4 acc[4] = {};
    LAS f32x4* xb = (LAS f32x4*)(lds + KD_XB);
    if (w < 10) {
        const int z = i16 & 6;
        const LAS char* a0 = lds + i16 * 128 + ((g ^ z) * 16), * a1 = lds + i16 * 128 + (((4 + g) ^ z) * 16);
        const LAS char* e0 = kh ? a1 + 4 * 8192 : a0;
        const LAS char* e1 = kh ? a0 + 5 * 8192 : a1;
        bf16x8 A[2][3][4];
#pragma unroll
        for (int c = 0; c < 4; ++c) {
            if (c < 3) {
#pragma unroll
                for (int ii = 0; ii < 3; ++ii) {
                    const int i = 3 * c + ii;
#pragma unroll
                    for (int mt = 0; mt < 4; ++mt) A[c & 1][ii][mt] = *(const LAS bf16x8*)(((i & 1) ? e1 : e0) + (i >> 1) * 8192 + mt * 2048);
                }
            }
            __builtin_amdgcn_sched_barrier(0);
            if (c > 0) {
#pragma unroll
                for (int ii = 0; ii < 3; ++ii)
#pragma unroll
                    for (int mt = 0; mt < 4; ++mt) acc[mt] = MFMA16(W[3 * (c - 1) + ii], A[(c - 1) & 1][ii][mt], acc[mt]);
            }
            __builtin_amdgcn_sched_barrier(0);
        }
        xb[((w * 2 + 0) * 64) + lane] = sel4(kh, acc[0], acc[2]);
        xb[((w * 2 + 1) * 64) + lane] = sel4(kh, acc[1], acc[3]);
    }
    asm volatile("s_waitcnt lgkmcnt(0)" ::: "memory");
    __builtin_amdgcn_s_barrier();
    STAMP(ws, 3, 2);
    LAS f32x2* ps = (LAS f32x2*)(lds + KD_PS);
    if (w < 10) {
        const int bl = act ? b : 0;
        const int hrow = 16 * bl + 4 * g + q;
        const LAS char* hb = lds + KD_HT + hrow * 128 + 8 * (p & 1);
#pragma unroll
        for (int t = 0; t < 2; ++t) {
            const int mt = 2 * kh + t;
            const f32x4 a = sel4(kh, acc[2 + t], acc[t]) + xb[((w ^ 1) * 2 + t) * 64 + lane];
            const u32x2 hu = __builtin_bit_cast(u32x2, tr_read(hb + (((2 * mt + (p >> 1)) ^ (fsw(hrow) << 1)) * 16)));
            float o[4], s1 = 0.f, s2 = 0.f;
#pragma unroll
            for (int k = 0; k < 4; ++k) {
                const float h = (k & 1) ? bf_hi(hu[k >> 1]) : bf_lo(hu[k >> 1]);
                const float xbv = rbf(rbf(h - st[t][0]) * st[t][1]);
                o[k] = rbf(xbv + rbf(a[k]));
                s1 += o[k]; s2 += o[k] * o[k];
            }
            s1 = rsum_g(s1); s2 = rsum_g(s2);
            if (lane < 16) ps[b * 64 + mt * 16 + lane] = act ? (f32x2){s1, s2} : (f32x2){0.f, 0.f};
            if (act) {
                const size_t o_ = (size_t)(tok0 + mt * 16 + i16) * D + nb * 16 + 4 * g;
                *(u32x2*)((u16*)(ws + WS_X1) + o_) = (u32x2){pk_bf16(o[0], o[1]), pk_bf16(o[2], o[3])};
                *(u32x2*)((u16*)(ws + WS_HR) + o_) = hu;
            }
        }
    }
    asm volatile("s_waitcnt lgkmcnt(0)" ::: "memory");
    __builtin_amdgcn_s_barrier();
    if (tid < 64) {
        f32x2 t = ps[tid];
#pragma unroll
        for (int k = 1; k < 5; ++k) t += ps[k * 64 + tid];
        ((f32x2*)(ws + WS_PST))[(size_t)(tok0 + tid) * 8 + j] = t;
    }
}

NOPK __global__ __launch_bounds__(768) void kd_kernel(char* __restrict__ ws) {
    extern __shared__ __attribute__((aligned(16))) char smem[];
    STAMP(ws, 3, 0);
    phase_d(ws, (LAS char*)smem, (blockIdx.x & 7) * 4 + (blockIdx.x >> 6), (blockIdx.x >> 3) & 7);
    STAMP(ws, 3, 3);
}

constexpr int KE_HID = 73728, KE_ST = 2 * 73728, KE_LDS = KE_ST + 1024 + 16;
constexpr int RING = 4;
#define KE_MAP2D 1
#define KE_PRIO 1
#define KE_PF 0
#define KE_FLAGS 1
NOPK __global__ __launch_bounds__(768) void ke_kernel(char* __restrict__ ws) {
    extern __shared__ __attribute__((aligned(16))) char smem[];
    LAS char* lds = (LAS char*)smem;
    const int tid = threadIdx.x, w = __builtin_amdgcn_readfirstlane(tid >> 6), lane = tid & 63;
    const int j = 2 * (blockIdx.x & 3) + ((blockIdx.x >> 3) & 1), panel = ((blockIdx.x >> 2) & 1) * 16 + (blockIdx.x >> 4), tok0 = panel * 64;
    const int g = lane >> 4, i16 = lane & 15, nbs = 3 * w;
    STAMP(ws, 4, 0);
    const rsrc_t rp = mk_rsrc(ws + WS_PST, L * 64);
    f32x4 pv[4];
    if (w == 11) {
#pragma unroll
        for (int k = 0; k < 4; ++k) pv[k] = bldf4(rp, lane * 64 + k * 16, tok0 * 64);
    }
    {
        const rsrc_t rx = mk_rsrc(ws + WS_X1 + (size_t)tok0 * D * 2, 64 * D * 2);
        const int vo = (lane >> 3) * 1152 + (((lane & 7) ^ ((lane >> 3) & 6)) * 16);
#pragma unroll
        for (int k = 0; k < 6; ++k) {
            const int idx = 6 * w + k;
            blds16(rx, lds + idx * 1024, vo, (idx & 7) * 9216 + (idx >> 3) * 128);
        }
    }
    const rsrc_t rw = mk_rsrc(ws + WS_WF + (size_t)(2 + 2 * j) * WSUB * 2, 2 * WSUB * 2);
    const int wv = lane * 16, wsb = nbs * 1024;
    bf16x8 W[RING][3];
#pragma unroll
    for (int s = 0; s < RING; ++s)
#pragma unroll
        for (int b = 0; b < 3; ++b) W[s][b] = bld16(rw, wv + b * 1024, wsb + s * 36864);
    f32x4 c0[3], c1[3];
    {
        const rsrc_t rc = mk_rsrc(ws + WS_CS, 2 * (D + F) * 4);
#pragma unroll
        for (int b = 0; b < 3; ++b) {
            c0[b] = bldf4(rc, g * 16 + b * 64, (D + j * D + nbs * 16) * 4);
            c1[b] = bldf4(rc, g * 16 + b * 64, (D + j * D + nbs * 16 + D + F) * 4);
        }
    }
    __builtin_amdgcn_sched_barrier(0);
    LAS f32x4* st = (LAS f32x4*)(lds + KE_ST);
    if (w == 11) {
        float s1 = 0.f, s2 = 0.f;
#pragma unroll
        for (int k = 0; k < 4; ++k) { s1 += pv[k][0] + pv[k][2]; s2 += pv[k][1] + pv[k][3]; }
        const float mean = s1 * (1.0f / D), var = fmaxf(s2 * (1.0f / D) - mean * mean, 0.f);
        const float mb = rbf(mean), sb = ln_sd(var), al = __builtin_amdgcn_rcpf(sb);
        const float k0 = -2.0f * 0.7978845608028654f * 1.4426950408889634f;
        st[lane] = (f32x4){k0 * al, k0 * 0.044715f * al * al * al, sb, -mb};
        if (lane < 4) ((LAS unsigned*)(lds + KE_ST + 1024))[lane] = 0u;
    }
    asm volatile("s_waitcnt vmcnt(0) lgkmcnt(0)" ::: "memory");
    __builtin_amdgcn_s_barrier();
    STAMP(ws, 4, 1);
    const int z = i16 & 6;
    const int o0 = i16 * 128 + (g ^ z) * 16, o1 = i16 * 128 + ((4 + g) ^ z) * 16;
    f32x4 acc[3][4];
#pragma unroll
    for (int mt = 0; mt < 4; ++mt) {
        const float nm = st[mt * 16 + i16][3];
#pragma unroll
        for (int b = 0; b < 3; ++b) acc[b][mt] = (c0[b] + c1[b]) * nm;
    }
    bf16x8 A[2][4];
    LAS unsigned* flg = (LAS unsigned*)(lds + KE_ST + 1024);
#pragma unroll
    for (int ph = 0; ph < 2; ++ph) {
        const LAS char* ab = lds + ph * KE_HID;
        if (ph == 1) {
            while (__hip_atomic_load(flg + 0, __ATOMIC_RELAXED, __HIP_MEMORY_SCOPE_WORKGROUP) < 4u) __builtin_amdgcn_s_sleep(1);
            __builtin_amdgcn_sched_barrier(0);
            asm volatile("" ::: "memory");
        }
#pragma unroll
        for (int mt = 0; mt < 4; ++mt) A[0][mt] = *(const LAS bf16x8*)(ab + o0 + mt * 2048);
#pragma unroll
        for (int s = 0; s < 18; ++s) {
            if (ph == 1 && (s == 5 || s == 11)) {
                while (__hip_atomic_load(flg + (s == 5 ? 1 : 2), __ATOMIC_RELAXED, __HIP_MEMORY_SCOPE_WORKGROUP) < 4u) __builtin_amdgcn_s_sleep(1);
                __builtin_amdgcn_sched_barrier(0);
                asm volatile("" ::: "memory");
            }
            if (s + 1 < 18) {
#pragma unroll
                for (int mt = 0; mt < 4; ++mt) A[(s + 1) & 1][mt] = *(const LAS bf16x8*)(ab + (((s + 1) & 1) ? o1 : o0) + mt * 2048 + ((s + 1) >> 1) * 8192);
            }
            const int t = ph * 18 + s;
#pragma unroll
            for (int b = 0; b < 3; ++b) {
#pragma unroll
                for (int mt = 0; mt < 4; ++mt) acc[b][mt] = MFMA16(W[t % RING][b], A[s & 1][mt], acc[b][mt]);
                if (t + RING < 36) W[t % RING][b] = bld16(rw, wv + b * 1024, wsb + (t + RING) * 36864);
            }
            __builtin_amdgcn_sched_barrier(0);
        }
        if (ph == 0) {
            STAMP(ws, 4, 2);
#pragma unroll
            for (int mt = 0; mt < 4; ++mt) {
                const f32x4 k = st[mt * 16 + i16];
#pragma unroll
                for (int b = 0; b < 3; ++b) {
                    float o[4];
#pragma unroll
                    for (int e = 0; e < 4; ++e) {
                        const float a = acc[b][mt][e];
                        const float ex = __builtin_amdgcn_exp2f(a * (k[0] + k[1] * (a * a)));
                        o[e] = a * __builtin_amdgcn_rcpf(k[2] + k[2] * ex);
                    }
                    const int nb = nbs + b;
                    *(LAS u32x2*)(lds + KE_HID + (nb >> 2) * 8192 + (mt * 16 + i16) * 128 + (((2 * (nb & 3) + (g >> 1)) ^ z) * 16) + 8 * (g & 1)) =
                        (u32x2){pk_bf16(o[0], o[1]), pk_bf16(o[2], o[3])};
                    acc[b][mt] = (f32x4){0.f, 0.f, 0.f, 0.f};
                }
            }
            asm volatile("s_waitcnt lgkmcnt(0)" ::: "memory");
            if (lane == 0) __hip_atomic_fetch_add(flg + (w >> 2), 1u, __ATOMIC_RELAXED, __HIP_MEMORY_SCOPE_WORKGROUP);
            STAMP(ws, 4, 3);
            if (w >= 8) __builtin_amdgcn_s_setprio(2); else if (w >= 4) __builtin_amdgcn_s_setprio(1);
        }
    }
    STAMP(ws, 4, 4);
    u32x4* pp = (u32x4*)(ws + WS_PART) + ((size_t)(panel * 8 + j) * 72 + nbs * 2) * 64 + lane;
#pragma unroll
    for (int b = 0; b < 3; ++b)
#pragma unroll
        for (int m2 = 0; m2 < 2; ++m2) {
            const f32x4 a0 = acc[b][2 * m2], a1 = acc[b][2 * m2 + 1];
            pp[(b * 2 + m2) * 64] = (u32x4){pk_bf16(a0[0], a0[1]), pk_bf16(a0[2], a0[3]), pk_bf16(a1[0], a1[1]), pk_bf16(a1[2], a1[3])};
        }
    STAMP(ws, 4, 5);
}

NOPK __global__ __launch_bounds__(576) void kf_kernel(const char* __restrict__ ws, u16* __restrict__ out) {
    const int tid = threadIdx.x, w = tid >> 6, lane = tid & 63;
    const int jj = blockIdx.x & 7, panel = blockIdx.x >> 3;
    STAMP((char*)ws, 5, 0);
    const int pr = jj * 9 + w, nb = pr >> 1, m2 = pr & 1;
    const u32x4* pp = (const u32x4*)(ws + WS_PART) + ((size_t)(panel * 8) * 72 + pr) * 64 + lane;
    u32x4 v[8];
#pragma unroll
    for (int k = 0; k < 8; ++k) v[k] = pp[(size_t)k * 72 * 64];
    const int g = lane >> 4, i16 = lane & 15;
    u32x2 xa[2], ha[2];
    size_t off[2];
#pragma unroll
    for (int t = 0; t < 2; ++t) {
        off[t] = (size_t)(panel * 64 + m2 * 32 + t * 16 + i16) * D + nb * 16 + 4 * g;
        xa[t] = *(const u32x2*)((const u16*)(ws + WS_X1) + off[t]);
        ha[t] = *(const u32x2*)((const u16*)(ws + WS_HR) + off[t]);
    }
    float s[8] = {};
#pragma unroll
    for (int k = 0; k < 8; ++k)
#pragma unroll
        for (int e = 0; e < 4; ++e) { s[2 * e] += bf_lo(v[k][e]); s[2 * e + 1] += bf_hi(v[k][e]); }
#pragma unroll
    for (int t = 0; t < 2; ++t) {
        float o[4];
#pragma unroll
        for (int k = 0; k < 4; ++k) {
            const float x1 = (k & 1) ? bf_hi(xa[t][k >> 1]) : bf_lo(xa[t][k >> 1]);
            const float h = (k & 1) ? bf_hi(ha[t][k >> 1]) : bf_lo(ha[t][k >> 1]);
            o[k] = h + rbf(x1 + rbf(s[4 * t + k]));
        }
        *(u32x2*)(out + off[t]) = (u32x2){pk_bf16(o[0], o[1]), pk_bf16(o[2], o[3])};
    }
    STAMP((char*)ws, 5, 1);
}

extern "C" void kernel_launch(void* const* d_in, const int* in_sizes, int n_in,
                              void* d_out, int out_size, void* d_ws, size_t ws_size,
                              hipStream_t stream) {
    char* ws = (char*)d_ws;
    KAArgs a;
    a.x = (const u16*)d_in[0];
    a.are = (const u16*)d_in[1]; a.aim = (const u16*)d_in[2];
    a.bre = (const u16*)d_in[3]; a.bim = (const u16*)d_in[4];
    a.cre = (const u16*)d_in[5]; a.cim = (const u16*)d_in[6];
    a.logdt = (const u16*)d_in[7];
    a.w1 = (const u16*)d_in[10]; a.w2 = (const u16*)d_in[11];
    a.w3 = (const u16*)d_in[12]; a.w4 = (const u16*)d_in[13];
    a.ws = ws;
    const u16* dskip = (const u16*)d_in[8];
    (void)hipFuncSetAttribute((const void*)kc_kernel, hipFuncAttributeMaxDynamicSharedMemorySize, KC_LDS);
    (void)hipFuncSetAttribute((const void*)kd_kernel, hipFuncAttributeMaxDynamicSharedMemorySize, KD_LDS);
    (void)hipFuncSetAttribute((const void*)ke_kernel, hipFuncAttributeMaxDynamicSharedMemorySize, KE_LDS);
    ka_kernel<<<KA_LN + KA_KG + KA_PREP, 256, KA_LDS, stream>>>(a);
    kb_kernel<<<D, 256, KB_LDS, stream>>>(ws, dskip);
    kc_kernel<<<256, 768, KC_LDS, stream>>>(ws);
    kd_kernel<<<256, 768, KD_LDS, stream>>>(ws);
    ke_kernel<<<256, 768, KE_LDS, stream>>>(ws);
    kf_kernel<<<256, 576, 0, stream>>>(ws, (u16*)d_out);
}
```

```cpp
#include <hip/hip_runtime.h>
#include <hip/hip_bf16.h>
#include <stdint.h>

typedef unsigned short u16;
typedef short bf16x8 __attribute__((ext_vector_type(8)));
typedef short s16x4 __attribute__((ext_vector_type(4)));
typedef float f32x2 __attribute__((ext_vector_type(2)));
typedef float f32x4 __attribute__((ext_vector_type(4)));
typedef float f32x16 __attribute__((ext_vector_type(16)));
typedef unsigned u32x2 __attribute__((ext_vector_type(2)));
typedef unsigned u32x4 __attribute__((ext_vector_type(4)));
typedef __bf16 bf16x2_t __attribute__((ext_vector_type(2)));
typedef _Float16 f16x2 __attribute__((ext_vector_type(2)));
typedef _Float16 f16x8 __attribute__((ext_vector_type(8)));
#define LAS __attribute__((address_space(3)))
#define GAS __attribute__((address_space(1)))
#if defined(__HIP_DEVICE_COMPILE__)
#define NOPK __attribute__((target("no-packed-fp32-ops")))
#else
#define NOPK
#endif

constexpr int L = 2048, D = 576, NS = 128, F = 4608;
constexpr int WSUB = D * D;
constexpr float LN_EPS = 1e-5f;
constexpr int NWG = 256, NTHR = 768;
constexpr int SC1 = 16;

constexpr size_t WS_EPOCH = 0;
constexpr size_t WS_FA = 256;
constexpr size_t WS_FH = WS_FA + 1024;
constexpr size_t WS_FW = WS_FH + 1024;
constexpr size_t WS_FG = WS_FW + 2048;
constexpr size_t WS_FX = WS_FG + 1024;
constexpr size_t WS_FP = WS_FX + 1024;
constexpr size_t WS_GR = 8192;
constexpr size_t WS_HT = WS_GR + (size_t)L * 8;
constexpr size_t WS_WF = WS_HT + (size_t)D * L * 2;
constexpr size_t WS_CS = WS_WF + (size_t)18 * WSUB * 2;
constexpr size_t WS_ST2 = WS_CS + (size_t)2 * (D + F) * 4;
constexpr size_t WS_G = WS_ST2 + (size_t)L * 8;
constexpr size_t WS_X1 = WS_G + (size_t)(L + 4) * D * 2;
constexpr size_t WS_HR = WS_X1 + (size_t)L * D * 2;
constexpr size_t WS_PST = WS_HR + (size_t)L * D * 2;
constexpr size_t WS_PART = WS_PST + (size_t)L * 8 * 8;
constexpr size_t WS_END = WS_PART + (size_t)32 * 8 * 72 * 512 * 2;

#define STAMP(ws, i) do {} while (0)
#define STAMP_DECL
#define STAMP_ARG
#define STAMP_PASS
#define STAMP_FLUSH(ws) do {} while (0)

__device__ __forceinline__ unsigned pk_bf16(float lo, float hi) {
    f32x2 v = {lo, hi};
    return __builtin_bit_cast(unsigned, __builtin_convertvector(v, bf16x2_t));
}
__device__ __forceinline__ float bf_lo(unsigned p) { return __uint_as_float(p << 16); }
__device__ __forceinline__ float bf_hi(unsigned p) { return __uint_as_float(p & 0xffff0000u); }
__device__ __forceinline__ float bf2f(u16 x) { return __uint_as_float((unsigned)x << 16); }
__device__ __forceinline__ float rbf(float x) { return bf_lo(pk_bf16(x, x)); }
__device__ __forceinline__ float dot2bf(unsigned a, unsigned b, float c) {
    return __builtin_amdgcn_fdot2_f32_bf16(__builtin_bit_cast(bf16x2_t, a), __builtin_bit_cast(bf16x2_t, b), c, false);
}
__device__ __forceinline__ float gelu_tanh(float x) {
    const float c0 = -2.0f * 0.7978845608028654f * 1.4426950408889634f;
    const float c1 = c0 * 0.044715f;
    float e = __builtin_amdgcn_exp2f(x * (c0 + c1 * x * x));
    return x * __builtin_amdgcn_rcpf(1.0f + e);
}
__device__ __forceinline__ s16x4 tr_read(const LAS char* p) {
    return __builtin_amdgcn_ds_read_tr16_b64_v4i16((LAS s16x4*)p);
}
__device__ __forceinline__ bf16x8 cat8(s16x4 a, s16x4 b) {
    return __builtin_shufflevector(a, b, 0, 1, 2, 3, 4, 5, 6, 7);
}
__device__ __forceinline__ f32x4 sel4(bool c, f32x4 a, f32x4 b) {
    f32x4 r;
#pragma unroll
    for (int k = 0; k < 4; ++k) { float x = a[k], y = b[k]; asm volatile("" : "+v"(x), "+v"(y)); r[k] = c ? x : y; }
    return r;
}
__device__ __forceinline__ float rsum_g(float x) {
    const unsigned u = __float_as_uint(x);
    const auto a = __builtin_amdgcn_permlane16_swap(u, u, false, false);
    const float y = __uint_as_float(a[0]) + __uint_as_float(a[1]);
    const unsigned v = __float_as_uint(y);
    const auto b = __builtin_amdgcn_permlane32_swap(v, v, false, false);
    return __uint_as_float(b[0]) + __uint_as_float(b[1]);
}
template <int CTRL> __device__ __forceinline__ float dpp_f(float x) {
    return __uint_as_float(__builtin_amdgcn_update_dpp(0u, __float_as_uint(x), CTRL, 0xf, 0xf, false));
}
__device__ __forceinline__ float rsum_64(float x) {
    x += dpp_f<0xB1>(x);
    x += dpp_f<0x4E>(x);
    x += dpp_f<0x141>(x);
    x += dpp_f<0x140>(x);
    return rsum_g(x);
}
template <int CTRL> __device__ __forceinline__ unsigned dpp_u(unsigned x) { return __builtin_amdgcn_update_dpp(0u, x, CTRL, 0xf, 0xf, false); }
__device__ __forceinline__ unsigned umax2(unsigned a, unsigned b) { return a > b ? a : b; }
__device__ __forceinline__ float rmax_64(float xf) {
    unsigned x = __float_as_uint(xf);
    x = umax2(x, dpp_u<0xB1>(x)); x = umax2(x, dpp_u<0x4E>(x)); x = umax2(x, dpp_u<0x141>(x)); x = umax2(x, dpp_u<0x140>(x));
    const auto a = __builtin_amdgcn_permlane16_swap(x, x, false, false); x = umax2(a[0], a[1]);
    const auto b = __builtin_amdgcn_permlane32_swap(x, x, false, false); x = umax2(b[0], b[1]);
    return __uint_as_float(x);
}
__device__ __forceinline__ float ln_sd(float var) { return rbf(__builtin_amdgcn_sqrtf(rbf(rbf(var) + LN_EPS))); }
__device__ __forceinline__ int fsw(int row) { return ((row >> 1) & 1) | (((row >> 3) & 1) << 1); }
#define MFMA16(a, b, c) __builtin_amdgcn_mfma_f32_16x16x32_bf16((a), (b), (c), 0, 0, 0)
#define MFMA32(a, b, c) __builtin_amdgcn_mfma_f32_32x32x16_bf16((a), (b), (c), 0, 0, 0)
#define VM0() asm volatile("s_waitcnt vmcnt(0)" ::: "memory")
#define LGKM0() asm volatile("s_waitcnt lgkmcnt(0)" ::: "memory")
#define BAR() do { asm volatile("" ::: "memory"); __builtin_amdgcn_s_barrier(); asm volatile("" ::: "memory"); } while (0)

typedef __amdgpu_buffer_rsrc_t rsrc_t;
__device__ __forceinline__ rsrc_t mk_rsrc(const void* p, unsigned bytes) { return __builtin_amdgcn_make_buffer_rsrc((void*)p, 0, bytes, 0x00020000); }
template <int AUX> __device__ __forceinline__ bf16x8 bld16(rsrc_t r, int voff, int soff) {
    return __builtin_bit_cast(bf16x8, __builtin_amdgcn_raw_buffer_load_b128(r, voff, soff, AUX));
}
template <int AUX> __device__ __forceinline__ f32x4 bldf4(rsrc_t r, int voff, int soff) {
    return __builtin_bit_cast(f32x4, __builtin_amdgcn_raw_buffer_load_b128(r, voff, soff, AUX));
}
template <int AUX> __device__ __forceinline__ u32x4 bldu4(rsrc_t r, int voff, int soff) {
    return __builtin_amdgcn_raw_buffer_load_b128(r, voff, soff, AUX);
}
template <int AUX> __device__ __forceinline__ u32x2 bldu2(rsrc_t r, int voff, int soff) {
    return __builtin_amdgcn_raw_buffer_load_b64(r, voff, soff, AUX);
}
template <int AUX> __device__ __forceinline__ void blds16(rsrc_t r, LAS char* l, int voff, int soff) {
    __builtin_amdgcn_raw_ptr_buffer_load_lds(r, (LAS void*)l, 16, voff, soff, 0, AUX);
}
__device__ __forceinline__ void bst16(u32x4 v, rsrc_t r, int voff, int soff) { __builtin_amdgcn_raw_buffer_store_b128(v, r, voff, soff, SC1); }
__device__ __forceinline__ void bst8(u32x2 v, rsrc_t r, int voff, int soff) { __builtin_amdgcn_raw_buffer_store_b64(v, r, voff, soff, SC1); }
__device__ __forceinline__ void bst4(unsigned v, rsrc_t r, int voff, int soff) { __builtin_amdgcn_raw_buffer_store_b32(v, r, voff, soff, SC1); }

struct Args {
    const u16* x; const u16 *are, *aim, *bre, *bim, *cre, *cim, *logdt, *dskip;
    const u16 *w1, *w2, *w3, *w4;
    char* ws; u16* out;
};

typedef GAS unsigned gu32;
typedef GAS unsigned long long gu64;
#define RLX_AGENT __ATOMIC_RELAXED, __HIP_MEMORY_SCOPE_AGENT
constexpr unsigned long long WAIT_TICKS = 400000ull;
__device__ __forceinline__ void publish(char* ws, size_t flag_off, int idx, unsigned tag) {
    if (threadIdx.x == 0) __hip_atomic_store((gu32*)(ws + flag_off) + idx, tag, RLX_AGENT);
}
__device__ __forceinline__ void wait_flags(char* ws, size_t offa, int a0, int na, size_t offb, int b0, int nb, unsigned tag) {
    const int lane = threadIdx.x & 63;
    const gu32* p[4]; bool on[4];
#pragma unroll
    for (int k = 0; k < 4; ++k) {
        const int i = lane + 64 * k;
        on[k] = i < na + nb;
        p[k] = i < na ? (const gu32*)(ws + offa) + a0 + i : (const gu32*)(ws + offb) + b0 + (on[k] ? i - na : 0);
    }
    const unsigned long long t0 = __builtin_amdgcn_s_memrealtime();
    for (;;) {
        bool ok = true;
#pragma unroll
        for (int k = 0; k < 4; ++k)
            if (64 * k < na + nb) { const unsigned v = __hip_atomic_load(p[k], RLX_AGENT); ok = ok && (!on[k] || v == tag); }
        if (__all(ok)) break;
        if (__builtin_amdgcn_s_memrealtime() - t0 > WAIT_TICKS) break;
        __builtin_amdgcn_s_sleep(2);
    }
}

struct StatRow { u32x4 v0, v1; };
__device__ __forceinline__ StatRow row_stats_load(const Args& a, int w, int lane) {
    const int row = blockIdx.x * 8 + (w & 7);
    const rsrc_t rx = mk_rsrc(a.x + (size_t)row * D, w < 8 ? D * 2 : 0);
    StatRow r;
    r.v0 = bldu4<0>(rx, lane * 16, 0); r.v1 = bldu4<0>(rx, 1024 + lane * 16, 0);
    return r;
}
__device__ __forceinline__ void row_stats(const Args& a, const StatRow& r, unsigned tag, int w, int lane) {
    if (w < 8) {
        float s1 = 0.f, s2 = 0.f;
#pragma unroll
        for (int k = 0; k < 4; ++k) {
            s1 = dot2bf(r.v0[k], 0x3f803f80u, s1); s2 = dot2bf(r.v0[k], r.v0[k], s2);
            s1 = dot2bf(r.v1[k], 0x3f803f80u, s1); s2 = dot2bf(r.v1[k], r.v1[k], s2);
        }
        s1 = rsum_64(s1); s2 = rsum_64(s2);
        const float mean = s1 * (1.0f / D), var = fmaxf(s2 * (1.0f / D) - mean * mean, 0.f);
        const unsigned val = (pk_bf16(mean, ln_sd(var)));
        if (lane == 0) __hip_atomic_store((gu64*)(a.ws + WS_GR) + blockIdx.x * 8 + w, ((unsigned long long)tag << 32) | val, RLX_AGENT);
    }
}

__device__ __forceinline__ void prep_job(const Args& a, int job, LAS char* lds, int lane, unsigned tag) {
    const int sub = job / 18, rem = job % 18, kh = rem / 9, ns = rem % 9;
    const u16* src; int ld;
    if (sub == 0) { src = a.w1; ld = D; }
    else if (sub == 1) { src = a.w2; ld = D; }
    else if ((sub & 1) == 0) { src = a.w3 + ((sub - 2) >> 1) * D; ld = F; }
    else { src = a.w4 + (size_t)((sub - 2) >> 1) * D * D; ld = D; }
    src += (size_t)(kh * 288) * ld + ns * 64;
    const rsrc_t rs = mk_rsrc(src, 288 * ld * 2);
    const rsrc_t rd = mk_rsrc(a.ws + WS_WF + (size_t)sub * WSUB * 2, WSUB * 2);
    const int ldb = ld * 2, fl = (lane >> 4) & 1;
    const int v0 = (lane >> 3) * ldb + (((lane & 7) ^ (fl << 1)) * 16), v1 = (lane >> 3) * ldb + (((lane & 7) ^ ((fl | 2) << 1)) * 16);
    const int g = lane >> 4, i16 = lane & 15, q = i16 >> 2, p = lane & 3;
    const int f = ((q >> 1) & 1) | ((g & 1) << 1);
    int rb[4];
#pragma unroll
    for (int n = 0; n < 4; ++n) rb[n] = (8 * g + q) * 128 + (((2 * n + (p >> 1)) ^ (f << 1)) * 16) + 8 * (p & 1);
#define PREP_DMA(u) do { _Pragma("unroll") for (int i_ = 0; i_ < 4; ++i_) blds16<0>(rs, lds + ((u) % 3) * 4096 + i_ * 1024, (i_ & 1) ? v1 : v0, ((u) * 32 + i_ * 8) * ldb); } while (0)
    PREP_DMA(0); PREP_DMA(1); PREP_DMA(2);
    float cs[4] = {0.f, 0.f, 0.f, 0.f};
#pragma unroll
    for (int u = 0; u < 9; ++u) {
        if (u == 0) asm volatile("s_waitcnt vmcnt(8)" ::: "memory");
        else if (u == 1) asm volatile("s_waitcnt vmcnt(12)" ::: "memory");
        else if (u <= 6) asm volatile("s_waitcnt vmcnt(16)" ::: "memory");
        else if (u == 7) asm volatile("s_waitcnt vmcnt(12)" ::: "memory");
        else asm volatile("s_waitcnt vmcnt(8)" ::: "memory");
        bf16x8 v[4];
#pragma unroll
        for (int n = 0; n < 4; ++n) v[n] = cat8(tr_read(lds + (u % 3) * 4096 + rb[n]), tr_read(lds + (u % 3) * 4096 + rb[n] + 512));
        LGKM0();
        __builtin_amdgcn_sched_barrier(0);
#pragma unroll
        for (int n = 0; n < 4; ++n) {
            const u32x4 x = __builtin_bit_cast(u32x4, v[n]);
            bst16(x, rd, lane * 16, ((kh * 9 + u) * 36 + ns * 4 + n) * 1024);
#pragma unroll
            for (int k = 0; k < 4; ++k) cs[n] = dot2bf(x[k], 0x3f803f80u, cs[n]);
        }
        __builtin_amdgcn_sched_barrier(0);
        if (u + 3 < 9) PREP_DMA(u + 3);
        __builtin_amdgcn_sched_barrier(0);
    }
#undef PREP_DMA
    if ((sub & 1) == 0) {
        const rsrc_t rc = mk_rsrc(a.ws + WS_CS, 2 * (D + F) * 4);
#pragma unroll
        for (int n = 0; n < 4; ++n) {
            const float c = rsum_g(cs[n]);
            if (lane < 16) bst4(__float_as_uint(c), rc, lane * 4, (kh * (D + F) + (sub == 0 ? 0 : D + ((sub - 2) >> 1) * D) + ns * 64 + n * 16) * 4);
        }
    }
    VM0();
    if (lane == 0) __hip_atomic_store((gu32*)(a.ws + WS_FW) + job, tag, RLX_AGENT);
}

constexpr int CV_RC = 4160;
constexpr int CV_U = 4 * CV_RC;
constexpr int CV_X = CV_U + 2288 * 2;
constexpr int CV_RED = CV_X + 4096;
constexpr int CV_CH = CV_RED + 24576;
constexpr int KG_ROW = 528;
constexpr int KG_XI = 32 * KG_ROW, KG_T = CV_RED + 4096;

struct cpx { float re, im; };
__device__ __forceinline__ cpx cmul(cpx a, cpx b) { return {a.re * b.re - a.im * b.im, a.re * b.im + a.im * b.re}; }
__device__ __forceinline__ cpx cexp_rev(float a2, float rev, float m) {
    const float e = __builtin_amdgcn_exp2f(a2 * m), p = __builtin_amdgcn_fractf(rev * m);
    return {e * __builtin_amdgcn_cosf(p), e * __builtin_amdgcn_sinf(p)};
}
__device__ __forceinline__ unsigned pk_f16(cpx c) { f32x2 v = {c.re, c.im}; return __builtin_bit_cast(unsigned, __builtin_convertvector(v, f16x2)); }

__device__ __forceinline__ void conv_put(LAS char* lds, int m, u32x4 A, u32x2 B) {
    u32x4 c1 = {__builtin_amdgcn_alignbit(A[1], A[0], 16), __builtin_amdgcn_alignbit(A[2], A[1], 16), __builtin_amdgcn_alignbit(A[3], A[2], 16), __builtin_amdgcn_alignbit(B[0], A[3], 16)};
    u32x4 c2 = {A[1], A[2], A[3], B[0]};
    u32x4 c3 = {c1[1], c1[2], c1[3], __builtin_amdgcn_alignbit(B[1], B[0], 16)};
    *(LAS u32x4*)(lds + 0 * CV_RC + m * 16) = A;
    *(LAS u32x4*)(lds + 1 * CV_RC + m * 16) = c1;
    *(LAS u32x4*)(lds + 2 * CV_RC + m * 16) = c2;
    *(LAS u32x4*)(lds + 3 * CV_RC + m * 16) = c3;
}

__device__ __forceinline__ void phase_conv(const Args& a, LAS char* lds0, unsigned tagv, const StatRow& sr, unsigned& tag_out STAMP_ARG) {
    const int tid = threadIdx.x, w = __builtin_amdgcn_readfirstlane(tid >> 6), lane = tid & 63;
    const int b = blockIdx.x, chl = w >> 2, wq = w & 3, t8 = tid & 255, ch = 3 * b + chl;
    LAS char* lds = lds0 + chl * CV_CH;
    const int part = wq >> 1, n = (wq & 1) * 64 + lane;
    const int o = ch * NS + n, o2 = ch * NS + (n ^ 64);
    const u16 r_ld = a.logdt[ch], r_ar = a.are[o], r_ai = a.aim[o], r_ar2 = a.are[o2];
    u16 r_b[4] = {}, r_c[4] = {};
    if (part) { r_b[0] = a.bre[o]; r_b[1] = a.bim[o]; r_c[0] = a.cre[o]; r_c[1] = a.cim[o]; r_b[2] = a.bre[o2]; r_b[3] = a.bim[o2]; r_c[2] = a.cre[o2]; r_c[3] = a.cim[o2]; }
    const u16 r_ds = a.dskip[ch];
    __builtin_amdgcn_sched_barrier(0);
    const unsigned tag = __builtin_amdgcn_readfirstlane(tagv) + 1u;
    tag_out = tag;
    if (tid == 0) __hip_atomic_store((gu32*)(a.ws + WS_FA) + b, tag, RLX_AGENT);
    row_stats(a, sr, tag, w, lane);
    STAMP(a.ws, 1);
    __builtin_amdgcn_sched_barrier(0);
    u32x2 xr[4];
    const rsrc_t rx = mk_rsrc(a.x, L * D * 2);
    const int xvo = tid * (2 * D * 2) + ((6 * b) & ~3);
#define GATHER(i) do { __builtin_amdgcn_sched_barrier(0); if ((i) < 2 || w < 4) xr[i] = bldu2<0>(rx, xvo + ((i) & 1) * D * 2, ((i) >> 1) * 1536 * D * 2); __builtin_amdgcn_sched_barrier(0); } while (0)
    GATHER(0);
    const float dt = rbf(expf(bf2f(r_ld)));
    const float are = dt * bf2f(r_ar);
    const float ap = rmax_64(fmaxf(fmaxf(are, dt * bf2f(r_ar2)), 0.f));
    const float a2 = (are - ap) * 1.4426950408889634f, rev = dt * bf2f(r_ai) * 0.15915494309189535f;
    float kscale = 1.f;
    if (part == 0) {
        cpx e[4];
        const cpx g8 = cexp_rev(a2, rev, 8.f), g32 = cexp_rev(a2, rev, 32.f);
        e[0] = {1.f, 0.f}; e[1] = g8; e[2] = cmul(g8, g8); e[3] = cmul(e[2], g8);
        LAS char* er = lds + n * 4;
#pragma unroll
        for (int t = 0; t < 8; ++t) {
            if (t == 0) GATHER(1);
            if (t == 3) GATHER(2);
            if (t == 6) GATHER(3);
#pragma unroll
            for (int c = 0; c < 4; ++c) {
                *(LAS unsigned*)(er + (4 * t + c) * KG_ROW) = pk_f16(e[c]);
                if (t < 7) e[c] = (t == 0 && c == 0) ? g32 : cmul(e[c], g32);
            }
        }
    } else {
        const float b0 = dt * bf2f(r_b[0]), b1 = dt * bf2f(r_b[1]), c0 = bf2f(r_c[0]), c1 = bf2f(r_c[1]);
        const float b2 = dt * bf2f(r_b[2]), b3 = dt * bf2f(r_b[3]), c2 = bf2f(r_c[2]), c3 = bf2f(r_c[3]);
        const float wr = c0 * b0 - c1 * b1, wi = c0 * b1 + c1 * b0, vr = c2 * b2 - c3 * b3, vi = c2 * b3 + c3 * b2;
        const float wm = rmax_64(fmaxf(fmaxf(fabsf(wr), fabsf(wi)), fmaxf(fabsf(vr), fabsf(vi))));
        const unsigned ex = (__float_as_uint(wm) >> 23) & 0xffu;
        const unsigned exc = ex < 2u ? 2u : (ex > 252u ? 252u : ex);
        const float sc = __uint_as_float((254u - exc) << 23);
        kscale = __uint_as_float(exc << 23);
        const cpx W = {wr * sc, -wi * sc};
        const cpx g1 = cexp_rev(a2, -rev, 1.f), p256 = cexp_rev(a2, -rev, 256.f);
        const cpx g2 = cmul(g1, g1), g4 = cmul(g2, g2), p512 = cmul(p256, p256);
        LAS char* xr_ = lds + KG_XI + n * 4;
#define XST(i, v) *(LAS unsigned*)(xr_ + (i) * KG_ROW) = pk_f16(v)
        GATHER(1);
        cpx x = W; XST(0, x);
#pragma unroll
        for (int i = 1; i < 8; ++i) { x = cmul(x, g1); XST(i, x); }
        GATHER(2);
        x = cmul(W, p256); XST(8, x);
#pragma unroll
        for (int i = 9; i < 12; ++i) { x = cmul(x, g2); XST(i, x); }
        cpx y = cmul(W, p512); XST(12, y); XST(13, cmul(y, g4));
        x = cmul(y, p256); XST(14, x); XST(15, cmul(x, g4));
        GATHER(3);
        y = cmul(y, p512); XST(16, y);
#pragma unroll
        for (int i = 17; i < 21; ++i) { y = cmul(y, p256); XST(i, y); }
#undef XST
#undef GATHER
        if (lane == 0 && wq == 2) *(LAS float*)(lds + KG_T - 16) = kscale;
    }
    const int p = lane & 15, g = lane >> 4;
    LAS float* T = (LAS float*)(lds + KG_T);
    int base, idx0, istr;
    if (wq & 1) {
        base = p < 4 ? 1024 + 256 * p : (p == 4 ? 2048 : 0);
        idx0 = p < 4 ? 512 + 32 * p : (p == 4 ? 640 : 680);
        istr = 1;
    } else {
        const int q4 = p & 3;
        base = p < 8 ? p : (p < 12 ? 256 + 2 * q4 : 512 + 4 * (p & 1) + 128 * (p & 2));
        idx0 = p < 8 ? p : (p < 12 ? 256 + q4 : 384 + (p & 1) + 32 * (p & 2));
        istr = p < 8 ? 8 : (p < 12 ? 4 : 2);
    }
    const int j0 = (wq >> 1) * 16 + 4 * g;
    LAS float* tw = T + idx0 + j0 * istr;
    const LAS char* ea = lds + ((wq >> 1) * 16 + p) * KG_ROW + g * 16;
    const LAS char* xa = lds + KG_XI + ((wq & 1) * 16 + p) * KG_ROW + g * 16;
    STAMP(a.ws, 29);
    LGKM0(); BAR();
    STAMP(a.ws, 2);
    {
        f32x4 acc = {};
        f16x8 A[8], B[8];
#pragma unroll
        for (int t = 0; t < 8; ++t) { A[t] = *(const LAS f16x8*)(ea + t * 64); B[t] = *(const LAS f16x8*)(xa + t * 64); }
        float ks = *(const LAS float*)(lds + KG_T - 16);
        __builtin_amdgcn_sched_barrier(0);
#pragma unroll
        for (int t = 0; t < 8; ++t) acc = __builtin_amdgcn_mfma_f32_16x16x32_f16(A[t], B[t], acc, 0, 0, 0);
        if (ap > 0.f) {
#pragma unroll
            for (int q = 0; q < 4; ++q) acc[q] *= __builtin_amdgcn_exp2f(ap * 1.4426950408889634f * (float)(base + 8 * (j0 + q)));
        }
#pragma unroll
        for (int q = 0; q < 4; ++q) tw[q * istr] = acc[q] * ks;
    }
    LGKM0(); BAR();
    STAMP(a.ws, 3);
    {
        float v[12];
        const float lf = (float)(2047 - t8 * 8);
        const unsigned tb = (unsigned)(size_t)T - 17024u * 4u;
        if (wq < 3) {
#pragma unroll
            for (int e = 0; e < 12; e += 2) {
                const unsigned bits = pk_bf16(lf - e, lf - (e + 1));
                v[e] = *(const LAS float*)(size_t)(tb + (bits & 0xffffu) * 4u);
                v[e + 1] = *(const LAS float*)(size_t)(tb + (bits >> 16) * 4u);
            }
        } else {
#pragma unroll
            for (int e = 0; e < 12; ++e) {
                const int l = 2047 - (t8 * 8 + e);
                const int idx = l < 128 ? (l < 0 ? 0 : l) : (int)(pk_bf16((float)l, 0.f) & 0xffffu) - 17024;
                const float t = T[idx];
                v[e] = l < 0 ? 0.f : t;
            }
        }
        conv_put(lds, t8, (u32x4){pk_bf16(v[0], v[1]), pk_bf16(v[2], v[3]), pk_bf16(v[4], v[5]), pk_bf16(v[6], v[7])}, (u32x2){pk_bf16(v[8], v[9]), pk_bf16(v[10], v[11])});
        if (t8 < 4) conv_put(lds, 256 + t8, (u32x4){0u, 0u, 0u, 0u}, (u32x2){0u, 0u});
        if (t8 >= 64 && t8 < 94) *(LAS u32x4*)(lds + CV_U + (t8 - 64) * 16) = (u32x4){0u, 0u, 0u, 0u};
    }
    STAMP(a.ws, 4);
    {
        u32x4 gv[2];
        const rsrc_t rgn = mk_rsrc(a.ws + WS_GR, L * 8);
        const unsigned long long t0 = __builtin_amdgcn_s_memrealtime();
        for (;;) {
            gv[0] = bldu4<SC1>(rgn, tid * 16, 0);
            gv[1] = w < 4 ? bldu4<SC1>(rgn, tid * 16, 1536 * 8) : gv[0];
            const bool ok = gv[0][1] == tag && gv[0][3] == tag && gv[1][1] == tag && gv[1][3] == tag;
            if (__all(ok)) break;
            if (__builtin_amdgcn_s_memrealtime() - t0 > WAIT_TICKS) break;
            __builtin_amdgcn_s_sleep(2);
            asm volatile("" ::: "memory");
        }
#pragma unroll
        for (int i = 0; i < 2; ++i) {
            if (i == 0 || w < 4) {
                const int row = 2 * tid + 1536 * i;
                u32x2 x0 = xr[2 * i], x1 = xr[2 * i + 1];
                if (b & 1) {
                    x0 = (u32x2){__builtin_amdgcn_alignbit(x0[1], x0[0], 16), x0[1] >> 16};
                    x1 = (u32x2){__builtin_amdgcn_alignbit(x1[1], x1[0], 16), x1[1] >> 16};
                }
                const float m0 = bf_lo(gv[i][0]), m1 = bf_lo(gv[i][2]);
                const float s0 = __builtin_amdgcn_rcpf(bf_hi(gv[i][0])), s1 = __builtin_amdgcn_rcpf(bf_hi(gv[i][2]));
#pragma unroll
                for (int c = 0; c < 3; ++c) {
                    const unsigned xp = c == 0 ? __builtin_amdgcn_perm(x1[0], x0[0], 0x05040100u) : c == 1 ? __builtin_amdgcn_perm(x1[0], x0[0], 0x07060302u)
                                                                                                            : __builtin_amdgcn_perm(x1[1], x0[1], 0x05040100u);
                    const unsigned dd = pk_bf16(bf_lo(xp) - m0, bf_hi(xp) - m1);
                    *(LAS unsigned*)(lds0 + c * CV_CH + CV_U + 480 + row * 2) = pk_bf16(bf_lo(dd) * s0, bf_hi(dd) * s1);
                    *(LAS unsigned*)(lds0 + c * CV_CH + CV_X + row * 2) = xp;
                }
            }
        }
    }
    LGKM0(); BAR();
    STAMP(a.ws, 5);
    f32x4 acc[8];
#pragma unroll
    for (int c = 0; c < 8; ++c) acc[c] = (f32x4){0.f, 0.f, 0.f, 0.f};
    const int cpy = (3 - p) & 3;
#pragma unroll
    for (int rr = 0; rr < 2; ++rr) {
        const int r = 2 * wq + rr;
        const LAS char* ab = lds + cpy * CV_RC + 2 * (2031 - 32 * r + 8 * g - p - cpy) - 512 * 7;
        const LAS char* fb = lds + CV_U + 2 * (240 - 32 * r + 16 * p - 16 + 8 * g);
        bf16x8 A[8], Fm[8];
#pragma unroll
        for (int s = 0; s < 8; ++s) {
            const s16x4 lo = *(const volatile LAS s16x4*)(ab + 512 * (7 - s));
            const s16x4 hi = *(const volatile LAS s16x4*)(ab + 512 * (7 - s) + 8);
            A[s] = cat8(lo, hi);
        }
#pragma unroll
        for (int e = 0; e < 8; ++e) Fm[e] = *(const LAS bf16x8*)(fb + 512 * e);
#pragma unroll
        for (int c = 0; c < 8; ++c)
#pragma unroll
            for (int s = 0; s <= c; ++s) acc[c] = MFMA16(A[s], Fm[c - s], acc[c]);
    }
    STAMP(a.ws, 6);
    LAS f32x4* red = (LAS f32x4*)(lds + CV_RED);
#pragma unroll
    for (int c = 0; c < 8; ++c) {
        const int ow = c >> 1;
        if (ow != wq) red[(ow * 6 + (c & 1) * 3 + (wq > ow ? wq - 1 : wq)) * 64 + lane] = acc[c];
    }
    LGKM0(); BAR();
    const rsrc_t rh = mk_rsrc(a.ws + WS_HT + (size_t)ch * L * 2, L * 2);
    const float dsk = bf2f(r_ds);
#pragma unroll
    for (int i = 0; i < 2; ++i) {
        f32x4 y;
#pragma unroll
        for (int k = 0; k < 4; ++k) {
            const float y01 = (wq & 1) ? acc[2 + i][k] : acc[i][k], y23 = (wq & 1) ? acc[6 + i][k] : acc[4 + i][k];
            y[k] = (wq & 2) ? y23 : y01;
        }
#pragma unroll
        for (int k = 0; k < 3; ++k) y += red[(wq * 6 + i * 3 + k) * 64 + lane];
        const int l0 = 256 * (2 * wq + i) + 16 * p + 4 * g;
        const u32x2 uu = *(const LAS u32x2*)(lds + CV_U + 480 + 2 * l0), xv = *(const LAS u32x2*)(lds + CV_X + 2 * l0);
        float o[4];
#pragma unroll
        for (int k = 0; k < 4; ++k) {
            const float u = (k & 1) ? bf_hi(uu[k >> 1]) : bf_lo(uu[k >> 1]);
            const float x = (k & 1) ? bf_hi(xv[k >> 1]) : bf_lo(xv[k >> 1]);
            o[k] = x + __builtin_fmaf(u, dsk, y[k]);
        }
        bst8((u32x2){pk_bf16(o[0], o[1]), pk_bf16(o[2], o[3])}, rh, 2 * l0, 0);
    }
    STAMP(a.ws, 28);
    VM0(); BAR();
    publish(a.ws, WS_FH, b, tag);
}

__device__ __forceinline__ int nb0_of(int j) { return (9 * j + 1) >> 1; }

constexpr int KC_XB = 73728, KC_ST = KC_XB + 20 * 1024;
__device__ __forceinline__ void phase_c(char* __restrict__ ws, LAS char* lds, int panel, int j, int je, unsigned tag STAMP_ARG) {
    const int tid = threadIdx.x, w = __builtin_amdgcn_readfirstlane(tid >> 6), lane = tid & 63;
    const int tok0 = panel * 64, nbs = nb0_of(j), nblk = nb0_of(j + 1) - nbs;
    const int g = lane >> 4, i16 = lane & 15, q = i16 >> 2, p = lane & 3;
    const int b = w >> 1, kh = w & 1;
    const bool act = w < 10 && b < nblk;
    const int nb = nbs + (act ? b : 0);
    if (w == 0) wait_flags(ws, WS_FW, 0, 36, WS_FW, (2 + 2 * je) * 18, 36, tag);
    BAR();
    bf16x8 W[9];
    f32x4 cs0, cs1;
    if (w < 10) {
        const rsrc_t rw = mk_rsrc(ws + WS_WF, WSUB * 2);
#pragma unroll
        for (int i = 0; i < 9; ++i) W[i] = bld16<SC1>(rw, lane * 16, ((kh * 9 + i) * 36 + nb) * 1024);
        const rsrc_t rc = mk_rsrc(ws + WS_CS, 2 * (D + F) * 4);
        cs0 = bldf4<SC1>(rc, g * 16, nb * 64); cs1 = bldf4<SC1>(rc, g * 16, nb * 64 + (D + F) * 4);
    }
    if (w == 11) wait_flags(ws, WS_FH, 0, 192, WS_FH, 0, 0, tag);
    BAR();
    STAMP(ws, 8);
    {
        const rsrc_t rh = mk_rsrc(ws + WS_HT, D * L * 2);
        const int fl = (lane >> 4) & 1;
        const int v0 = (lane >> 3) * 4096 + (((lane & 7) ^ (fl << 1)) * 16), v1 = (lane >> 3) * 4096 + (((lane & 7) ^ ((fl | 2) << 1)) * 16);
#pragma unroll
        for (int k = 0; k < 6; ++k) { const int idx = 6 * w + k; blds16<SC1>(rh, lds + idx * 1024, (k & 1) ? v1 : v0, idx * 32768 + tok0 * 2); }
    }
    VM0(); BAR();
    STAMP(ws, 9);
    const int f = ((q >> 1) & 1) | ((g & 1) << 1);
    const int lo_ = (8 * g + q) * 128 + 8 * (p & 1);
    f32x4 acc[4] = {};
    LAS f32x2* st = (LAS f32x2*)(lds + KC_ST);
    LAS f32x4* xb = (LAS f32x4*)(lds + KC_XB);
    if (w < 10) {
        const LAS char* hb[4];
#pragma unroll
        for (int mt = 0; mt < 4; ++mt) hb[mt] = lds + kh * 9 * 4096 + lo_ + (((2 * mt + (p >> 1)) ^ (f << 1)) * 16);
        bf16x8 H[2][2][4];
#pragma unroll
        for (int c = 0; c < 6; ++c) {
            if (c < 5) {
#pragma unroll
                for (int i = 0; i < 2; ++i)
                    if (2 * c + i < 9) {
#pragma unroll
                        for (int mt = 0; mt < 4; ++mt) H[c & 1][i][mt] = cat8(tr_read(hb[mt] + (2 * c + i) * 4096), tr_read(hb[mt] + (2 * c + i) * 4096 + 512));
                    }
            }
            __builtin_amdgcn_sched_barrier(0);
            if (c > 0) {
#pragma unroll
                for (int i = 0; i < 2; ++i)
                    if (2 * (c - 1) + i < 9) {
#pragma unroll
                        for (int mt = 0; mt < 4; ++mt) acc[mt] = MFMA16(W[2 * (c - 1) + i], H[(c - 1) & 1][i][mt], acc[mt]);
                    }
            }
            __builtin_amdgcn_sched_barrier(0);
        }
        xb[((w * 2 + 0) * 64) + lane] = sel4(kh, acc[0], acc[2]);
        xb[((w * 2 + 1) * 64) + lane] = sel4(kh, acc[1], acc[3]);
    } else {
        __builtin_amdgcn_s_setprio(3);
        const bf16x8 ones = {0x3f80, 0x3f80, 0x3f80, 0x3f80, 0x3f80, 0x3f80, 0x3f80, 0x3f80};
        const int m0 = 2 * (w - 10);
        const LAS char* h0 = lds + lo_ + (((2 * m0 + (p >> 1)) ^ (f << 1)) * 16);
        const LAS char* h1 = lds + lo_ + (((2 * m0 + 2 + (p >> 1)) ^ (f << 1)) * 16);
        bf16x8 H[2][3][2];
#pragma unroll
        for (int c = 0; c < 7; ++c) {
            if (c < 6) {
#pragma unroll
                for (int i = 0; i < 3; ++i) {
                    H[c & 1][i][0] = cat8(tr_read(h0 + (3 * c + i) * 4096), tr_read(h0 + (3 * c + i) * 4096 + 512));
                    H[c & 1][i][1] = cat8(tr_read(h1 + (3 * c + i) * 4096), tr_read(h1 + (3 * c + i) * 4096 + 512));
                }
            }
            __builtin_amdgcn_sched_barrier(0);
            if (c > 0) {
#pragma unroll
                for (int i = 0; i < 3; ++i) {
                    const bf16x8 H0 = H[(c - 1) & 1][i][0], H1 = H[(c - 1) & 1][i][1];
                    acc[0] = MFMA16(ones, H0, acc[0]); acc[1] = MFMA16(H0, H0, acc[1]);
                    acc[2] = MFMA16(ones, H1, acc[2]); acc[3] = MFMA16(H1, H1, acc[3]);
                }
            }
            __builtin_amdgcn_sched_barrier(0);
        }
        if (g == q) {
            const rsrc_t r2 = mk_rsrc(ws + WS_ST2, L * 8);
#pragma unroll
            for (int t = 0; t < 2; ++t) {
                const f32x4 d = acc[2 * t + 1];
                const float s2 = p == 0 ? d[0] : p == 1 ? d[1] : p == 2 ? d[2] : d[3];
                const float mean = acc[2 * t][0] * (1.0f / D), var = fmaxf(s2 * (1.0f / D) - mean * mean, 0.f);
                const float mb = rbf(mean), rs = __builtin_amdgcn_rcpf(ln_sd(var));
                const float m2 = rbf((mean - mb) * rs), rs2 = __builtin_amdgcn_rcpf(ln_sd(var * rs * rs));
                st[(m0 + t) * 16 + i16] = (f32x2){rs * rs2, (mb * rs + m2) * rs2};
                if (j == 0) bst8((u32x2){__float_as_uint(mb), __float_as_uint(rs)}, r2, (tok0 + (m0 + t) * 16 + i16) * 8, 0);
            }
        }
    }
    __builtin_amdgcn_s_setprio(0);
    STAMP(ws, 24);
    LGKM0(); BAR();
    STAMP(ws, 10);
    const rsrc_t rg = mk_rsrc(ws + WS_G, (L + 4) * D * 2);
    if (act) {
        const f32x4 cs = cs0 + cs1;
#pragma unroll
        for (int t = 0; t < 2; ++t) {
            const f32x4 a = sel4(kh, acc[2 + t], acc[t]) + xb[((w ^ 1) * 2 + t) * 64 + lane];
            const f32x2 ab = st[(2 * kh + t) * 16 + i16];
            float o[4];
#pragma unroll
            for (int k = 0; k < 4; ++k) o[k] = gelu_tanh(ab[0] * a[k] - ab[1] * cs[k]);
            bst8((u32x2){pk_bf16(o[0], o[1]), pk_bf16(o[2], o[3])}, rg, (i16 * D + 4 * g) * 2, ((2 + tok0 + kh * 32 + t * 16) * D + nb * 16) * 2);
        }
        if ((panel == 0 || panel == 31) && kh == 0 && i16 < 2) bst8((u32x2){0u, 0u}, rg, (i16 * D + 4 * g) * 2, ((panel == 0 ? 0 : L + 2) * D + nb * 16) * 2);
    }
    STAMP(ws, 27);
    VM0(); BAR();
    publish(ws, WS_FG, panel * 8 + j, tag);
}

constexpr int KD_HT = 73728, KD_XB = KD_HT + 10240, KD_PS = KD_XB + 20 * 1024;
__device__ __forceinline__ void phase_d(char* __restrict__ ws, LAS char* lds, int panel, int j, unsigned tag STAMP_ARG) {
    const int tid = threadIdx.x, w = __builtin_amdgcn_readfirstlane(tid >> 6), lane = tid & 63;
    const int tok0 = panel * 64, nbs = nb0_of(j), nblk = nb0_of(j + 1) - nbs;
    const int g = lane >> 4, i16 = lane & 15, q = i16 >> 2, p = lane & 3;
    const int b = w >> 1, kh = w & 1;
    const bool act = w < 10 && b < nblk;
    const int nb = nbs + (act ? b : 0);
    bf16x8 W[9];
    if (w < 10) {
        const rsrc_t rw = mk_rsrc(ws + WS_WF + WSUB * 2, WSUB * 2);
#pragma unroll
        for (int i = 0; i < 9; ++i) W[i] = bld16<SC1>(rw, lane * 16, ((kh * 9 + i) * 36 + nb) * 1024);
    }
    if (w < 2 * nblk) {
        const rsrc_t rh = mk_rsrc(ws + WS_HT, D * L * 2);
        const int fl = ((lane >> 4) & 1) | ((w & 1) << 1);
        blds16<SC1>(rh, lds + KD_HT + w * 1024, (lane >> 3) * 4096 + (((lane & 7) ^ (fl << 1)) * 16), (nbs * 16 + w * 8) * 4096 + tok0 * 2);
    }
    if (w == 11) {
        const int p0 = panel > 0 ? panel - 1 : 0, p1 = panel < 31 ? panel + 1 : 31;
        wait_flags(ws, WS_FG, p0 * 8, (p1 - p0 + 1) * 8, WS_FG, 0, 0, tag);
    }
    BAR();
    STAMP(ws, 12);
    f32x2 st[2];
    {
        const rsrc_t rg = mk_rsrc(ws + WS_G, (L + 4) * D * 2);
        const int c7 = (lane & 7) ^ ((lane >> 3) & 6), vb = (lane >> 3) * 1152 + c7 * 16;
#pragma unroll
        for (int k = 0; k < 6; ++k) {
            const int idx = 6 * w + k, cg = idx >> 3, c = cg * 8 + c7;
            const int o2 = (0x4031 >> (((c * 57) >> 10) * 4)) & 15;
            blds16<SC1>(rg, lds + idx * 1024, vb + o2 * 1152, (tok0 + (idx & 7) * 8) * 1152 + cg * 128);
        }
        const rsrc_t r2 = mk_rsrc(ws + WS_ST2, L * 8);
#pragma unroll
        for (int t = 0; t < 2; ++t) st[t] = __builtin_bit_cast(f32x2, bldu2<SC1>(r2, i16 * 8, (tok0 + kh * 32 + t * 16) * 8));
    }
    VM0(); BAR();
    STAMP(ws, 13);
    f32x4 acc[4] = {};
    LAS f32x4* xb = (LAS f32x4*)(lds + KD_XB);
    if (w < 10) {
        const int z = i16 & 6;
        const LAS char* a0 = lds + i16 * 128 + ((g ^ z) * 16), * a1 = lds + i16 * 128 + (((4 + g) ^ z) * 16);
        const LAS char* e0 = kh ? a1 + 4 * 8192 : a0;
        const LAS char* e1 = kh ? a0 + 5 * 8192 : a1;
        bf16x8 A[2][2][4];
#pragma unroll
        for (int c = 0; c < 6; ++c) {
            if (c < 5) {
#pragma unroll
                for (int ii = 0; ii < 2; ++ii) {
                    const int i = 2 * c + ii;
                    if (i < 9) {
#pragma unroll
                        for (int mt = 0; mt < 4; ++mt) A[c & 1][ii][mt] = *(const LAS bf16x8*)(((i & 1) ? e1 : e0) + (i >> 1) * 8192 + mt * 2048);
                    }
                }
            }
            __builtin_amdgcn_sched_barrier(0);
            if (c > 0) {
#pragma unroll
                for (int ii = 0; ii < 2; ++ii)
                    if (2 * (c - 1) + ii < 9) {
#pragma unroll
                        for (int mt = 0; mt < 4; ++mt) acc[mt] = MFMA16(W[2 * (c - 1) + ii], A[(c - 1) & 1][ii][mt], acc[mt]);
                    }
            }
            __builtin_amdgcn_sched_barrier(0);
        }
        xb[((w * 2 + 0) * 64) + lane] = sel4(kh, acc[0], acc[2]);
        xb[((w * 2 + 1) * 64) + lane] = sel4(kh, acc[1], acc[3]);
    }
    STAMP(ws, 25);
    LGKM0(); BAR();
    STAMP(ws, 14);
    LAS f32x2* ps = (LAS f32x2*)(lds + KD_PS);
    if (w < 10) {
        const int bl = act ? b : 0;
        const int hrow = 16 * bl + 4 * g + q;
        const LAS char* hb = lds + KD_HT + hrow * 128 + 8 * (p & 1);
        const rsrc_t r1 = mk_rsrc(ws + WS_X1, L * D * 2), rr = mk_rsrc(ws + WS_HR, L * D * 2);
#pragma unroll
        for (int t = 0; t < 2; ++t) {
            const int mt = 2 * kh + t;
            const f32x4 a = sel4(kh, acc[2 + t], acc[t]) + xb[((w ^ 1) * 2 + t) * 64 + lane];
            const u32x2 hu = __builtin_bit_cast(u32x2, tr_read(hb + (((2 * mt + (p >> 1)) ^ (fsw(hrow) << 1)) * 16)));
            const float rs = st[t][1], nm = -st[t][0] * rs;
            u32x2 ov;
#pragma unroll
            for (int k = 0; k < 2; ++k)
                ov[k] = pk_bf16(__builtin_fmaf(bf_lo(hu[k]), rs, nm) + a[2 * k], __builtin_fmaf(bf_hi(hu[k]), rs, nm) + a[2 * k + 1]);
            float s1 = dot2bf(ov[1], 0x3f803f80u, dot2bf(ov[0], 0x3f803f80u, 0.f)), s2 = dot2bf(ov[1], ov[1], dot2bf(ov[0], ov[0], 0.f));
            s1 = rsum_g(s1); s2 = rsum_g(s2);
            if (lane < 16) ps[b * 64 + mt * 16 + lane] = act ? (f32x2){s1, s2} : (f32x2){0.f, 0.f};
            if (act) {
                const int so = ((tok0 + mt * 16) * D + nb * 16) * 2, vo = (i16 * D + 4 * g) * 2;
                bst8(ov, r1, vo, so);
                bst8(hu, rr, vo, so);
            }
        }
    }
    STAMP(ws, 26);
    LGKM0(); BAR();
    if (tid < 64) {
        f32x2 t = ps[tid];
#pragma unroll
        for (int k = 1; k < 5; ++k) t += ps[k * 64 + tid];
        bst8(__builtin_bit_cast(u32x2, t), mk_rsrc(ws + WS_PST, L * 64), tid * 64, tok0 * 64 + j * 8);
    }
    VM0(); BAR();
    publish(ws, WS_FX, panel * 8 + j, tag);
}

constexpr int KE_HID = 73728, KE_ST = 2 * 73728, KE_FLG = KE_ST + 1024;
constexpr int RING = 4;
__device__ __forceinline__ void phase_e(char* __restrict__ ws, LAS char* lds, int panel, int j, unsigned tag STAMP_ARG) {
    const int tid = threadIdx.x, w = __builtin_amdgcn_readfirstlane(tid >> 6), lane = tid & 63;
    const int tok0 = panel * 64, g = lane >> 4, i16 = lane & 15, nbs = 3 * w;
    const rsrc_t rw = mk_rsrc(ws + WS_WF + (size_t)(2 + 2 * j) * WSUB * 2, 2 * WSUB * 2);
    const int wv = lane * 16, wsb = nbs * 1024;
    bf16x8 W[RING][3];
#pragma unroll
    for (int s = 0; s < RING; ++s)
#pragma unroll
        for (int b = 0; b < 3; ++b) W[s][b] = bld16<SC1>(rw, wv + b * 1024, wsb + s * 36864);
    f32x4 c0[3], c1[3];
    {
        const rsrc_t rc = mk_rsrc(ws + WS_CS, 2 * (D + F) * 4);
#pragma unroll
        for (int b = 0; b < 3; ++b) {
            c0[b] = bldf4<SC1>(rc, g * 16 + b * 64, (D + j * D + nbs * 16) * 4);
            c1[b] = bldf4<SC1>(rc, g * 16 + b * 64, (D + j * D + nbs * 16 + D + F) * 4);
        }
    }
    __builtin_amdgcn_sched_barrier(0);
    if (w == 11) wait_flags(ws, WS_FX, panel * 8, 8, WS_FX, 0, 0, tag);
    BAR();
    STAMP(ws, 16);
    f32x4 pv[4];
    if (w == 11) {
        const rsrc_t rp = mk_rsrc(ws + WS_PST, L * 64);
#pragma unroll
        for (int k = 0; k < 4; ++k) pv[k] = bldf4<SC1>(rp, lane * 64 + k * 16, tok0 * 64);
    }
    {
        const rsrc_t rx = mk_rsrc(ws + WS_X1 + (size_t)tok0 * D * 2, 64 * D * 2);
        const int vo = (lane >> 3) * 1152 + (((lane & 7) ^ ((lane >> 3) & 6)) * 16);
#pragma unroll
        for (int k = 0; k < 6; ++k) {
            const int idx = 6 * w + k;
            blds16<SC1>(rx, lds + idx * 1024, vo, (idx & 7) * 9216 + (idx >> 3) * 128);
        }
    }
    __builtin_amdgcn_sched_barrier(0);
    LAS f32x4* st = (LAS f32x4*)(lds + KE_ST);
    LAS unsigned* flg = (LAS unsigned*)(lds + KE_FLG);
    if (w == 11) {
        float s1 = 0.f, s2 = 0.f;
#pragma unroll
        for (int k = 0; k < 4; ++k) { s1 += pv[k][0] + pv[k][2]; s2 += pv[k][1] + pv[k][3]; }
        const float mean = s1 * (1.0f / D), var = fmaxf(s2 * (1.0f / D) - mean * mean, 0.f);
        const float mb = rbf(mean), sb = ln_sd(var), al = __builtin_amdgcn_rcpf(sb);
        const float k0 = -2.0f * 0.7978845608028654f * 1.4426950408889634f;
        st[lane] = (f32x4){k0 * al, k0 * 0.044715f * al * al * al, sb, -mb};
        if (lane < 4) flg[lane] = 0u;
    }
    VM0(); LGKM0(); BAR();
    STAMP(ws, 17);
    const int z = i16 & 6;
    const int o0 = i16 * 128 + (g ^ z) * 16, o1 = i16 * 128 + ((4 + g) ^ z) * 16;
    f32x4 acc[3][4];
#pragma unroll
    for (int mt = 0; mt < 4; ++mt) {
        const float nm = st[mt * 16 + i16][3];
#pragma unroll
        for (int b = 0; b < 3; ++b) acc[b][mt] = (c0[b] + c1[b]) * nm;
    }
    bf16x8 A[2][4];
#pragma unroll
    for (int ph = 0; ph < 2; ++ph) {
        const LAS char* ab = lds + ph * KE_HID;
        if (ph == 1) {
            while (__hip_atomic_load(flg + 0, __ATOMIC_RELAXED, __HIP_MEMORY_SCOPE_WORKGROUP) < 4u) __builtin_amdgcn_s_sleep(1);
            __builtin_amdgcn_sched_barrier(0);
            asm volatile("" ::: "memory");
        }
#pragma unroll
        for (int mt = 0; mt < 4; ++mt) A[0][mt] = *(const LAS bf16x8*)(ab + o0 + mt * 2048);
#pragma unroll
        for (int s = 0; s < 18; ++s) {
            if (ph == 1 && (s == 5 || s == 11)) {
                while (__hip_atomic_load(flg + (s == 5 ? 1 : 2), __ATOMIC_RELAXED, __HIP_MEMORY_SCOPE_WORKGROUP) < 4u) __builtin_amdgcn_s_sleep(1);
                __builtin_amdgcn_sched_barrier(0);
                asm volatile("" ::: "memory");
            }
            if (s + 1 < 18) {
#pragma unroll
                for (int mt = 0; mt < 4; ++mt) A[(s + 1) & 1][mt] = *(const LAS bf16x8*)(ab + (((s + 1) & 1) ? o1 : o0) + mt * 2048 + ((s + 1) >> 1) * 8192);
            }
            const int t = ph * 18 + s;
#pragma unroll
            for (int b = 0; b < 3; ++b) {
#pragma unroll
                for (int mt = 0; mt < 4; ++mt) acc[b][mt] = MFMA16(W[t % RING][b], A[s & 1][mt], acc[b][mt]);
                if (t + RING < 36) W[t % RING][b] = bld16<0>(rw, wv + b * 1024, wsb + (t + RING) * 36864);
            }
            __builtin_amdgcn_sched_barrier(0);
        }
        if (ph == 0) {
            STAMP(ws, 18);
#pragma unroll
            for (int mt = 0; mt < 4; ++mt) {
                const f32x4 k = st[mt * 16 + i16];
#pragma unroll
                for (int b = 0; b < 3; ++b) {
                    float o[4];
#pragma unroll
                    for (int e = 0; e < 4; ++e) {
                        const float a = acc[b][mt][e];
                        const float ex = __builtin_amdgcn_exp2f(a * (k[0] + k[1] * (a * a)));
                        o[e] = a * __builtin_amdgcn_rcpf(k[2] + k[2] * ex);
                    }
                    const int nb = nbs + b;
                    *(LAS u32x2*)(lds + KE_HID + (nb >> 2) * 8192 + (mt * 16 + i16) * 128 + (((2 * (nb & 3) + (g >> 1)) ^ z) * 16) + 8 * (g & 1)) =
                        (u32x2){pk_bf16(o[0], o[1]), pk_bf16(o[2], o[3])};
                    acc[b][mt] = (f32x4){0.f, 0.f, 0.f, 0.f};
                }
            }
            LGKM0();
            if (lane == 0) __hip_atomic_fetch_add(flg + (w >> 2), 1u, __ATOMIC_RELAXED, __HIP_MEMORY_SCOPE_WORKGROUP);
            STAMP(ws, 19);
            if (w >= 8) __builtin_amdgcn_s_setprio(2); else if (w >= 4) __builtin_amdgcn_s_setprio(1);
        }
    }
    __builtin_amdgcn_s_setprio(0);
    STAMP(ws, 20);
    const rsrc_t rp = mk_rsrc(ws + WS_PART + (size_t)(panel * 8 + j) * 72 * 1024, 72 * 1024);
#pragma unroll
    for (int b = 0; b < 3; ++b)
#pragma unroll
        for (int m2 = 0; m2 < 2; ++m2) {
            const f32x4 a0 = acc[b][2 * m2], a1 = acc[b][2 * m2 + 1];
            bst16((u32x4){pk_bf16(a0[0], a0[1]), pk_bf16(a0[2], a0[3]), pk_bf16(a1[0], a1[1]), pk_bf16(a1[2], a1[3])}, rp, lane * 16, ((nbs + b) * 2 + m2) * 1024);
        }
    VM0(); BAR();
    publish(ws, WS_FP, panel * 8 + j, tag);
}

__device__ __forceinline__ void phase_f(char* __restrict__ ws, u16* __restrict__ out, int panel, int jj, unsigned tag STAMP_ARG) {
    const int tid = threadIdx.x, w = __builtin_amdgcn_readfirstlane(tid >> 6), lane = tid & 63;
    const int g = lane >> 4, i16 = lane & 15;
    const int pr = jj * 9 + (w < 9 ? w : 0), nb = pr >> 1, m2 = pr & 1;
    u32x2 xa[2], ha[2];
    const int so = ((panel * 64 + m2 * 32) * D + nb * 16) * 2, vo = (i16 * D + 4 * g) * 2;
    if (w < 9) {
    }
    if (w == 11) wait_flags(ws, WS_FP, panel * 8, 8, WS_FX, panel * 8, 8, tag);
    BAR();
    STAMP(ws, 22);
    if (w < 9) {
        const rsrc_t rp = mk_rsrc(ws + WS_PART + (size_t)(panel * 8) * 72 * 1024, 8 * 72 * 1024);
        u32x4 v[8];
#pragma unroll
        for (int k = 0; k < 8; ++k) v[k] = bldu4<SC1>(rp, lane * 16, (k * 72 + pr) * 1024);
        const rsrc_t r1 = mk_rsrc(ws + WS_X1, L * D * 2), rr = mk_rsrc(ws + WS_HR, L * D * 2);
#pragma unroll
        for (int t = 0; t < 2; ++t) { xa[t] = bldu2<SC1>(r1, vo, so + t * 16 * D * 2); ha[t] = bldu2<SC1>(rr, vo, so + t * 16 * D * 2); }
        float s[8] = {};
#pragma unroll
        for (int k = 0; k < 8; ++k)
#pragma unroll
            for (int e = 0; e < 4; ++e) { s[2 * e] += bf_lo(v[k][e]); s[2 * e + 1] += bf_hi(v[k][e]); }
#pragma unroll
        for (int t = 0; t < 2; ++t) {
            float o[4];
#pragma unroll
            for (int k = 0; k < 4; ++k) {
                const float x1 = (k & 1) ? bf_hi(xa[t][k >> 1]) : bf_lo(xa[t][k >> 1]);
                const float h = (k & 1) ? bf_hi(ha[t][k >> 1]) : bf_lo(ha[t][k >> 1]);
                o[k] = h + (x1 + s[4 * t + k]);
            }
            *(u32x2*)((char*)out + so + t * 16 * D * 2 + vo) = (u32x2){pk_bf16(o[0], o[1]), pk_bf16(o[2], o[3])};
        }
    }
}

constexpr int LDS_CTRL = 3 * CV_CH > KE_FLG + 16 ? 3 * CV_CH : KE_FLG + 16;
constexpr int LDS_BYTES = LDS_CTRL + 64;

NOPK __global__ __launch_bounds__(NTHR) void mega_kernel(Args a) {
    extern __shared__ __attribute__((aligned(16))) char smem[];
    LAS char* lds = (LAS char*)smem;
    const int tid = threadIdx.x, w = __builtin_amdgcn_readfirstlane(tid >> 6), lane = tid & 63, b = blockIdx.x;
    STAMP_DECL
    STAMP(a.ws, 0);
    const unsigned tagv = __hip_atomic_load((gu32*)(a.ws + WS_EPOCH), RLX_AGENT);
    const StatRow sr = row_stats_load(a, w, lane);
    __builtin_amdgcn_sched_barrier(0);
    unsigned tag;
    if (b < 192) phase_conv(a, lds, tagv, sr, tag STAMP_PASS);
    else {
        tag = __builtin_amdgcn_readfirstlane(tagv) + 1u;
        if (tid == 0) __hip_atomic_store((gu32*)(a.ws + WS_FA) + b, tag, RLX_AGENT);
        row_stats(a, sr, tag, w, lane);
        STAMP(a.ws, 1);
        const int job = w * 64 + (b - 192);
        if (job < 324) prep_job(a, job, lds + w * 12288, lane, tag);
        BAR();
    }
    STAMP(a.ws, 7);
    const int je = 2 * (b & 3) + ((b >> 3) & 1);
    phase_c(a.ws, lds, (b & 7) * 4 + (b >> 6), (b >> 3) & 7, je, tag STAMP_PASS);
    STAMP(a.ws, 11);
    phase_d(a.ws, lds, (b & 7) * 4 + (b >> 6), (b >> 3) & 7, tag STAMP_PASS);
    STAMP(a.ws, 15);
    phase_e(a.ws, lds, ((b >> 2) & 1) * 16 + (b >> 4), je, tag STAMP_PASS);
    STAMP(a.ws, 21);
    phase_f(a.ws, a.out, b >> 3, b & 7, tag STAMP_PASS);
    if (b == 0 && w == 10) {
        wait_flags(a.ws, WS_FA, 0, 256, WS_FA, 0, 0, tag);
        if (lane == 0) __hip_atomic_store((gu32*)(a.ws + WS_EPOCH), tag, RLX_AGENT);
    }
    STAMP(a.ws, 23);
    STAMP_FLUSH(a.ws);
}

extern "C" void kernel_launch(void* const* d_in, const int* in_sizes, int n_in,
                              void* d_out, int out_size, void* d_ws, size_t ws_size,
                              hipStream_t stream) {
    Args a;
    a.x = (const u16*)d_in[0];
    a.are = (const u16*)d_in[1]; a.aim = (const u16*)d_in[2];
    a.bre = (const u16*)d_in[3]; a.bim = (const u16*)d_in[4];
    a.cre = (const u16*)d_in[5]; a.cim = (const u16*)d_in[6];
    a.logdt = (const u16*)d_in[7]; a.dskip = (const u16*)d_in[8];
    a.w1 = (const u16*)d_in[10]; a.w2 = (const u16*)d_in[11];
    a.w3 = (const u16*)d_in[12]; a.w4 = (const u16*)d_in[13];
    a.ws = (char*)d_ws; a.out = (u16*)d_out;
    (void)hipFuncSetAttribute((const void*)mega_kernel, hipFuncAttributeMaxDynamicSharedMemorySize, LDS_BYTES);
    mega_kernel<<<NWG, NTHR, LDS_BYTES, stream>>>(a);
}
```

```cpp
#include <hip/hip_runtime.h>
#include <hip/hip_bf16.h>
#include <stdint.h>

typedef unsigned short u16;
typedef short bf16x8 __attribute__((ext_vector_type(8)));
typedef short s16x4 __attribute__((ext_vector_type(4)));
typedef float f32x2 __attribute__((ext_vector_type(2)));
typedef float f32x4 __attribute__((ext_vector_type(4)));
typedef float f32x16 __attribute__((ext_vector_type(16)));
typedef unsigned u32x2 __attribute__((ext_vector_type(2)));
typedef unsigned u32x4 __attribute__((ext_vector_type(4)));
typedef __bf16 bf16x2_t __attribute__((ext_vector_type(2)));
typedef _Float16 f16x2 __attribute__((ext_vector_type(2)));
typedef _Float16 f16x8 __attribute__((ext_vector_type(8)));
#define LAS __attribute__((address_space(3)))
#define GAS __attribute__((address_space(1)))
#if defined(__HIP_DEVICE_COMPILE__)
#define NOPK __attribute__((target("no-packed-fp32-ops")))
#else
#define NOPK
#endif

constexpr int L = 2048, D = 576, NS = 128, F = 4608;
constexpr int WSUB = D * D;
constexpr float LN_EPS = 1e-5f;
constexpr int NWG = 256, NTHR = 768;
constexpr int SC1 = 16;

constexpr size_t WS_EPOCH = 0;
constexpr int REP = 8;
constexpr size_t WS_FA = 256;
constexpr size_t WS_FW = WS_FA + 1024;
constexpr size_t WS_FG = WS_FW + 2048;
constexpr size_t WS_FX = WS_FG + 4096;
constexpr size_t WS_FP = WS_FX + 4096;
constexpr size_t WS_FH = WS_FP + 4096;
constexpr size_t WS_GR = 32768;
constexpr size_t WS_HT = WS_GR + (size_t)L * 8;
constexpr size_t WS_WF = WS_HT + (size_t)D * L * 2;
constexpr size_t WS_CS = WS_WF + (size_t)18 * WSUB * 2;
constexpr size_t WS_ST2 = WS_CS + (size_t)2 * (D + F) * 4;
constexpr size_t WS_G = WS_ST2 + (size_t)L * 8;
constexpr size_t WS_X1 = WS_G + (size_t)(L + 4) * D * 2;
constexpr size_t WS_HR = WS_X1 + (size_t)L * D * 2;
constexpr size_t WS_PST = WS_HR + (size_t)L * D * 2;
constexpr size_t WS_PART = WS_PST + (size_t)L * 8 * 8;
constexpr size_t WS_END = WS_PART + (size_t)32 * 8 * 72 * 512 * 2;

#define STAMP(ws, i) do {} while (0)
#define STAMP_DECL
#define STAMP_ARG
#define STAMP_PASS
#define STAMP_FLUSH(ws) do {} while (0)

__device__ __forceinline__ unsigned pk_bf16(float lo, float hi) {
    f32x2 v = {lo, hi};
    return __builtin_bit_cast(unsigned, __builtin_convertvector(v, bf16x2_t));
}
__device__ __forceinline__ float bf_lo(unsigned p) { return __uint_as_float(p << 16); }
__device__ __forceinline__ float bf_hi(unsigned p) { return __uint_as_float(p & 0xffff0000u); }
__device__ __forceinline__ float bf2f(u16 x) { return __uint_as_float((unsigned)x << 16); }
__device__ __forceinline__ float rbf(float x) { return bf_lo(pk_bf16(x, x)); }
__device__ __forceinline__ float dot2bf(unsigned a, unsigned b, float c) {
    return __builtin_amdgcn_fdot2_f32_bf16(__builtin_bit_cast(bf16x2_t, a), __builtin_bit_cast(bf16x2_t, b), c, false);
}
__device__ __forceinline__ float gelu_tanh(float x) {
    const float c0 = -2.0f * 0.7978845608028654f * 1.4426950408889634f;
    const float c1 = c0 * 0.044715f;
    float e = __builtin_amdgcn_exp2f(x * (c0 + c1 * x * x));
    return x * __builtin_amdgcn_rcpf(1.0f + e);
}
__device__ __forceinline__ s16x4 tr_read(const LAS char* p) {
    return __builtin_amdgcn_ds_read_tr16_b64_v4i16((LAS s16x4*)p);
}
__device__ __forceinline__ bf16x8 cat8(s16x4 a, s16x4 b) {
    return __builtin_shufflevector(a, b, 0, 1, 2, 3, 4, 5, 6, 7);
}
__device__ __forceinline__ f32x4 sel4(bool c, f32x4 a, f32x4 b) {
    f32x4 r;
#pragma unroll
    for (int k = 0; k < 4; ++k) { float x = a[k], y = b[k]; asm volatile("" : "+v"(x), "+v"(y)); r[k] = c ? x : y; }
    return r;
}
__device__ __forceinline__ float rsum_g(float x) {
    const unsigned u = __float_as_uint(x);
    const auto a = __builtin_amdgcn_permlane16_swap(u, u, false, false);
    const float y = __uint_as_float(a[0]) + __uint_as_float(a[1]);
    const unsigned v = __float_as_uint(y);
    const auto b = __builtin_amdgcn_permlane32_swap(v, v, false, false);
    return __uint_as_float(b[0]) + __uint_as_float(b[1]);
}
template <int CTRL> __device__ __forceinline__ float dpp_f(float x) {
    return __uint_as_float(__builtin_amdgcn_update_dpp(0u, __float_as_uint(x), CTRL, 0xf, 0xf, false));
}
__device__ __forceinline__ float rsum_64(float x) {
    x += dpp_f<0xB1>(x);
    x += dpp_f<0x4E>(x);
    x += dpp_f<0x141>(x);
    x += dpp_f<0x140>(x);
    return rsum_g(x);
}
template <int CTRL> __device__ __forceinline__ unsigned dpp_u(unsigned x) { return __builtin_amdgcn_update_dpp(0u, x, CTRL, 0xf, 0xf, false); }
__device__ __forceinline__ unsigned umax2(unsigned a, unsigned b) { return a > b ? a : b; }
__device__ __forceinline__ float rmax_64(float xf) {
    unsigned x = __float_as_uint(xf);
    x = umax2(x, dpp_u<0xB1>(x)); x = umax2(x, dpp_u<0x4E>(x)); x = umax2(x, dpp_u<0x141>(x)); x = umax2(x, dpp_u<0x140>(x));
    const auto a = __builtin_amdgcn_permlane16_swap(x, x, false, false); x = umax2(a[0], a[1]);
    const auto b = __builtin_amdgcn_permlane32_swap(x, x, false, false); x = umax2(b[0], b[1]);
    return __uint_as_float(x);
}
__device__ __forceinline__ float ln_sd(float var) { return rbf(__builtin_amdgcn_sqrtf(rbf(rbf(var) + LN_EPS))); }
__device__ __forceinline__ int fsw(int row) { return ((row >> 1) & 1) | (((row >> 3) & 1) << 1); }
#define MFMA16(a, b, c) __builtin_amdgcn_mfma_f32_16x16x32_bf16((a), (b), (c), 0, 0, 0)
#define MFMA32(a, b, c) __builtin_amdgcn_mfma_f32_32x32x16_bf16((a), (b), (c), 0, 0, 0)
#define VM0() asm volatile("s_waitcnt vmcnt(0)" ::: "memory")
#define LGKM0() asm volatile("s_waitcnt lgkmcnt(0)" ::: "memory")
#define BAR() do { asm volatile("" ::: "memory"); __builtin_amdgcn_s_barrier(); asm volatile("" ::: "memory"); } while (0)

typedef __amdgpu_buffer_rsrc_t rsrc_t;
__device__ __forceinline__ rsrc_t mk_rsrc(const void* p, unsigned bytes) { return __builtin_amdgcn_make_buffer_rsrc((void*)p, 0, bytes, 0x00020000); }
template <int AUX> __device__ __forceinline__ bf16x8 bld16(rsrc_t r, int voff, int soff) {
    return __builtin_bit_cast(bf16x8, __builtin_amdgcn_raw_buffer_load_b128(r, voff, soff, AUX));
}
template <int AUX> __device__ __forceinline__ f32x4 bldf4(rsrc_t r, int voff, int soff) {
    return __builtin_bit_cast(f32x4, __builtin_amdgcn_raw_buffer_load_b128(r, voff, soff, AUX));
}
template <int AUX> __device__ __forceinline__ u32x4 bldu4(rsrc_t r, int voff, int soff) {
    return __builtin_amdgcn_raw_buffer_load_b128(r, voff, soff, AUX);
}
template <int AUX> __device__ __forceinline__ u32x2 bldu2(rsrc_t r, int voff, int soff) {
    return __builtin_amdgcn_raw_buffer_load_b64(r, voff, soff, AUX);
}
template <int AUX> __device__ __forceinline__ void blds16(rsrc_t r, LAS char* l, int voff, int soff) {
    __builtin_amdgcn_raw_ptr_buffer_load_lds(r, (LAS void*)l, 16, voff, soff, 0, AUX);
}
__device__ __forceinline__ void bst16(u32x4 v, rsrc_t r, int voff, int soff) { __builtin_amdgcn_raw_buffer_store_b128(v, r, voff, soff, SC1); }
__device__ __forceinline__ void bst8(u32x2 v, rsrc_t r, int voff, int soff) { __builtin_amdgcn_raw_buffer_store_b64(v, r, voff, soff, SC1); }
__device__ __forceinline__ void bst4(unsigned v, rsrc_t r, int voff, int soff) { __builtin_amdgcn_raw_buffer_store_b32(v, r, voff, soff, SC1); }

struct Args {
    const u16* x; const u16 *are, *aim, *bre, *bim, *cre, *cim, *logdt, *dskip;
    const u16 *w1, *w2, *w3, *w4;
    char* ws; u16* out;
};

typedef GAS unsigned gu32;
typedef GAS unsigned long long gu64;
#define RLX_AGENT __ATOMIC_RELAXED, __HIP_MEMORY_SCOPE_AGENT
#define PREP_DELAY 100ull
constexpr unsigned long long WAIT_TICKS = 400000ull;
__device__ __forceinline__ void publish(char* ws, size_t flag_off, int idx, unsigned tag) {
    if (threadIdx.x == 0) __hip_atomic_store((gu32*)(ws + flag_off) + idx, tag, RLX_AGENT);
}
__device__ __forceinline__ void publish_rep(char* ws, size_t flag_off, int idx, unsigned tag, int lane) {
    if (lane < REP) __hip_atomic_store((gu32*)(ws + flag_off) + lane * 256 + idx, tag, RLX_AGENT);
}
__device__ __forceinline__ void wait_flags(char* ws, size_t offa, int a0, int na, size_t offb, int b0, int nb, unsigned tag) {
    const int lane = threadIdx.x & 63;
    const gu32* p[4]; bool on[4];
#pragma unroll
    for (int k = 0; k < 4; ++k) {
        const int i = lane + 64 * k;
        on[k] = i < na + nb;
        p[k] = i < na ? (const gu32*)(ws + offa) + a0 + i : (const gu32*)(ws + offb) + b0 + (on[k] ? i - na : 0);
    }
    const unsigned long long t0 = __builtin_amdgcn_s_memrealtime();
    for (;;) {
        bool ok = true;
#pragma unroll
        for (int k = 0; k < 4; ++k)
            if (64 * k < na + nb) { const unsigned v = __hip_atomic_load(p[k], RLX_AGENT); ok = ok && (!on[k] || v == tag); }
        if (__all(ok)) break;
        if (__builtin_amdgcn_s_memrealtime() - t0 > WAIT_TICKS) break;
        __builtin_amdgcn_s_sleep(2);
    }
}

__device__ __forceinline__ void wait_panels(char* ws, size_t o0, int p0, size_t o1, int p1, size_t o2, int p2, unsigned tag) {
    const int lane = threadIdx.x & 63, k = (lane >> 3) % 3;
    const gu32* p = (const gu32*)(ws + (k == 0 ? o0 : k == 1 ? o1 : o2)) + (k == 0 ? p0 : k == 1 ? p1 : p2) * 32 + (lane & 7);
    const unsigned long long t0 = __builtin_amdgcn_s_memrealtime();
    for (;;) {
        if (__all(__hip_atomic_load(p, RLX_AGENT) == tag)) break;
        if (__builtin_amdgcn_s_memrealtime() - t0 > WAIT_TICKS) break;
        __builtin_amdgcn_s_sleep(2);
    }
}
struct StatRow { u32x4 v0, v1; };
__device__ __forceinline__ StatRow row_stats_load(const Args& a, int w, int lane) {
    const int row = blockIdx.x * 8 + (w & 7);
    const rsrc_t rx = mk_rsrc(a.x + (size_t)row * D, w < 8 ? D * 2 : 0);
    StatRow r;
    r.v0 = bldu4<0>(rx, lane * 16, 0); r.v1 = bldu4<0>(rx, 1024 + lane * 16, 0);
    return r;
}
__device__ __forceinline__ void row_stats(const Args& a, const StatRow& r, unsigned tag, int w, int lane) {
    if (w < 8) {
        float s1 = 0.f, s2 = 0.f;
#pragma unroll
        for (int k = 0; k < 4; ++k) {
            s1 = dot2bf(r.v0[k], 0x3f803f80u, s1); s2 = dot2bf(r.v0[k], r.v0[k], s2);
            s1 = dot2bf(r.v1[k], 0x3f803f80u, s1); s2 = dot2bf(r.v1[k], r.v1[k], s2);
        }
        s1 = rsum_64(s1); s2 = rsum_64(s2);
        const float mean = s1 * (1.0f / D), var = fmaxf(s2 * (1.0f / D) - mean * mean, 0.f);
        const unsigned val = (pk_bf16(mean, ln_sd(var)));
        if (lane == 0) __hip_atomic_store((gu64*)(a.ws + WS_GR) + blockIdx.x * 8 + w, ((unsigned long long)tag << 32) | val, RLX_AGENT);
    }
}

struct PrepRegs { u32x4 R[36]; };
__device__ __forceinline__ void prep_issue(const Args& a, int job, int lane, PrepRegs& P) {
    const int sub = job / 18, rem = job % 18, kh = rem / 9, ns = rem % 9;
    const u16* src; int ld;
    if (sub == 0) { src = a.w1; ld = D; }
    else if (sub == 1) { src = a.w2; ld = D; }
    else if ((sub & 1) == 0) { src = a.w3 + ((sub - 2) >> 1) * D; ld = F; }
    else { src = a.w4 + (size_t)((sub - 2) >> 1) * D * D; ld = D; }
    src += (size_t)(kh * 288) * ld + ns * 64;
    const rsrc_t rs = mk_rsrc(src, 288 * ld * 2);
    const int ldb = ld * 2;
    const int vo = (lane >> 3) * ldb + (lane & 7) * 16;
#pragma unroll
    for (int i = 0; i < 36; ++i) P.R[i] = bldu4<0>(rs, vo, i * 8 * ldb);
    __builtin_amdgcn_sched_barrier(0);
}
__device__ __forceinline__ void prep_finish(const Args& a, int job, LAS char* lds, int lane, unsigned tag, const PrepRegs& P STAMP_ARG) {
    const int sub = job / 18, rem = job % 18, kh = rem / 9, ns = rem % 9;
    const rsrc_t rd = mk_rsrc(a.ws + WS_WF + (size_t)sub * WSUB * 2, WSUB * 2);
    const int fl = (lane >> 4) & 1;
    const int w0 = (lane >> 3) * 128 + (((lane & 7) ^ (fl << 1)) * 16), w1 = (lane >> 3) * 128 + (((lane & 7) ^ ((fl | 2) << 1)) * 16);
    const int g = lane >> 4, i16 = lane & 15, q = i16 >> 2, p = lane & 3;
    const int f = ((q >> 1) & 1) | ((g & 1) << 1);
    int rb[4];
#pragma unroll
    for (int n = 0; n < 4; ++n) rb[n] = (8 * g + q) * 128 + (((2 * n + (p >> 1)) ^ (f << 1)) * 16) + 8 * (p & 1);
    float cs[4] = {0.f, 0.f, 0.f, 0.f};
#pragma unroll
    for (int u = 0; u < 9; ++u) {
        LAS char* sl = lds + (u & 1) * 4096;
#pragma unroll
        for (int i = 0; i < 4; ++i) *(LAS u32x4*)(sl + i * 1024 + ((i & 1) ? w1 : w0)) = P.R[4 * u + i];
        LGKM0();
        if (u == 0) STAMP(a.ws, 30);
        if (u == 8) STAMP(a.ws, 31);
        __builtin_amdgcn_sched_barrier(0);
#pragma unroll
        for (int n = 0; n < 4; ++n) {
            const u32x4 x = __builtin_bit_cast(u32x4, cat8(tr_read(sl + rb[n]), tr_read(sl + rb[n] + 512)));
            bst16(x, rd, lane * 16, ((kh * 9 + u) * 36 + ns * 4 + n) * 1024);
#pragma unroll
            for (int k = 0; k < 4; ++k) cs[n] = dot2bf(x[k], 0x3f803f80u, cs[n]);
        }
        __builtin_amdgcn_sched_barrier(0);
    }
    if ((sub & 1) == 0) {
        const rsrc_t rc = mk_rsrc(a.ws + WS_CS, 2 * (D + F) * 4);
#pragma unroll
        for (int n = 0; n < 4; ++n) {
            const float c = rsum_g(cs[n]);
            if (lane < 16) bst4(__float_as_uint(c), rc, lane * 4, (kh * (D + F) + (sub == 0 ? 0 : D + ((sub - 2) >> 1) * D) + ns * 64 + n * 16) * 4);
        }
    }
    STAMP(a.ws, 32);
    VM0();
    STAMP(a.ws, 33);
    if (lane == 0) __hip_atomic_store((gu32*)(a.ws + WS_FW) + job, tag, RLX_AGENT);
}

constexpr int CV_RC = 4160;
constexpr int CV_U = 4 * CV_RC;
constexpr int CV_X = CV_U + 2288 * 2;
constexpr int CV_RED = CV_X + 4096;
constexpr int CV_CH = CV_RED + 24576;
constexpr int KG_ROW = 528;
constexpr int KG_XI = 32 * KG_ROW, KG_T = CV_RED + 4096;

struct cpx { float re, im; };
__device__ __forceinline__ cpx cmul(cpx a, cpx b) { return {a.re * b.re - a.im * b.im, a.re * b.im + a.im * b.re}; }
__device__ __forceinline__ cpx cexp_rev(float a2, float rev, float m) {
    const float e = __builtin_amdgcn_exp2f(a2 * m), p = __builtin_amdgcn_fractf(rev * m);
    return {e * __builtin_amdgcn_cosf(p), e * __builtin_amdgcn_sinf(p)};
}
__device__ __forceinline__ unsigned pk_f16(cpx c) { f32x2 v = {c.re, c.im}; return __builtin_bit_cast(unsigned, __builtin_convertvector(v, f16x2)); }

__device__ __forceinline__ void conv_put(LAS char* lds, int m, u32x4 A, u32x2 B) {
    u32x4 c1 = {__builtin_amdgcn_alignbit(A[1], A[0], 16), __builtin_amdgcn_alignbit(A[2], A[1], 16), __builtin_amdgcn_alignbit(A[3], A[2], 16), __builtin_amdgcn_alignbit(B[0], A[3], 16)};
    u32x4 c2 = {A[1], A[2], A[3], B[0]};
    u32x4 c3 = {c1[1], c1[2], c1[3], __builtin_amdgcn_alignbit(B[1], B[0], 16)};
    *(LAS u32x4*)(lds + 0 * CV_RC + m * 16) = A;
    *(LAS u32x4*)(lds + 1 * CV_RC + m * 16) = c1;
    *(LAS u32x4*)(lds + 2 * CV_RC + m * 16) = c2;
    *(LAS u32x4*)(lds + 3 * CV_RC + m * 16) = c3;
}

__device__ __forceinline__ void phase_conv(const Args& a, LAS char* lds0, unsigned tagv, const StatRow& sr, unsigned& tag_out STAMP_ARG) {
    const int tid = threadIdx.x, w = __builtin_amdgcn_readfirstlane(tid >> 6), lane = tid & 63;
    const int b = blockIdx.x, chl = w >> 2, wq = w & 3, t8 = tid & 255, ch = 3 * b + chl;
    LAS char* lds = lds0 + chl * CV_CH;
    const int part = wq >> 1, n = (wq & 1) * 64 + lane;
    const int o = ch * NS + n, o2 = ch * NS + (n ^ 64);
    const u16 r_ld = a.logdt[ch], r_ar = a.are[o], r_ai = a.aim[o], r_ar2 = a.are[o2];
    u16 r_b[4] = {}, r_c[4] = {};
    if (part) { r_b[0] = a.bre[o]; r_b[1] = a.bim[o]; r_c[0] = a.cre[o]; r_c[1] = a.cim[o]; r_b[2] = a.bre[o2]; r_b[3] = a.bim[o2]; r_c[2] = a.cre[o2]; r_c[3] = a.cim[o2]; }
    const u16 r_ds = a.dskip[ch];
    __builtin_amdgcn_sched_barrier(0);
    const unsigned tag = __builtin_amdgcn_readfirstlane(tagv) + 1u;
    tag_out = tag;
    if (tid == 0) __hip_atomic_store((gu32*)(a.ws + WS_FA) + b, tag, RLX_AGENT);
    row_stats(a, sr, tag, w, lane);
    STAMP(a.ws, 1);
    __builtin_amdgcn_sched_barrier(0);
    u32x2 xr[4];
    const rsrc_t rx = mk_rsrc(a.x, L * D * 2);
    const int xvo = tid * (2 * D * 2) + ((6 * b) & ~3);
#define GATHER(i) do { __builtin_amdgcn_sched_barrier(0); if ((i) < 2 || w < 4) xr[i] = bldu2<0>(rx, xvo + ((i) & 1) * D * 2, ((i) >> 1) * 1536 * D * 2); __builtin_amdgcn_sched_barrier(0); } while (0)
    GATHER(0);
    const float dt = rbf(expf(bf2f(r_ld)));
    const float are = dt * bf2f(r_ar);
    const float ap = rmax_64(fmaxf(fmaxf(are, dt * bf2f(r_ar2)), 0.f));
    const float a2 = (are - ap) * 1.4426950408889634f, rev = dt * bf2f(r_ai) * 0.15915494309189535f;
    float kscale = 1.f;
    if (part == 0) {
        cpx e[4];
        const cpx g8 = cexp_rev(a2, rev, 8.f), g32 = cexp_rev(a2, rev, 32.f);
        e[0] = {1.f, 0.f}; e[1] = g8; e[2] = cmul(g8, g8); e[3] = cmul(e[2], g8);
        LAS char* er = lds + n * 4;
#pragma unroll
        for (int t = 0; t < 8; ++t) {
            if (t == 0) GATHER(1);
            if (t == 3) GATHER(2);
            if (t == 6) GATHER(3);
#pragma unroll
            for (int c = 0; c < 4; ++c) {
                *(LAS unsigned*)(er + (4 * t + c) * KG_ROW) = pk_f16(e[c]);
                if (t < 7) e[c] = (t == 0 && c == 0) ? g32 : cmul(e[c], g32);
            }
        }
    } else {
        const float b0 = dt * bf2f(r_b[0]), b1 = dt * bf2f(r_b[1]), c0 = bf2f(r_c[0]), c1 = bf2f(r_c[1]);
        const float b2 = dt * bf2f(r_b[2]), b3 = dt * bf2f(r_b[3]), c2 = bf2f(r_c[2]), c3 = bf2f(r_c[3]);
        const float wr = c0 * b0 - c1 * b1, wi = c0 * b1 + c1 * b0, vr = c2 * b2 - c3 * b3, vi = c2 * b3 + c3 * b2;
        const float wm = rmax_64(fmaxf(fmaxf(fabsf(wr), fabsf(wi)), fmaxf(fabsf(vr), fabsf(vi))));
        const unsigned ex = (__float_as_uint(wm) >> 23) & 0xffu;
        const unsigned exc = ex < 2u ? 2u : (ex > 252u ? 252u : ex);
        const float sc = __uint_as_float((254u - exc) << 23);
        kscale = __uint_as_float(exc << 23);
        const cpx W = {wr * sc, -wi * sc};
        const cpx g1 = cexp_rev(a2, -rev, 1.f), p256 = cexp_rev(a2, -rev, 256.f);
        const cpx g2 = cmul(g1, g1), g4 = cmul(g2, g2), p512 = cmul(p256, p256);
        LAS char* xr_ = lds + KG_XI + n * 4;
#define XST(i, v) *(LAS unsigned*)(xr_ + (i) * KG_ROW) = pk_f16(v)
        GATHER(1);
        cpx x = W; XST(0, x);
#pragma unroll
        for (int i = 1; i < 8; ++i) { x = cmul(x, g1); XST(i, x); }
        GATHER(2);
        x = cmul(W, p256); XST(8, x);
#pragma unroll
        for (int i = 9; i < 12; ++i) { x = cmul(x, g2); XST(i, x); }
        cpx y = cmul(W, p512); XST(12, y); XST(13, cmul(y, g4));
        x = cmul(y, p256); XST(14, x); XST(15, cmul(x, g4));
        GATHER(3);
        y = cmul(y, p512); XST(16, y);
#pragma unroll
        for (int i = 17; i < 21; ++i) { y = cmul(y, p256); XST(i, y); }
#undef XST
#undef GATHER
        if (lane == 0 && wq == 2) *(LAS float*)(lds + KG_T - 16) = kscale;
    }
    const int p = lane & 15, g = lane >> 4;
    LAS float* T = (LAS float*)(lds + KG_T);
    int base, idx0, istr;
    if (wq & 1) {
        base = p < 4 ? 1024 + 256 * p : (p == 4 ? 2048 : 0);
        idx0 = p < 4 ? 512 + 32 * p : (p == 4 ? 640 : 680);
        istr = 1;
    } else {
        const int q4 = p & 3;
        base = p < 8 ? p : (p < 12 ? 256 + 2 * q4 : 512 + 4 * (p & 1) + 128 * (p & 2));
        idx0 = p < 8 ? p : (p < 12 ? 256 + q4 : 384 + (p & 1) + 32 * (p & 2));
        istr = p < 8 ? 8 : (p < 12 ? 4 : 2);
    }
    const int j0 = (wq >> 1) * 16 + 4 * g;
    LAS float* tw = T + idx0 + j0 * istr;
    const LAS char* ea = lds + ((wq >> 1) * 16 + p) * KG_ROW + g * 16;
    const LAS char* xa = lds + KG_XI + ((wq & 1) * 16 + p) * KG_ROW + g * 16;
    STAMP(a.ws, 29);
    LGKM0(); BAR();
    STAMP(a.ws, 2);
    {
        f32x4 acc = {};
        f16x8 A[8], B[8];
#pragma unroll
        for (int t = 0; t < 8; ++t) { A[t] = *(const LAS f16x8*)(ea + t * 64); B[t] = *(const LAS f16x8*)(xa + t * 64); }
        float ks = *(const LAS float*)(lds + KG_T - 16);
        __builtin_amdgcn_sched_barrier(0);
#pragma unroll
        for (int t = 0; t < 8; ++t) acc = __builtin_amdgcn_mfma_f32_16x16x32_f16(A[t], B[t], acc, 0, 0, 0);
        if (ap > 0.f) {
#pragma unroll
            for (int q = 0; q < 4; ++q) acc[q] *= __builtin_amdgcn_exp2f(ap * 1.4426950408889634f * (float)(base + 8 * (j0 + q)));
        }
#pragma unroll
        for (int q = 0; q < 4; ++q) tw[q * istr] = acc[q] * ks;
    }
    LGKM0(); BAR();
    STAMP(a.ws, 3);
    {
        float v[12];
        const float lf = (float)(2047 - t8 * 8);
        const unsigned tb = (unsigned)(size_t)T - 17024u * 4u;
        if (wq < 3) {
#pragma unroll
            for (int e = 0; e < 12; e += 2) {
                const unsigned bits = pk_bf16(lf - e, lf - (e + 1));
                v[e] = *(const LAS float*)(size_t)(tb + (bits & 0xffffu) * 4u);
                v[e + 1] = *(const LAS float*)(size_t)(tb + (bits >> 16) * 4u);
            }
        } else {
#pragma unroll
            for (int e = 0; e < 12; ++e) {
                const int l = 2047 - (t8 * 8 + e);
                const int idx = l < 128 ? (l < 0 ? 0 : l) : (int)(pk_bf16((float)l, 0.f) & 0xffffu) - 17024;
                const float t = T[idx];
                v[e] = l < 0 ? 0.f : t;
            }
        }
        conv_put(lds, t8, (u32x4){pk_bf16(v[0], v[1]), pk_bf16(v[2], v[3]), pk_bf16(v[4], v[5]), pk_bf16(v[6], v[7])}, (u32x2){pk_bf16(v[8], v[9]), pk_bf16(v[10], v[11])});
        if (t8 < 4) conv_put(lds, 256 + t8, (u32x4){0u, 0u, 0u, 0u}, (u32x2){0u, 0u});
        if (t8 >= 64 && t8 < 94) *(LAS u32x4*)(lds + CV_U + (t8 - 64) * 16) = (u32x4){0u, 0u, 0u, 0u};
    }
    STAMP(a.ws, 4);
    {
        u32x4 gv[2];
        const rsrc_t rgn = mk_rsrc(a.ws + WS_GR, L * 8);
        const unsigned long long t0 = __builtin_amdgcn_s_memrealtime();
        for (;;) {
            gv[0] = bldu4<SC1>(rgn, tid * 16, 0);
            gv[1] = w < 4 ? bldu4<SC1>(rgn, tid * 16, 1536 * 8) : gv[0];
            const bool ok = gv[0][1] == tag && gv[0][3] == tag && gv[1][1] == tag && gv[1][3] == tag;
            if (__all(ok)) break;
            if (__builtin_amdgcn_s_memrealtime() - t0 > WAIT_TICKS) break;
            __builtin_amdgcn_s_sleep(2);
            asm volatile("" ::: "memory");
        }
#pragma unroll
        for (int i = 0; i < 2; ++i) {
            if (i == 0 || w < 4) {
                const int row = 2 * tid + 1536 * i;
                u32x2 x0 = xr[2 * i], x1 = xr[2 * i + 1];
                if (b & 1) {
                    x0 = (u32x2){__builtin_amdgcn_alignbit(x0[1], x0[0], 16), x0[1] >> 16};
                    x1 = (u32x2){__builtin_amdgcn_alignbit(x1[1], x1[0], 16), x1[1] >> 16};
                }
                const float m0 = bf_lo(gv[i][0]), m1 = bf_lo(gv[i][2]);
                const float s0 = __builtin_amdgcn_rcpf(bf_hi(gv[i][0])), s1 = __builtin_amdgcn_rcpf(bf_hi(gv[i][2]));
#pragma unroll
                for (int c = 0; c < 3; ++c) {
                    const unsigned xp = c == 0 ? __builtin_amdgcn_perm(x1[0], x0[0], 0x05040100u) : c == 1 ? __builtin_amdgcn_perm(x1[0], x0[0], 0x07060302u)
                                                                                                            : __builtin_amdgcn_perm(x1[1], x0[1], 0x05040100u);
                    const unsigned dd = pk_bf16(bf_lo(xp) - m0, bf_hi(xp) - m1);
                    *(LAS unsigned*)(lds0 + c * CV_CH + CV_U + 480 + row * 2) = pk_bf16(bf_lo(dd) * s0, bf_hi(dd) * s1);
                    *(LAS unsigned*)(lds0 + c * CV_CH + CV_X + row * 2) = xp;
                }
            }
        }
    }
    LGKM0(); BAR();
    STAMP(a.ws, 5);
    f32x4 acc[8];
#pragma unroll
    for (int c = 0; c < 8; ++c) acc[c] = (f32x4){0.f, 0.f, 0.f, 0.f};
    const int cpy = (3 - p) & 3;
#pragma unroll
    for (int rr = 0; rr < 2; ++rr) {
        const int r = 2 * wq + rr;
        const LAS char* ab = lds + cpy * CV_RC + 2 * (2031 - 32 * r + 8 * g - p - cpy) - 512 * 7;
        const LAS char* fb = lds + CV_U + 2 * (240 - 32 * r + 16 * p - 16 + 8 * g);
        bf16x8 A[8], Fm[8];
#pragma unroll
        for (int s = 0; s < 8; ++s) {
            const s16x4 lo = *(const volatile LAS s16x4*)(ab + 512 * (7 - s));
            const s16x4 hi = *(const volatile LAS s16x4*)(ab + 512 * (7 - s) + 8);
            A[s] = cat8(lo, hi);
        }
#pragma unroll
        for (int e = 0; e < 8; ++e) Fm[e] = *(const LAS bf16x8*)(fb + 512 * e);
#pragma unroll
        for (int c = 0; c < 8; ++c)
#pragma unroll
            for (int s = 0; s <= c; ++s) acc[c] = MFMA16(A[s], Fm[c - s], acc[c]);
    }
    STAMP(a.ws, 6);
    LAS f32x4* red = (LAS f32x4*)(lds + CV_RED);
#pragma unroll
    for (int c = 0; c < 8; ++c) {
        const int ow = c >> 1;
        if (ow != wq) red[(ow * 6 + (c & 1) * 3 + (wq > ow ? wq - 1 : wq)) * 64 + lane] = acc[c];
    }
    LGKM0(); BAR();
    const rsrc_t rh = mk_rsrc(a.ws + WS_HT + (size_t)ch * L * 2, L * 2);
    const float dsk = bf2f(r_ds);
#pragma unroll
    for (int i = 0; i < 2; ++i) {
        f32x4 y;
#pragma unroll
        for (int k = 0; k < 4; ++k) {
            const float y01 = (wq & 1) ? acc[2 + i][k] : acc[i][k], y23 = (wq & 1) ? acc[6 + i][k] : acc[4 + i][k];
            y[k] = (wq & 2) ? y23 : y01;
        }
#pragma unroll
        for (int k = 0; k < 3; ++k) y += red[(wq * 6 + i * 3 + k) * 64 + lane];
        const int l0 = 256 * (2 * wq + i) + 16 * p + 4 * g;
        const u32x2 uu = *(const LAS u32x2*)(lds + CV_U + 480 + 2 * l0), xv = *(const LAS u32x2*)(lds + CV_X + 2 * l0);
        float o[4];
#pragma unroll
        for (int k = 0; k < 4; ++k) {
            const float u = (k & 1) ? bf_hi(uu[k >> 1]) : bf_lo(uu[k >> 1]);
            const float x = (k & 1) ? bf_hi(xv[k >> 1]) : bf_lo(xv[k >> 1]);
            o[k] = x + __builtin_fmaf(u, dsk, y[k]);
        }
        bst8((u32x2){pk_bf16(o[0], o[1]), pk_bf16(o[2], o[3])}, rh, 2 * l0, 0);
    }
    STAMP(a.ws, 28);
    VM0(); BAR();
    if (w == 0) publish_rep(a.ws, WS_FH, b, tag, lane);
}

__device__ __forceinline__ int nb0_of(int j) { return (9 * j + 1) >> 1; }

constexpr int KC_XB = 73728, KC_ST = KC_XB + 20 * 1024;
__device__ __forceinline__ void phase_c(char* __restrict__ ws, LAS char* lds, int panel, int j, unsigned tag STAMP_ARG) {
    const int tid = threadIdx.x, w = __builtin_amdgcn_readfirstlane(tid >> 6), lane = tid & 63;
    const int tok0 = panel * 64, nbs = nb0_of(j), nblk = nb0_of(j + 1) - nbs;
    const int g = lane >> 4, i16 = lane & 15, q = i16 >> 2, p = lane & 3;
    const int b = w >> 1, kh = w & 1;
    const bool act = w < 10 && b < nblk;
    const int nb = nbs + (act ? b : 0);
    if (w == 0) wait_flags(ws, WS_FW, 0, 36, WS_FW, 0, 0, tag);
    BAR();
    bf16x8 W[9];
    f32x4 cs0, cs1;
    if (w < 10) {
        const rsrc_t rw = mk_rsrc(ws + WS_WF, WSUB * 2);
#pragma unroll
        for (int i = 0; i < 9; ++i) W[i] = bld16<SC1>(rw, lane * 16, ((kh * 9 + i) * 36 + nb) * 1024);
        const rsrc_t rc = mk_rsrc(ws + WS_CS, 2 * (D + F) * 4);
        cs0 = bldf4<SC1>(rc, g * 16, nb * 64); cs1 = bldf4<SC1>(rc, g * 16, nb * 64 + (D + F) * 4);
    }
    if (w == 11) wait_flags(ws, WS_FH, (blockIdx.x & (REP - 1)) * 256, 192, WS_FH, 0, 0, tag);
    BAR();
    STAMP(ws, 8);
    {
        const rsrc_t rh = mk_rsrc(ws + WS_HT, D * L * 2);
        const int fl = (lane >> 4) & 1;
        const int v0 = (lane >> 3) * 4096 + (((lane & 7) ^ (fl << 1)) * 16), v1 = (lane >> 3) * 4096 + (((lane & 7) ^ ((fl | 2) << 1)) * 16);
#pragma unroll
        for (int k = 0; k < 6; ++k) { const int idx = 6 * w + k; blds16<SC1>(rh, lds + idx * 1024, (k & 1) ? v1 : v0, idx * 32768 + tok0 * 2); }
    }
    VM0(); BAR();
    STAMP(ws, 9);
    const int f = ((q >> 1) & 1) | ((g & 1) << 1);
    const int lo_ = (8 * g + q) * 128 + 8 * (p & 1);
    f32x4 acc[4] = {};
    LAS f32x2* st = (LAS f32x2*)(lds + KC_ST);
    LAS f32x4* xb = (LAS f32x4*)(lds + KC_XB);
    if (w < 10) {
        const LAS char* hb[4];
#pragma unroll
        for (int mt = 0; mt < 4; ++mt) hb[mt] = lds + kh * 9 * 4096 + lo_ + (((2 * mt + (p >> 1)) ^ (f << 1)) * 16);
        bf16x8 H[2][2][4];
#pragma unroll
        for (int c = 0; c < 6; ++c) {
            if (c < 5) {
#pragma unroll
                for (int i = 0; i < 2; ++i)
                    if (2 * c + i < 9) {
#pragma unroll
                        for (int mt = 0; mt < 4; ++mt) H[c & 1][i][mt] = cat8(tr_read(hb[mt] + (2 * c + i) * 4096), tr_read(hb[mt] + (2 * c + i) * 4096 + 512));
                    }
            }
            __builtin_amdgcn_sched_barrier(0);
            if (c > 0) {
#pragma unroll
                for (int i = 0; i < 2; ++i)
                    if (2 * (c - 1) + i < 9) {
#pragma unroll
                        for (int mt = 0; mt < 4; ++mt) acc[mt] = MFMA16(W[2 * (c - 1) + i], H[(c - 1) & 1][i][mt], acc[mt]);
                    }
            }
            __builtin_amdgcn_sched_barrier(0);
        }
        xb[((w * 2 + 0) * 64) + lane] = sel4(kh, acc[0], acc[2]);
        xb[((w * 2 + 1) * 64) + lane] = sel4(kh, acc[1], acc[3]);
    } else {
        __builtin_amdgcn_s_setprio(3);
        const bf16x8 ones = {0x3f80, 0x3f80, 0x3f80, 0x3f80, 0x3f80, 0x3f80, 0x3f80, 0x3f80};
        const int m0 = 2 * (w - 10);
        const LAS char* h0 = lds + lo_ + (((2 * m0 + (p >> 1)) ^ (f << 1)) * 16);
        const LAS char* h1 = lds + lo_ + (((2 * m0 + 2 + (p >> 1)) ^ (f << 1)) * 16);
        bf16x8 H[2][3][2];
#pragma unroll
        for (int c = 0; c < 7; ++c) {
            if (c < 6) {
#pragma unroll
                for (int i = 0; i < 3; ++i) {
                    H[c & 1][i][0] = cat8(tr_read(h0 + (3 * c + i) * 4096), tr_read(h0 + (3 * c + i) * 4096 + 512));
                    H[c & 1][i][1] = cat8(tr_read(h1 + (3 * c + i) * 4096), tr_read(h1 + (3 * c + i) * 4096 + 512));
                }
            }
            __builtin_amdgcn_sched_barrier(0);
            if (c > 0) {
#pragma unroll
                for (int i = 0; i < 3; ++i) {
                    const bf16x8 H0 = H[(c - 1) & 1][i][0], H1 = H[(c - 1) & 1][i][1];
                    acc[0] = MFMA16(ones, H0, acc[0]); acc[1] = MFMA16(H0, H0, acc[1]);
                    acc[2] = MFMA16(ones, H1, acc[2]); acc[3] = MFMA16(H1, H1, acc[3]);
                }
            }
            __builtin_amdgcn_sched_barrier(0);
        }
        if (g == q) {
            const rsrc_t r2 = mk_rsrc(ws + WS_ST2, L * 8);
#pragma unroll
            for (int t = 0; t < 2; ++t) {
                const f32x4 d = acc[2 * t + 1];
                const float s2 = p == 0 ? d[0] : p == 1 ? d[1] : p == 2 ? d[2] : d[3];
                const float mean = acc[2 * t][0] * (1.0f / D), var = fmaxf(s2 * (1.0f / D) - mean * mean, 0.f);
                const float mb = rbf(mean), rs = __builtin_amdgcn_rcpf(ln_sd(var));
                const float m2 = rbf((mean - mb) * rs), rs2 = __builtin_amdgcn_rcpf(ln_sd(var * rs * rs));
                st[(m0 + t) * 16 + i16] = (f32x2){rs * rs2, (mb * rs + m2) * rs2};
                if (j == 0) bst8((u32x2){__float_as_uint(mb), __float_as_uint(rs)}, r2, (tok0 + (m0 + t) * 16 + i16) * 8, 0);
            }
        }
    }
    __builtin_amdgcn_s_setprio(0);
    STAMP(ws, 24);
    LGKM0(); BAR();
    STAMP(ws, 10);
    const rsrc_t rg = mk_rsrc(ws + WS_G, (L + 4) * D * 2);
    if (act) {
        const f32x4 cs = cs0 + cs1;
#pragma unroll
        for (int t = 0; t < 2; ++t) {
            const f32x4 a = sel4(kh, acc[2 + t], acc[t]) + xb[((w ^ 1) * 2 + t) * 64 + lane];
            const f32x2 ab = st[(2 * kh + t) * 16 + i16];
            float o[4];
#pragma unroll
            for (int k = 0; k < 4; ++k) o[k] = gelu_tanh(ab[0] * a[k] - ab[1] * cs[k]);
            bst8((u32x2){pk_bf16(o[0], o[1]), pk_bf16(o[2], o[3])}, rg, (i16 * D + 4 * g) * 2, ((2 + tok0 + kh * 32 + t * 16) * D + nb * 16) * 2);
        }
        if ((panel == 0 || panel == 31) && kh == 0 && i16 < 2) bst8((u32x2){0u, 0u}, rg, (i16 * D + 4 * g) * 2, ((panel == 0 ? 0 : L + 2) * D + nb * 16) * 2);
    }
    STAMP(ws, 27);
    VM0(); BAR();
    publish(ws, WS_FG, panel * 32 + j, tag);
}

constexpr int KD_HT = 73728, KD_XB = KD_HT + 10240, KD_PS = KD_XB + 20 * 1024;
__device__ __forceinline__ void phase_d(char* __restrict__ ws, LAS char* lds, int panel, int j, int je, unsigned tag STAMP_ARG) {
    const int tid = threadIdx.x, w = __builtin_amdgcn_readfirstlane(tid >> 6), lane = tid & 63;
    const int tok0 = panel * 64, nbs = nb0_of(j), nblk = nb0_of(j + 1) - nbs;
    const int g = lane >> 4, i16 = lane & 15, q = i16 >> 2, p = lane & 3;
    const int b = w >> 1, kh = w & 1;
    const bool act = w < 10 && b < nblk;
    const int nb = nbs + (act ? b : 0);
    bf16x8 W[9];
    if (w < 10) {
        const rsrc_t rw = mk_rsrc(ws + WS_WF + WSUB * 2, WSUB * 2);
#pragma unroll
        for (int i = 0; i < 9; ++i) W[i] = bld16<SC1>(rw, lane * 16, ((kh * 9 + i) * 36 + nb) * 1024);
    }
    if (w < 2 * nblk) {
        const rsrc_t rh = mk_rsrc(ws + WS_HT, D * L * 2);
        const int fl = ((lane >> 4) & 1) | ((w & 1) << 1);
        blds16<SC1>(rh, lds + KD_HT + w * 1024, (lane >> 3) * 4096 + (((lane & 7) ^ (fl << 1)) * 16), (nbs * 16 + w * 8) * 4096 + tok0 * 2);
    }
    if (w == 10) wait_flags(ws, WS_FW, (2 + 2 * je) * 18, 18, WS_FW, 0, 0, tag);
    if (w == 11) {
        const int p0 = panel > 0 ? panel - 1 : 0, p1 = panel < 31 ? panel + 1 : 31;
        wait_panels(ws, WS_FG, p0, WS_FG, panel, WS_FG, p1, tag);
    }
    BAR();
    STAMP(ws, 12);
    f32x2 st[2];
    {
        const rsrc_t rg = mk_rsrc(ws + WS_G, (L + 4) * D * 2);
        const int c7 = (lane & 7) ^ ((lane >> 3) & 6), vb = (lane >> 3) * 1152 + c7 * 16;
#pragma unroll
        for (int k = 0; k < 6; ++k) {
            const int idx = 6 * w + k, cg = idx >> 3, c = cg * 8 + c7;
            const int o2 = (0x4031 >> (((c * 57) >> 10) * 4)) & 15;
            blds16<SC1>(rg, lds + idx * 1024, vb + o2 * 1152, (tok0 + (idx & 7) * 8) * 1152 + cg * 128);
        }
        const rsrc_t r2 = mk_rsrc(ws + WS_ST2, L * 8);
#pragma unroll
        for (int t = 0; t < 2; ++t) st[t] = __builtin_bit_cast(f32x2, bldu2<SC1>(r2, i16 * 8, (tok0 + kh * 32 + t * 16) * 8));
    }
    VM0(); BAR();
    STAMP(ws, 13);
    f32x4 acc[4] = {};
    LAS f32x4* xb = (LAS f32x4*)(lds + KD_XB);
    if (w < 10) {
        const int z = i16 & 6;
        const LAS char* a0 = lds + i16 * 128 + ((g ^ z) * 16), * a1 = lds + i16 * 128 + (((4 + g) ^ z) * 16);
        const LAS char* e0 = kh ? a1 + 4 * 8192 : a0;
        const LAS char* e1 = kh ? a0 + 5 * 8192 : a1;
        bf16x8 A[2][2][4];
#pragma unroll
        for (int c = 0; c < 6; ++c) {
            if (c < 5) {
#pragma unroll
                for (int ii = 0; ii < 2; ++ii) {
                    const int i = 2 * c + ii;
                    if (i < 9) {
#pragma unroll
                        for (int mt = 0; mt < 4; ++mt) A[c & 1][ii][mt] = *(const LAS bf16x8*)(((i & 1) ? e1 : e0) + (i >> 1) * 8192 + mt * 2048);
                    }
                }
            }
            __builtin_amdgcn_sched_barrier(0);
            if (c > 0) {
#pragma unroll
                for (int ii = 0; ii < 2; ++ii)
                    if (2 * (c - 1) + ii < 9) {
#pragma unroll
                        for (int mt = 0; mt < 4; ++mt) acc[mt] = MFMA16(W[2 * (c - 1) + ii], A[(c - 1) & 1][ii][mt], acc[mt]);
                    }
            }
            __builtin_amdgcn_sched_barrier(0);
        }
        xb[((w * 2 + 0) * 64) + lane] = sel4(kh, acc[0], acc[2]);
        xb[((w * 2 + 1) * 64) + lane] = sel4(kh, acc[1], acc[3]);
    }
    STAMP(ws, 25);
    LGKM0(); BAR();
    STAMP(ws, 14);
    LAS f32x2* ps = (LAS f32x2*)(lds + KD_PS);
    if (w < 10) {
        const int bl = act ? b : 0;
        const int hrow = 16 * bl + 4 * g + q;
        const LAS char* hb = lds + KD_HT + hrow * 128 + 8 * (p & 1);
        const rsrc_t r1 = mk_rsrc(ws + WS_X1, L * D * 2), rr = mk_rsrc(ws + WS_HR, L * D * 2);
#pragma unroll
        for (int t = 0; t < 2; ++t) {
            const int mt = 2 * kh + t;
            const f32x4 a = sel4(kh, acc[2 + t], acc[t]) + xb[((w ^ 1) * 2 + t) * 64 + lane];
            const u32x2 hu = __builtin_bit_cast(u32x2, tr_read(hb + (((2 * mt + (p >> 1)) ^ (fsw(hrow) << 1)) * 16)));
            const float rs = st[t][1], nm = -st[t][0] * rs;
            u32x2 ov;
#pragma unroll
            for (int k = 0; k < 2; ++k)
                ov[k] = pk_bf16(__builtin_fmaf(bf_lo(hu[k]), rs, nm) + a[2 * k], __builtin_fmaf(bf_hi(hu[k]), rs, nm) + a[2 * k + 1]);
            float s1 = dot2bf(ov[1], 0x3f803f80u, dot2bf(ov[0], 0x3f803f80u, 0.f)), s2 = dot2bf(ov[1], ov[1], dot2bf(ov[0], ov[0], 0.f));
            s1 = rsum_g(s1); s2 = rsum_g(s2);
            if (lane < 16) ps[b * 64 + mt * 16 + lane] = act ? (f32x2){s1, s2} : (f32x2){0.f, 0.f};
            if (act) {
                const int so = ((tok0 + mt * 16) * D + nb * 16) * 2, vo = (i16 * D + 4 * g) * 2;
                bst8(ov, r1, vo, so);
                bst8(hu, rr, vo, so);
            }
        }
    }
    STAMP(ws, 26);
    LGKM0(); BAR();
    if (tid < 64) {
        f32x2 t = ps[tid];
#pragma unroll
        for (int k = 1; k < 4; ++k) t += ps[k * 64 + tid];
        if (nblk == 5) t += ps[4 * 64 + tid];
        bst8(__builtin_bit_cast(u32x2, t), mk_rsrc(ws + WS_PST, L * 64), tid * 64, tok0 * 64 + j * 8);
    }
    VM0(); BAR();
    publish(ws, WS_FX, panel * 32 + j, tag);
}

constexpr int BG_LDS = 110592;
__device__ __forceinline__ void phase_cd_background(const Args& a, LAS char* lds, int panel, int job, unsigned tag STAMP_ARG) {
    char* ws = a.ws;
    const int tid = threadIdx.x, w = __builtin_amdgcn_readfirstlane(tid >> 6), lane = tid & 63, tok0 = panel * 64;
    BAR(); BAR();
    {
        const rsrc_t rh = mk_rsrc(ws + WS_HT, D * L * 2);
        const int fl = (lane >> 4) & 1;
        const int v0 = (lane >> 3) * 4096 + (((lane & 7) ^ (fl << 1)) * 16), v1 = (lane >> 3) * 4096 + (((lane & 7) ^ ((fl | 2) << 1)) * 16);
#pragma unroll
        for (int k = 0; k < 6; ++k) { const int idx = 6 * w + k; blds16<SC1>(rh, lds + idx * 1024, (k & 1) ? v1 : v0, idx * 32768 + tok0 * 2); }
    }
    VM0(); BAR();
    PrepRegs P;
    prep_issue(a, job, lane, P);
    BAR(); BAR();
    BAR();
    {
        const rsrc_t rg = mk_rsrc(ws + WS_G, (L + 4) * D * 2);
        const int c7 = (lane & 7) ^ ((lane >> 3) & 6), vb = (lane >> 3) * 1152 + c7 * 16;
#pragma unroll
        for (int k = 0; k < 6; ++k) {
            const int idx = 6 * w + k, cg = idx >> 3, c = cg * 8 + c7;
            const int o2 = (0x4031 >> (((c * 57) >> 10) * 4)) & 15;
            blds16<SC1>(rg, lds + idx * 1024, vb + o2 * 1152, (tok0 + (idx & 7) * 8) * 1152 + cg * 128);
        }
    }
    VM0(); BAR();
    prep_finish(a, job, lds + BG_LDS + (w - 8) * 8192, lane, tag, P STAMP_PASS);
    BAR(); BAR(); BAR();
}

constexpr int KE_HID = 73728, KE_ST = 2 * 73728, KE_FLG = KE_ST + 1024;
constexpr int RING = 4;
__device__ __forceinline__ void phase_e(char* __restrict__ ws, LAS char* lds, int panel, int j, unsigned tag STAMP_ARG) {
    const int tid = threadIdx.x, w = __builtin_amdgcn_readfirstlane(tid >> 6), lane = tid & 63;
    const int tok0 = panel * 64, g = lane >> 4, i16 = lane & 15, nbs = 3 * w;
    const rsrc_t rw = mk_rsrc(ws + WS_WF + (size_t)(2 + 2 * j) * WSUB * 2, 2 * WSUB * 2);
    const int wv = lane * 16, wsb = nbs * 1024;
    bf16x8 W[RING][3];
#pragma unroll
    for (int s = 0; s < RING; ++s)
#pragma unroll
        for (int b = 0; b < 3; ++b) W[s][b] = bld16<SC1>(rw, wv + b * 1024, wsb + s * 36864);
    f32x4 c0[3], c1[3];
    {
        const rsrc_t rc = mk_rsrc(ws + WS_CS, 2 * (D + F) * 4);
#pragma unroll
        for (int b = 0; b < 3; ++b) {
            c0[b] = bldf4<SC1>(rc, g * 16 + b * 64, (D + j * D + nbs * 16) * 4);
            c1[b] = bldf4<SC1>(rc, g * 16 + b * 64, (D + j * D + nbs * 16 + D + F) * 4);
        }
    }
    __builtin_amdgcn_sched_barrier(0);
    if (w == 11) wait_panels(ws, WS_FX, panel, WS_FX, panel, WS_FX, panel, tag);
    BAR();
    STAMP(ws, 16);
    f32x4 pv[4];
    if (w == 11) {
        const rsrc_t rp = mk_rsrc(ws + WS_PST, L * 64);
#pragma unroll
        for (int k = 0; k < 4; ++k) pv[k] = bldf4<SC1>(rp, lane * 64 + k * 16, tok0 * 64);
    }
    {
        const rsrc_t rx = mk_rsrc(ws + WS_X1 + (size_t)tok0 * D * 2, 64 * D * 2);
        const int vo = (lane >> 3) * 1152 + (((lane & 7) ^ ((lane >> 3) & 6)) * 16);
#pragma unroll
        for (int k = 0; k < 6; ++k) {
            const int idx = 6 * w + k;
            blds16<SC1>(rx, lds + idx * 1024, vo, (idx & 7) * 9216 + (idx >> 3) * 128);
        }
    }
    __builtin_amdgcn_sched_barrier(0);
    LAS f32x4* st = (LAS f32x4*)(lds + KE_ST);
    LAS unsigned* flg = (LAS unsigned*)(lds + KE_FLG);
    if (w == 11) {
        float s1 = 0.f, s2 = 0.f;
#pragma unroll
        for (int k = 0; k < 4; ++k) { s1 += pv[k][0] + pv[k][2]; s2 += pv[k][1] + pv[k][3]; }
        const float mean = s1 * (1.0f / D), var = fmaxf(s2 * (1.0f / D) - mean * mean, 0.f);
        const float mb = rbf(mean), sb = ln_sd(var), al = __builtin_amdgcn_rcpf(sb);
        const float k0 = -2.0f * 0.7978845608028654f * 1.4426950408889634f;
        st[lane] = (f32x4){k0 * al, k0 * 0.044715f * al * al * al, sb, -mb};
        if (lane < 4) flg[lane] = 0u;
    }
    VM0(); LGKM0(); BAR();
    STAMP(ws, 17);
    const int z = i16 & 6;
    const int o0 = i16 * 128 + (g ^ z) * 16, o1 = i16 * 128 + ((4 + g) ^ z) * 16;
    f32x4 acc[3][4];
#pragma unroll
    for (int mt = 0; mt < 4; ++mt) {
        const float nm = st[mt * 16 + i16][3];
#pragma unroll
        for (int b = 0; b < 3; ++b) acc[b][mt] = (c0[b] + c1[b]) * nm;
    }
    bf16x8 A[2][4];
#pragma unroll
    for (int ph = 0; ph < 2; ++ph) {
        const LAS char* ab = lds + ph * KE_HID;
        if (ph == 1) {
            while (__hip_atomic_load(flg + 0, __ATOMIC_RELAXED, __HIP_MEMORY_SCOPE_WORKGROUP) < 4u) __builtin_amdgcn_s_sleep(1);
            __builtin_amdgcn_sched_barrier(0);
            asm volatile("" ::: "memory");
        }
#pragma unroll
        for (int mt = 0; mt < 4; ++mt) A[0][mt] = *(const LAS bf16x8*)(ab + o0 + mt * 2048);
#pragma unroll
        for (int s = 0; s < 18; ++s) {
            if (ph == 1 && (s == 5 || s == 11)) {
                while (__hip_atomic_load(flg + (s == 5 ? 1 : 2), __ATOMIC_RELAXED, __HIP_MEMORY_SCOPE_WORKGROUP) < 4u) __builtin_amdgcn_s_sleep(1);
                __builtin_amdgcn_sched_barrier(0);
                asm volatile("" ::: "memory");
            }
            if (ph == 0 && s == 6 && w == 0) {
                wait_flags(ws, WS_FW, (3 + 2 * j) * 18, 18, WS_FW, 0, 0, tag);
                if (lane == 0) __hip_atomic_store(flg + 3, 1u, __ATOMIC_RELAXED, __HIP_MEMORY_SCOPE_WORKGROUP);
            }
            if (ph == 0 && s == 18 - RING) {
                while (__hip_atomic_load(flg + 3, __ATOMIC_RELAXED, __HIP_MEMORY_SCOPE_WORKGROUP) == 0u) __builtin_amdgcn_s_sleep(1);
                __builtin_amdgcn_sched_barrier(0);
                asm volatile("" ::: "memory");
            }
            if (s + 1 < 18) {
#pragma unroll
                for (int mt = 0; mt < 4; ++mt) A[(s + 1) & 1][mt] = *(const LAS bf16x8*)(ab + (((s + 1) & 1) ? o1 : o0) + mt * 2048 + ((s + 1) >> 1) * 8192);
            }
            const int t = ph * 18 + s;
#pragma unroll
            for (int b = 0; b < 3; ++b) {
#pragma unroll
                for (int mt = 0; mt < 4; ++mt) acc[b][mt] = MFMA16(W[t % RING][b], A[s & 1][mt], acc[b][mt]);
                if (t + RING < 36) W[t % RING][b] = bld16<0>(rw, wv + b * 1024, wsb + (t + RING) * 36864);
            }
            __builtin_amdgcn_sched_barrier(0);
        }
        if (ph == 0) {
            STAMP(ws, 18);
#pragma unroll
            for (int mt = 0; mt < 4; ++mt) {
                const f32x4 k = st[mt * 16 + i16];
#pragma unroll
                for (int b = 0; b < 3; ++b) {
                    float o[4];
#pragma unroll
                    for (int e = 0; e < 4; ++e) {
                        const float a = acc[b][mt][e];
                        const float ex = __builtin_amdgcn_exp2f(a * (k[0] + k[1] * (a * a)));
                        o[e] = a * __builtin_amdgcn_rcpf(k[2] + k[2] * ex);
                    }
                    const int nb = nbs + b;
                    *(LAS u32x2*)(lds + KE_HID + (nb >> 2) * 8192 + (mt * 16 + i16) * 128 + (((2 * (nb & 3) + (g >> 1)) ^ z) * 16) + 8 * (g & 1)) =
                        (u32x2){pk_bf16(o[0], o[1]), pk_bf16(o[2], o[3])};
                    acc[b][mt] = (f32x4){0.f, 0.f, 0.f, 0.f};
                }
            }
            LGKM0();
            if (lane == 0) __hip_atomic_fetch_add(flg + (w >> 2), 1u, __ATOMIC_RELAXED, __HIP_MEMORY_SCOPE_WORKGROUP);
            STAMP(ws, 19);
            if (w >= 8) __builtin_amdgcn_s_setprio(2); else if (w >= 4) __builtin_amdgcn_s_setprio(1);
        }
    }
    __builtin_amdgcn_s_setprio(0);
    STAMP(ws, 20);
    const rsrc_t rp = mk_rsrc(ws + WS_PART + (size_t)(panel * 8 + j) * 72 * 1024, 72 * 1024);
#pragma unroll
    for (int b = 0; b < 3; ++b)
#pragma unroll
        for (int m2 = 0; m2 < 2; ++m2) {
            const f32x4 a0 = acc[b][2 * m2], a1 = acc[b][2 * m2 + 1];
            bst16((u32x4){pk_bf16(a0[0], a0[1]), pk_bf16(a0[2], a0[3]), pk_bf16(a1[0], a1[1]), pk_bf16(a1[2], a1[3])}, rp, lane * 16, ((nbs + b) * 2 + m2) * 1024);
        }
    VM0(); BAR();
    publish(ws, WS_FP, panel * 32 + j, tag);
}

__device__ __forceinline__ void phase_f(char* __restrict__ ws, u16* __restrict__ out, int panel, int jj, unsigned tag STAMP_ARG) {
    const int tid = threadIdx.x, w = __builtin_amdgcn_readfirstlane(tid >> 6), lane = tid & 63;
    const int g = lane >> 4, i16 = lane & 15;
    const int pr = jj * 9 + (w < 9 ? w : 0), nb = pr >> 1, m2 = pr & 1;
    u32x2 xa[2], ha[2];
    const int so = ((panel * 64 + m2 * 32) * D + nb * 16) * 2, vo = (i16 * D + 4 * g) * 2;
    if (w < 9) {
    }
    if (w == 11) wait_panels(ws, WS_FP, panel, WS_FX, panel, WS_FX, panel, tag);
    BAR();
    STAMP(ws, 22);
    if (w < 9) {
        const rsrc_t rp = mk_rsrc(ws + WS_PART + (size_t)(panel * 8) * 72 * 1024, 8 * 72 * 1024);
        u32x4 v[8];
#pragma unroll
        for (int k = 0; k < 8; ++k) v[k] = bldu4<SC1>(rp, lane * 16, (k * 72 + pr) * 1024);
        const rsrc_t r1 = mk_rsrc(ws + WS_X1, L * D * 2), rr = mk_rsrc(ws + WS_HR, L * D * 2);
#pragma unroll
        for (int t = 0; t < 2; ++t) { xa[t] = bldu2<SC1>(r1, vo, so + t * 16 * D * 2); ha[t] = bldu2<SC1>(rr, vo, so + t * 16 * D * 2); }
        float s[8] = {};
#pragma unroll
        for (int k = 0; k < 8; ++k)
#pragma unroll
            for (int e = 0; e < 4; ++e) { s[2 * e] += bf_lo(v[k][e]); s[2 * e + 1] += bf_hi(v[k][e]); }
#pragma unroll
        for (int t = 0; t < 2; ++t) {
            float o[4];
#pragma unroll
            for (int k = 0; k < 4; ++k) {
                const float x1 = (k & 1) ? bf_hi(xa[t][k >> 1]) : bf_lo(xa[t][k >> 1]);
                const float h = (k & 1) ? bf_hi(ha[t][k >> 1]) : bf_lo(ha[t][k >> 1]);
                o[k] = h + (x1 + s[4 * t + k]);
            }
            *(u32x2*)((char*)out + so + t * 16 * D * 2 + vo) = (u32x2){pk_bf16(o[0], o[1]), pk_bf16(o[2], o[3])};
        }
    }
}

constexpr int LDS_CTRL = 3 * CV_CH > KE_FLG + 16 ? 3 * CV_CH : KE_FLG + 16;
constexpr int LDS_BYTES = LDS_CTRL + 64;

NOPK __global__ __launch_bounds__(NTHR) void mega_kernel(Args a) {
    extern __shared__ __attribute__((aligned(16))) char smem[];
    LAS char* lds = (LAS char*)smem;
    const int tid = threadIdx.x, w = __builtin_amdgcn_readfirstlane(tid >> 6), lane = tid & 63, b = blockIdx.x;
    STAMP_DECL
    STAMP(a.ws, 0);
    const unsigned tagv = __hip_atomic_load((gu32*)(a.ws + WS_EPOCH), RLX_AGENT);
    const StatRow sr = row_stats_load(a, w, lane);
    __builtin_amdgcn_sched_barrier(0);
    unsigned tag;
    if (b < 192) phase_conv(a, lds, tagv, sr, tag STAMP_PASS);
    else {
        tag = __builtin_amdgcn_readfirstlane(tagv) + 1u;
        if (tid == 0) __hip_atomic_store((gu32*)(a.ws + WS_FA) + b, tag, RLX_AGENT);
        row_stats(a, sr, tag, w, lane);
        BAR();
        STAMP(a.ws, 1);
        const int i = w * 64 + (b - 192);
        if (i >= 36) {
            const unsigned long long t0 = __builtin_amdgcn_s_memrealtime();
            while (__builtin_amdgcn_s_memrealtime() - t0 < PREP_DELAY) __builtin_amdgcn_s_sleep(8);
        }
        if (i < 180) {
            const int job = i < 36 ? i : (2 + 2 * ((i - 36) / 18)) * 18 + (i - 36) % 18;
            PrepRegs P;
            prep_issue(a, job, lane, P);
            prep_finish(a, job, lds + w * 8192, lane, tag, P STAMP_PASS);
        }
        BAR();
    }
    STAMP(a.ws, 7);
    const int je = 2 * (b & 3) + ((b >> 3) & 1);
    {
        const int panel = (b & 7) * 4 + (b >> 6), j = (b >> 3) & 7;
        const int m = (w - 8) * 128 + ((b & 7) | ((b >> 4) << 3));
        if ((j & 1) && (w == 8 || w == 9) && m < 144) phase_cd_background(a, lds, panel, (3 + 2 * (m / 18)) * 18 + m % 18, tag STAMP_PASS);
        else {
            phase_c(a.ws, lds, panel, j, tag STAMP_PASS);
            STAMP(a.ws, 11);
            phase_d(a.ws, lds, panel, j, je, tag STAMP_PASS);
        }
    }
    STAMP(a.ws, 15);
    phase_e(a.ws, lds, ((b >> 2) & 1) * 16 + (b >> 4), je, tag STAMP_PASS);
    STAMP(a.ws, 21);
    phase_f(a.ws, a.out, b >> 3, b & 7, tag STAMP_PASS);
    if (b == 0 && w == 10) {
        wait_flags(a.ws, WS_FA, 0, 256, WS_FA, 0, 0, tag);
        if (lane == 0) __hip_atomic_store((gu32*)(a.ws + WS_EPOCH), tag, RLX_AGENT);
    }
    STAMP(a.ws, 23);
    STAMP_FLUSH(a.ws);
}

extern "C" void kernel_launch(void* const* d_in, const int* in_sizes, int n_in,
                              void* d_out, int out_size, void* d_ws, size_t ws_size,
                              hipStream_t stream) {
    Args a;
    a.x = (const u16*)d_in[0];
    a.are = (const u16*)d_in[1]; a.aim = (const u16*)d_in[2];
    a.bre = (const u16*)d_in[3]; a.bim = (const u16*)d_in[4];
    a.cre = (const u16*)d_in[5]; a.cim = (const u16*)d_in[6];
    a.logdt = (const u16*)d_in[7]; a.dskip = (const u16*)d_in[8];
    a.w1 = (const u16*)d_in[10]; a.w2 = (const u16*)d_in[11];
    a.w3 = (const u16*)d_in[12]; a.w4 = (const u16*)d_in[13];
    a.ws = (char*)d_ws; a.out = (u16*)d_out;
    (void)hipFuncSetAttribute((const void*)mega_kernel, hipFuncAttributeMaxDynamicSharedMemorySize, LDS_BYTES);
    mega_kernel<<<NWG, NTHR, LDS_BYTES, stream>>>(a);
}
```

```cpp
#include <hip/hip_runtime.h>
#include <hip/hip_bf16.h>
#include <stdint.h>

typedef unsigned short u16;
typedef short bf16x8 __attribute__((ext_vector_type(8)));
typedef short s16x4 __attribute__((ext_vector_type(4)));
typedef float f32x2 __attribute__((ext_vector_type(2)));
typedef float f32x4 __attribute__((ext_vector_type(4)));
typedef float f32x16 __attribute__((ext_vector_type(16)));
typedef unsigned u32x2 __attribute__((ext_vector_type(2)));
typedef unsigned u32x4 __attribute__((ext_vector_type(4)));
typedef __bf16 bf16x2_t __attribute__((ext_vector_type(2)));
typedef _Float16 f16x2 __attribute__((ext_vector_type(2)));
typedef _Float16 f16x8 __attribute__((ext_vector_type(8)));
#define LAS __attribute__((address_space(3)))
#define GAS __attribute__((address_space(1)))
#if defined(__HIP_DEVICE_COMPILE__)
#define NOPK __attribute__((target("no-packed-fp32-ops")))
#else
#define NOPK
#endif

constexpr int L = 2048, D = 576, NS = 128, F = 4608;
constexpr int WSUB = D * D;
constexpr float LN_EPS = 1e-5f;
constexpr int NWG = 256, NTHR = 768;
constexpr int SC1 = 16;

constexpr size_t WS_EPOCH = 0;
constexpr int REP = 8;
constexpr size_t WS_FA = 256;
constexpr size_t WS_FW = WS_FA + 1024;
constexpr size_t WS_FG = WS_FW + 2048;
constexpr size_t WS_FX = WS_FG + 4096;
constexpr size_t WS_FP = WS_FX + 4096;
constexpr size_t WS_FH = WS_FP + 4096;
constexpr size_t WS_GR = 32768;
constexpr size_t WS_HT = WS_GR + (size_t)L * 8;
constexpr size_t WS_WF = WS_HT + (size_t)D * L * 2;
constexpr size_t WS_CS = WS_WF + (size_t)18 * WSUB * 2;
constexpr size_t WS_ST2 = WS_CS + (size_t)2 * (D + F) * 4;
constexpr size_t WS_G = WS_ST2 + (size_t)L * 8;
constexpr size_t WS_X1 = WS_G + (size_t)(L + 4) * D * 2;
constexpr size_t WS_HR = WS_X1 + (size_t)L * D * 2;
constexpr size_t WS_PST = WS_HR + (size_t)L * D * 2;
constexpr size_t WS_PART = WS_PST + (size_t)32 * 36 * 64 * 8;
constexpr size_t WS_END = WS_PART + (size_t)32 * 8 * 72 * 512 * 2;

#define STAMP(ws, i) do {} while (0)
#define STAMP_DECL
#define STAMP_ARG
#define STAMP_PASS
#define STAMP_FLUSH(ws) do {} while (0)

__device__ __forceinline__ unsigned pk_bf16(float lo, float hi) {
    f32x2 v = {lo, hi};
    return __builtin_bit_cast(unsigned, __builtin_convertvector(v, bf16x2_t));
}
__device__ __forceinline__ float bf_lo(unsigned p) { return __uint_as_float(p << 16); }
__device__ __forceinline__ float bf_hi(unsigned p) { return __uint_as_float(p & 0xffff0000u); }
__device__ __forceinline__ float bf2f(u16 x) { return __uint_as_float((unsigned)x << 16); }
__device__ __forceinline__ float rbf(float x) { return bf_lo(pk_bf16(x, x)); }
__device__ __forceinline__ float dot2bf(unsigned a, unsigned b, float c) {
    return __builtin_amdgcn_fdot2_f32_bf16(__builtin_bit_cast(bf16x2_t, a), __builtin_bit_cast(bf16x2_t, b), c, false);
}
__device__ __forceinline__ float gelu_tanh(float x) {
    const float c0 = -2.0f * 0.7978845608028654f * 1.4426950408889634f;
    const float c1 = c0 * 0.044715f;
    float e = __builtin_amdgcn_exp2f(x * (c0 + c1 * x * x));
    return x * __builtin_amdgcn_rcpf(1.0f + e);
}
__device__ __forceinline__ s16x4 tr_read(const LAS char* p) {
    return __builtin_amdgcn_ds_read_tr16_b64_v4i16((LAS s16x4*)p);
}
__device__ __forceinline__ bf16x8 cat8(s16x4 a, s16x4 b) {
    return __builtin_shufflevector(a, b, 0, 1, 2, 3, 4, 5, 6, 7);
}
__device__ __forceinline__ f32x4 sel4(bool c, f32x4 a, f32x4 b) {
    f32x4 r;
#pragma unroll
    for (int k = 0; k < 4; ++k) { float x = a[k], y = b[k]; asm volatile("" : "+v"(x), "+v"(y)); r[k] = c ? x : y; }
    return r;
}
__device__ __forceinline__ float rsum_g(float x) {
    const unsigned u = __float_as_uint(x);
    const auto a = __builtin_amdgcn_permlane16_swap(u, u, false, false);
    const float y = __uint_as_float(a[0]) + __uint_as_float(a[1]);
    const unsigned v = __float_as_uint(y);
    const auto b = __builtin_amdgcn_permlane32_swap(v, v, false, false);
    return __uint_as_float(b[0]) + __uint_as_float(b[1]);
}
template <int CTRL> __device__ __forceinline__ float dpp_f(float x) {
    return __uint_as_float(__builtin_amdgcn_update_dpp(0u, __float_as_uint(x), CTRL, 0xf, 0xf, false));
}
__device__ __forceinline__ float rsum_64(float x) {
    x += dpp_f<0xB1>(x);
    x += dpp_f<0x4E>(x);
    x += dpp_f<0x141>(x);
    x += dpp_f<0x140>(x);
    return rsum_g(x);
}
template <int CTRL> __device__ __forceinline__ unsigned dpp_u(unsigned x) { return __builtin_amdgcn_update_dpp(0u, x, CTRL, 0xf, 0xf, false); }
__device__ __forceinline__ unsigned umax2(unsigned a, unsigned b) { return a > b ? a : b; }
__device__ __forceinline__ float rmax_64(float xf) {
    unsigned x = __float_as_uint(xf);
    x = umax2(x, dpp_u<0xB1>(x)); x = umax2(x, dpp_u<0x4E>(x)); x = umax2(x, dpp_u<0x141>(x)); x = umax2(x, dpp_u<0x140>(x));
    const auto a = __builtin_amdgcn_permlane16_swap(x, x, false, false); x = umax2(a[0], a[1]);
    const auto b = __builtin_amdgcn_permlane32_swap(x, x, false, false); x = umax2(b[0], b[1]);
    return __uint_as_float(x);
}
__device__ __forceinline__ float ln_sd(float var) { return rbf(__builtin_amdgcn_sqrtf(rbf(rbf(var) + LN_EPS))); }
__device__ __forceinline__ int fsw(int row) { return ((row >> 1) & 1) | (((row >> 3) & 1) << 1); }
#define MFMA16(a, b, c) __builtin_amdgcn_mfma_f32_16x16x32_bf16((a), (b), (c), 0, 0, 0)
#define MFMA32(a, b, c) __builtin_amdgcn_mfma_f32_32x32x16_bf16((a), (b), (c), 0, 0, 0)
#define VM0() asm volatile("s_waitcnt vmcnt(0)" ::: "memory")
#define LGKM0() asm volatile("s_waitcnt lgkmcnt(0)" ::: "memory")
#define BAR() do { asm volatile("" ::: "memory"); __builtin_amdgcn_s_barrier(); asm volatile("" ::: "memory"); } while (0)

typedef __amdgpu_buffer_rsrc_t rsrc_t;
__device__ __forceinline__ rsrc_t mk_rsrc(const void* p, unsigned bytes) { return __builtin_amdgcn_make_buffer_rsrc((void*)p, 0, bytes, 0x00020000); }
template <int AUX> __device__ __forceinline__ bf16x8 bld16(rsrc_t r, int voff, int soff) {
    return __builtin_bit_cast(bf16x8, __builtin_amdgcn_raw_buffer_load_b128(r, voff, soff, AUX));
}
template <int AUX> __device__ __forceinline__ f32x4 bldf4(rsrc_t r, int voff, int soff) {
    return __builtin_bit_cast(f32x4, __builtin_amdgcn_raw_buffer_load_b128(r, voff, soff, AUX));
}
template <int AUX> __device__ __forceinline__ u32x4 bldu4(rsrc_t r, int voff, int soff) {
    return __builtin_amdgcn_raw_buffer_load_b128(r, voff, soff, AUX);
}
template <int AUX> __device__ __forceinline__ u32x2 bldu2(rsrc_t r, int voff, int soff) {
    return __builtin_amdgcn_raw_buffer_load_b64(r, voff, soff, AUX);
}
template <int AUX> __device__ __forceinline__ void blds16(rsrc_t r, LAS char* l, int voff, int soff) {
    __builtin_amdgcn_raw_ptr_buffer_load_lds(r, (LAS void*)l, 16, voff, soff, 0, AUX);
}
__device__ __forceinline__ void bst16(u32x4 v, rsrc_t r, int voff, int soff) { __builtin_amdgcn_raw_buffer_store_b128(v, r, voff, soff, SC1); }
__device__ __forceinline__ void bst8(u32x2 v, rsrc_t r, int voff, int soff) { __builtin_amdgcn_raw_buffer_store_b64(v, r, voff, soff, SC1); }
__device__ __forceinline__ void bst4(unsigned v, rsrc_t r, int voff, int soff) { __builtin_amdgcn_raw_buffer_store_b32(v, r, voff, soff, SC1); }

struct Args {
    const u16* x; const u16 *are, *aim, *bre, *bim, *cre, *cim, *logdt, *dskip;
    const u16 *w1, *w2, *w3, *w4;
    char* ws; u16* out;
};

typedef GAS unsigned gu32;
typedef GAS unsigned long long gu64;
#define RLX_AGENT __ATOMIC_RELAXED, __HIP_MEMORY_SCOPE_AGENT
#define POLL_MODE 0
#define POLL_GAP 6
#define PREP_DELAY 100ull
constexpr unsigned long long WAIT_TICKS = 400000ull;
__device__ __forceinline__ void publish(char* ws, size_t flag_off, int idx, unsigned tag) {
    if (threadIdx.x == 0) __hip_atomic_store((gu32*)(ws + flag_off) + idx, tag, RLX_AGENT);
}
__device__ __forceinline__ void publish_rep(char* ws, size_t flag_off, int idx, unsigned tag, int lane) {
    if (lane < REP) __hip_atomic_store((gu32*)(ws + flag_off) + lane * 256 + idx, tag, RLX_AGENT);
}
__device__ __forceinline__ void wait_flags(char* ws, size_t offa, int a0, int na, size_t offb, int b0, int nb, unsigned tag) {
    const int lane = threadIdx.x & 63;
    const gu32* p[4]; bool on[4];
#pragma unroll
    for (int k = 0; k < 4; ++k) {
        const int i = lane + 64 * k;
        on[k] = i < na + nb;
        p[k] = i < na ? (const gu32*)(ws + offa) + a0 + i : (const gu32*)(ws + offb) + b0 + (on[k] ? i - na : 0);
    }
    const unsigned long long t0 = __builtin_amdgcn_s_memrealtime();
    for (;;) {
        bool ok = true;
#pragma unroll
        for (int k = 0; k < 4; ++k)
            if (64 * k < na + nb) { const unsigned v = __hip_atomic_load(p[k], RLX_AGENT); ok = ok && (!on[k] || v == tag); }
        if (__all(ok)) break;
        if (__builtin_amdgcn_s_memrealtime() - t0 > WAIT_TICKS) break;
        __builtin_amdgcn_s_sleep(2);
    }
}
__device__ __forceinline__ void wait_panels(char* ws, size_t o0, int p0, size_t o1, int p1, size_t o2, int p2, unsigned tag) {
    const int lane = threadIdx.x & 63, k = (lane >> 3) % 3;
    const gu32* p = (const gu32*)(ws + (k == 0 ? o0 : k == 1 ? o1 : o2)) + (k == 0 ? p0 : k == 1 ? p1 : p2) * 32 + (lane & 7);
    const unsigned long long t0 = __builtin_amdgcn_s_memrealtime();
    for (;;) {
        if (__all(__hip_atomic_load(p, RLX_AGENT) == tag)) break;
        if (__builtin_amdgcn_s_memrealtime() - t0 > WAIT_TICKS) break;
        __builtin_amdgcn_s_sleep(2);
    }
}
struct StatRow { u32x4 v0, v1; };
__device__ __forceinline__ StatRow row_stats_load(const Args& a, int w, int lane) {
    const int row = blockIdx.x * 8 + (w & 7);
    const rsrc_t rx = mk_rsrc(a.x + (size_t)row * D, w < 8 ? D * 2 : 0);
    StatRow r;
    r.v0 = bldu4<0>(rx, lane * 16, 0); r.v1 = bldu4<0>(rx, 1024 + lane * 16, 0);
    return r;
}
__device__ __forceinline__ void row_stats(const Args& a, const StatRow& r, unsigned tag, int w, int lane) {
    if (w < 8) {
        float s1 = 0.f, s2 = 0.f;
#pragma unroll
        for (int k = 0; k < 4; ++k) {
            s1 = dot2bf(r.v0[k], 0x3f803f80u, s1); s2 = dot2bf(r.v0[k], r.v0[k], s2);
            s1 = dot2bf(r.v1[k], 0x3f803f80u, s1); s2 = dot2bf(r.v1[k], r.v1[k], s2);
        }
        s1 = rsum_64(s1); s2 = rsum_64(s2);
        const float mean = s1 * (1.0f / D), var = fmaxf(s2 * (1.0f / D) - mean * mean, 0.f);
        const unsigned val = (pk_bf16(mean, ln_sd(var)));
        if (lane == 0) __hip_atomic_store((gu64*)(a.ws + WS_GR) + blockIdx.x * 8 + w, ((unsigned long long)tag << 32) | val, RLX_AGENT);
    }
}

struct PrepRegs { u32x4 R[36]; };
__device__ __forceinline__ void prep_issue(const Args& a, int job, int lane, PrepRegs& P) {
    const int sub = job / 18, rem = job % 18, kh = rem / 9, ns = rem % 9;
    const u16* src; int ld;
    if (sub == 0) { src = a.w1; ld = D; }
    else if (sub == 1) { src = a.w2; ld = D; }
    else if ((sub & 1) == 0) { src = a.w3 + ((sub - 2) >> 1) * D; ld = F; }
    else { src = a.w4 + (size_t)((sub - 2) >> 1) * D * D; ld = D; }
    src += (size_t)(kh * 288) * ld + ns * 64;
    const rsrc_t rs = mk_rsrc(src, 288 * ld * 2);
    const int ldb = ld * 2;
    const int vo = (lane >> 3) * ldb + (lane & 7) * 16;
#pragma unroll
    for (int i = 0; i < 36; ++i) P.R[i] = bldu4<0>(rs, vo, i * 8 * ldb);
    __builtin_amdgcn_sched_barrier(0);
}
template <bool PUBLISH, bool COLSUM>
__device__ __forceinline__ void prep_finish(const Args& a, int job, LAS char* lds, int lane, unsigned tag, const PrepRegs& P STAMP_ARG) {
    const int sub = job / 18, rem = job % 18, kh = rem / 9, ns = rem % 9;
    const rsrc_t rd = mk_rsrc(a.ws + WS_WF + (size_t)sub * WSUB * 2, WSUB * 2);
    const int fl = (lane >> 4) & 1;
    const int w0 = (lane >> 3) * 128 + (((lane & 7) ^ (fl << 1)) * 16), w1 = (lane >> 3) * 128 + (((lane & 7) ^ ((fl | 2) << 1)) * 16);
    const int g = lane >> 4, i16 = lane & 15, q = i16 >> 2, p = lane & 3;
    const int f = ((q >> 1) & 1) | ((g & 1) << 1);
    int rb[4];
#pragma unroll
    for (int n = 0; n < 4; ++n) rb[n] = (8 * g + q) * 128 + (((2 * n + (p >> 1)) ^ (f << 1)) * 16) + 8 * (p & 1);
    float cs[4] = {0.f, 0.f, 0.f, 0.f};
#pragma unroll
    for (int u = 0; u < 9; ++u) {
        LAS char* sl = lds + (u & 1) * 4096;
#pragma unroll
        for (int i = 0; i < 4; ++i) *(LAS u32x4*)(sl + i * 1024 + ((i & 1) ? w1 : w0)) = P.R[4 * u + i];
        if (u == 0) STAMP(a.ws, 30);
        if (u == 8) STAMP(a.ws, 31);
#pragma unroll
        for (int n = 0; n < 4; ++n) {
            const u32x4 x = __builtin_bit_cast(u32x4, cat8(tr_read(sl + rb[n]), tr_read(sl + rb[n] + 512)));
            bst16(x, rd, lane * 16, ((kh * 9 + u) * 36 + ns * 4 + n) * 1024);
            if (COLSUM) {
#pragma unroll
                for (int k = 0; k < 4; ++k) cs[n] = dot2bf(x[k], 0x3f803f80u, cs[n]);
            }
        }
    }
    if (COLSUM && (sub & 1) == 0) {
        const rsrc_t rc = mk_rsrc(a.ws + WS_CS, 2 * (D + F) * 4);
#pragma unroll
        for (int n = 0; n < 4; ++n) {
            const float c = rsum_g(cs[n]);
            if (lane < 16) bst4(__float_as_uint(c), rc, lane * 4, (kh * (D + F) + (sub == 0 ? 0 : D + ((sub - 2) >> 1) * D) + ns * 64 + n * 16) * 4);
        }
    }
    STAMP(a.ws, 32);
    if (PUBLISH) {
        VM0();
        STAMP(a.ws, 33);
        if (lane == 0) __hip_atomic_store((gu32*)(a.ws + WS_FW) + job, tag, RLX_AGENT);
    }
}

constexpr int CV_RC = 4160;
constexpr int CV_U = 4 * CV_RC;
constexpr int CV_X = CV_U + 2288 * 2;
constexpr int CV_RED = CV_X + 4096;
constexpr int CV_CH = CV_RED + 24576;
constexpr int KG_ROW = 544;
constexpr int KG_XI = 32 * KG_ROW, KG_T = CV_RED + 4096;

struct cpx { float re, im; };
__device__ __forceinline__ cpx cmul(cpx a, cpx b) { return {a.re * b.re - a.im * b.im, a.re * b.im + a.im * b.re}; }
__device__ __forceinline__ cpx cexp_rev(float a2, float rev, float m) {
    const float e = __builtin_amdgcn_exp2f(a2 * m), p = __builtin_amdgcn_fractf(rev * m);
    return {e * __builtin_amdgcn_cosf(p), e * __builtin_amdgcn_sinf(p)};
}
__device__ __forceinline__ unsigned pk_f16(cpx c) { f32x2 v = {c.re, c.im}; return __builtin_bit_cast(unsigned, __builtin_convertvector(v, f16x2)); }

__device__ __forceinline__ void conv_put(LAS char* lds, int m, u32x4 A, u32x2 B) {
    u32x4 c1 = {__builtin_amdgcn_alignbit(A[1], A[0], 16), __builtin_amdgcn_alignbit(A[2], A[1], 16), __builtin_amdgcn_alignbit(A[3], A[2], 16), __builtin_amdgcn_alignbit(B[0], A[3], 16)};
    u32x4 c2 = {A[1], A[2], A[3], B[0]};
    u32x4 c3 = {c1[1], c1[2], c1[3], __builtin_amdgcn_alignbit(B[1], B[0], 16)};
    *(LAS u32x4*)(lds + 0 * CV_RC + m * 16) = A;
    *(LAS u32x4*)(lds + 1 * CV_RC + m * 16) = c1;
    *(LAS u32x4*)(lds + 2 * CV_RC + m * 16) = c2;
    *(LAS u32x4*)(lds + 3 * CV_RC + m * 16) = c3;
}

constexpr int LDS_FWOK = 3 * CV_CH;
__device__ __forceinline__ void phase_conv(const Args& a, LAS char* lds0, unsigned tagv, const StatRow& sr, unsigned& tag_out STAMP_ARG) {
    const int tid = threadIdx.x, w = __builtin_amdgcn_readfirstlane(tid >> 6), lane = tid & 63;
    const int b = blockIdx.x, chl = w >> 2, wq = w & 3, t8 = tid & 255, ch = 3 * b + chl;
    LAS char* lds = lds0 + chl * CV_CH;
    const int part = wq >> 1, n = (wq & 1) * 64 + lane;
    const int o = ch * NS + n, o2 = ch * NS + (n ^ 64);
    const u16 r_ld = a.logdt[ch], r_ar = a.are[o], r_ai = a.aim[o], r_ar2 = a.are[o2];
    u16 r_b[4] = {}, r_c[4] = {};
    if (part) { r_b[0] = a.bre[o]; r_b[1] = a.bim[o]; r_c[0] = a.cre[o]; r_c[1] = a.cim[o]; r_b[2] = a.bre[o2]; r_b[3] = a.bim[o2]; r_c[2] = a.cre[o2]; r_c[3] = a.cim[o2]; }
    const u16 r_ds = a.dskip[ch];
    __builtin_amdgcn_sched_barrier(0);
    const unsigned tag = __builtin_amdgcn_readfirstlane(tagv) + 1u;
    tag_out = tag;
    if (tid == 0) __hip_atomic_store((gu32*)(a.ws + WS_FA) + b, tag, RLX_AGENT);
    row_stats(a, sr, tag, w, lane);
    STAMP(a.ws, 1);
    __builtin_amdgcn_sched_barrier(0);
    u32x2 xr[4];
    const rsrc_t rx = mk_rsrc(a.x, L * D * 2);
    const int xvo = tid * (2 * D * 2) + ((6 * b) & ~3);
#define GATHER(i) do { __builtin_amdgcn_sched_barrier(0); if ((i) < 2 || w < 4) xr[i] = bldu2<0>(rx, xvo + ((i) & 1) * D * 2, ((i) >> 1) * 1536 * D * 2); __builtin_amdgcn_sched_barrier(0); } while (0)
    GATHER(0);
    const float dt = rbf(expf(bf2f(r_ld)));
    const float are = dt * bf2f(r_ar);
    const float ap = rmax_64(fmaxf(fmaxf(are, dt * bf2f(r_ar2)), 0.f));
    const float a2 = (are - ap) * 1.4426950408889634f, rev = dt * bf2f(r_ai) * 0.15915494309189535f;
    float kscale = 1.f;
    if (part == 0) {
        cpx e[4];
        const cpx g8 = cexp_rev(a2, rev, 8.f), g32 = cexp_rev(a2, rev, 32.f);
        e[0] = {1.f, 0.f}; e[1] = g8; e[2] = cmul(g8, g8); e[3] = cmul(e[2], g8);
        LAS char* er = lds + n * 4;
#pragma unroll
        for (int t = 0; t < 8; ++t) {
            if (t == 0) GATHER(1);
            if (t == 3) GATHER(2);
            if (t == 6) GATHER(3);
#pragma unroll
            for (int c = 0; c < 4; ++c) {
                *(LAS unsigned*)(er + (4 * t + c) * KG_ROW) = pk_f16(e[c]);
                if (t < 7) e[c] = (t == 0 && c == 0) ? g32 : cmul(e[c], g32);
            }
        }
    } else {
        const float b0 = dt * bf2f(r_b[0]), b1 = dt * bf2f(r_b[1]), c0 = bf2f(r_c[0]), c1 = bf2f(r_c[1]);
        const float b2 = dt * bf2f(r_b[2]), b3 = dt * bf2f(r_b[3]), c2 = bf2f(r_c[2]), c3 = bf2f(r_c[3]);
        const float wr = c0 * b0 - c1 * b1, wi = c0 * b1 + c1 * b0, vr = c2 * b2 - c3 * b3, vi = c2 * b3 + c3 * b2;
        const float wm = rmax_64(fmaxf(fmaxf(fabsf(wr), fabsf(wi)), fmaxf(fabsf(vr), fabsf(vi))));
        const unsigned ex = (__float_as_uint(wm) >> 23) & 0xffu;
        const unsigned exc = ex < 2u ? 2u : (ex > 252u ? 252u : ex);
        const float sc = __uint_as_float((254u - exc) << 23);
        kscale = __uint_as_float(exc << 23);
        const cpx W = {wr * sc, -wi * sc};
        const cpx g1 = cexp_rev(a2, -rev, 1.f), p256 = cexp_rev(a2, -rev, 256.f);
        const cpx g2 = cmul(g1, g1), g4 = cmul(g2, g2), p512 = cmul(p256, p256);
        LAS char* xr_ = lds + KG_XI + n * 4;
#define XST(i, v) *(LAS unsigned*)(xr_ + (i) * KG_ROW) = pk_f16(v)
        GATHER(1);
        cpx x = W; XST(0, x);
#pragma unroll
        for (int i = 1; i < 8; ++i) { x = cmul(x, g1); XST(i, x); }
        GATHER(2);
        x = cmul(W, p256); XST(8, x);
#pragma unroll
        for (int i = 9; i < 12; ++i) { x = cmul(x, g2); XST(i, x); }
        cpx y = cmul(W, p512); XST(12, y); XST(13, cmul(y, g4));
        x = cmul(y, p256); XST(14, x); XST(15, cmul(x, g4));
        GATHER(3);
        y = cmul(y, p512); XST(16, y);
#pragma unroll
        for (int i = 17; i < 21; ++i) { y = cmul(y, p256); XST(i, y); }
#undef XST
#undef GATHER
        if (lane == 0 && wq == 2) *(LAS float*)(lds + KG_T - 16) = kscale;
    }
    const int p = lane & 15, g = lane >> 4;
    LAS float* T = (LAS float*)(lds + KG_T);
    int base, idx0, istr;
    if (wq & 1) {
        base = p < 4 ? 1024 + 256 * p : (p == 4 ? 2048 : 0);
        idx0 = p < 4 ? 512 + 32 * p : (p == 4 ? 640 : 680);
        istr = 1;
    } else {
        const int q4 = p & 3;
        base = p < 8 ? p : (p < 12 ? 256 + 2 * q4 : 512 + 4 * (p & 1) + 128 * (p & 2));
        idx0 = p < 8 ? p : (p < 12 ? 256 + q4 : 384 + (p & 1) + 32 * (p & 2));
        istr = p < 8 ? 8 : (p < 12 ? 4 : 2);
    }
    const int j0 = (wq >> 1) * 16 + 4 * g;
    LAS float* tw = T + idx0 + j0 * istr;
    const LAS char* ea = lds + ((wq >> 1) * 16 + p) * KG_ROW + g * 16;
    const LAS char* xa = lds + KG_XI + ((wq & 1) * 16 + p) * KG_ROW + g * 16;
    STAMP(a.ws, 29);
    LGKM0(); BAR();
    STAMP(a.ws, 2);
    {
        f32x4 acc = {};
        f16x8 A[8], B[8];
#pragma unroll
        for (int t = 0; t < 8; ++t) { A[t] = *(const LAS f16x8*)(ea + t * 64); B[t] = *(const LAS f16x8*)(xa + t * 64); }
        float ks = *(const LAS float*)(lds + KG_T - 16);
        __builtin_amdgcn_sched_barrier(0);
#pragma unroll
        for (int t = 0; t < 8; ++t) acc = __builtin_amdgcn_mfma_f32_16x16x32_f16(A[t], B[t], acc, 0, 0, 0);
        if (ap > 0.f) {
#pragma unroll
            for (int q = 0; q < 4; ++q) acc[q] *= __builtin_amdgcn_exp2f(ap * 1.4426950408889634f * (float)(base + 8 * (j0 + q)));
        }
#pragma unroll
        for (int q = 0; q < 4; ++q) tw[q * istr] = acc[q] * ks;
    }
    LGKM0(); BAR();
    STAMP(a.ws, 3);
    {
        float v[12];
        const float lf = (float)(2047 - t8 * 8);
        const unsigned tb = (unsigned)(size_t)T - 17024u * 4u;
        if (wq < 3) {
#pragma unroll
            for (int e = 0; e < 12; e += 2) {
                const unsigned bits = pk_bf16(lf - e, lf - (e + 1));
                v[e] = *(const LAS float*)(size_t)(tb + (bits & 0xffffu) * 4u);
                v[e + 1] = *(const LAS float*)(size_t)(tb + (bits >> 16) * 4u);
            }
        } else {
#pragma unroll
            for (int e = 0; e < 12; ++e) {
                const int l = 2047 - (t8 * 8 + e);
                const int idx = l < 128 ? (l < 0 ? 0 : l) : (int)(pk_bf16((float)l, 0.f) & 0xffffu) - 17024;
                const float t = T[idx];
                v[e] = l < 0 ? 0.f : t;
            }
        }
        conv_put(lds, t8, (u32x4){pk_bf16(v[0], v[1]), pk_bf16(v[2], v[3]), pk_bf16(v[4], v[5]), pk_bf16(v[6], v[7])}, (u32x2){pk_bf16(v[8], v[9]), pk_bf16(v[10], v[11])});
        if (t8 < 4) conv_put(lds, 256 + t8, (u32x4){0u, 0u, 0u, 0u}, (u32x2){0u, 0u});
        if (t8 >= 64 && t8 < 94) *(LAS u32x4*)(lds + CV_U + (t8 - 64) * 16) = (u32x4){0u, 0u, 0u, 0u};
    }
    STAMP(a.ws, 4);
    {
        u32x4 gv[2];
        const rsrc_t rgn = mk_rsrc(a.ws + WS_GR, L * 8);
        const unsigned long long t0 = __builtin_amdgcn_s_memrealtime();
        for (;;) {
            gv[0] = bldu4<SC1>(rgn, tid * 16, 0);
            gv[1] = w < 4 ? bldu4<SC1>(rgn, tid * 16, 1536 * 8) : gv[0];
            const bool ok = gv[0][1] == tag && gv[0][3] == tag && gv[1][1] == tag && gv[1][3] == tag;
            if (__all(ok)) break;
            if (__builtin_amdgcn_s_memrealtime() - t0 > WAIT_TICKS) break;
            __builtin_amdgcn_s_sleep(2);
            asm volatile("" ::: "memory");
        }
#pragma unroll
        for (int i = 0; i < 2; ++i) {
            if (i == 0 || w < 4) {
                const int row = 2 * tid + 1536 * i;
                u32x2 x0 = xr[2 * i], x1 = xr[2 * i + 1];
                if (b & 1) {
                    x0 = (u32x2){__builtin_amdgcn_alignbit(x0[1], x0[0], 16), x0[1] >> 16};
                    x1 = (u32x2){__builtin_amdgcn_alignbit(x1[1], x1[0], 16), x1[1] >> 16};
                }
                const float m0 = bf_lo(gv[i][0]), m1 = bf_lo(gv[i][2]);
                const float s0 = __builtin_amdgcn_rcpf(bf_hi(gv[i][0])), s1 = __builtin_amdgcn_rcpf(bf_hi(gv[i][2]));
#pragma unroll
                for (int c = 0; c < 3; ++c) {
                    const unsigned xp = c == 0 ? __builtin_amdgcn_perm(x1[0], x0[0], 0x05040100u) : c == 1 ? __builtin_amdgcn_perm(x1[0], x0[0], 0x07060302u)
                                                                                                            : __builtin_amdgcn_perm(x1[1], x0[1], 0x05040100u);
                    const unsigned dd = pk_bf16(bf_lo(xp) - m0, bf_hi(xp) - m1);
                    *(LAS unsigned*)(lds0 + c * CV_CH + CV_U + 480 + row * 2) = pk_bf16(bf_lo(dd) * s0, bf_hi(dd) * s1);
                    *(LAS unsigned*)(lds0 + c * CV_CH + CV_X + row * 2) = xp;
                }
            }
        }
    }
    LGKM0(); BAR();
    STAMP(a.ws, 5);
    unsigned fwv = tag;
    if (w == 11 && lane < 36) fwv = __hip_atomic_load((gu32*)(a.ws + WS_FW) + lane, RLX_AGENT);
    f32x4 acc[8];
#pragma unroll
    for (int c = 0; c < 8; ++c) acc[c] = (f32x4){0.f, 0.f, 0.f, 0.f};
    const int cpy = (3 - p) & 3;
#pragma unroll
    for (int rr = 0; rr < 2; ++rr) {
        const int r = 2 * wq + rr;
        const LAS char* ab = lds + cpy * CV_RC + 2 * (2031 - 32 * r + 8 * g - p - cpy) - 512 * 7;
        const LAS char* fb = lds + CV_U + 2 * (240 - 32 * r + 16 * p - 16 + 8 * g);
        bf16x8 A[8], Fm[8];
#pragma unroll
        for (int s = 0; s < 8; ++s) {
            const s16x4 lo = *(const volatile LAS s16x4*)(ab + 512 * (7 - s));
            const s16x4 hi = *(const volatile LAS s16x4*)(ab + 512 * (7 - s) + 8);
            A[s] = cat8(lo, hi);
        }
#pragma unroll
        for (int e = 0; e < 8; ++e) Fm[e] = *(const LAS bf16x8*)(fb + 512 * e);
#pragma unroll
        for (int c = 0; c < 8; ++c)
#pragma unroll
            for (int s = 0; s <= c; ++s) acc[c] = MFMA16(A[s], Fm[c - s], acc[c]);
    }
    STAMP(a.ws, 6);
    LAS f32x4* red = (LAS f32x4*)(lds + CV_RED);
#pragma unroll
    for (int c = 0; c < 8; ++c) {
        const int ow = c >> 1;
        if (ow != wq) red[(ow * 6 + (c & 1) * 3 + (wq > ow ? wq - 1 : wq)) * 64 + lane] = acc[c];
    }
    if (w == 11) { const bool okw = __all(fwv == tag); if (lane == 0) *(LAS unsigned*)(lds0 + LDS_FWOK) = okw ? 1u : 0u; }
    LGKM0(); BAR();
    const rsrc_t rh = mk_rsrc(a.ws + WS_HT + (size_t)ch * L * 2, L * 2);
    const float dsk = bf2f(r_ds);
#pragma unroll
    for (int i = 0; i < 2; ++i) {
        f32x4 y;
#pragma unroll
        for (int k = 0; k < 4; ++k) {
            const float y01 = (wq & 1) ? acc[2 + i][k] : acc[i][k], y23 = (wq & 1) ? acc[6 + i][k] : acc[4 + i][k];
            y[k] = (wq & 2) ? y23 : y01;
        }
#pragma unroll
        for (int k = 0; k < 3; ++k) y += red[(wq * 6 + i * 3 + k) * 64 + lane];
        const int l0 = 256 * (2 * wq + i) + 16 * p + 4 * g;
        const u32x2 uu = *(const LAS u32x2*)(lds + CV_U + 480 + 2 * l0), xv = *(const LAS u32x2*)(lds + CV_X + 2 * l0);
        float o[4];
#pragma unroll
        for (int k = 0; k < 4; ++k) {
            const float u = (k & 1) ? bf_hi(uu[k >> 1]) : bf_lo(uu[k >> 1]);
            const float x = (k & 1) ? bf_hi(xv[k >> 1]) : bf_lo(xv[k >> 1]);
            o[k] = x + __builtin_fmaf(u, dsk, y[k]);
        }
        bst8((u32x2){pk_bf16(o[0], o[1]), pk_bf16(o[2], o[3])}, rh, 2 * l0, 0);
    }
    STAMP(a.ws, 28);
    VM0(); BAR();
    if (w == 0) publish_rep(a.ws, WS_FH, b, tag, lane);
}

__device__ __forceinline__ int nb0_of(int j) { return (9 * j + 1) >> 1; }

constexpr int OUT_ROW = 176;
constexpr int KC_XB = 73728, KC_ST = KC_XB + 20 * 1024, KC_OUT = KC_ST + 512;
__device__ __forceinline__ void phase_c(char* __restrict__ ws, LAS char* lds, int panel, int j, unsigned tag STAMP_ARG) {
    const int tid = threadIdx.x, w = __builtin_amdgcn_readfirstlane(tid >> 6), lane = tid & 63;
    const int tok0 = panel * 64, nbs = nb0_of(j), nblk = nb0_of(j + 1) - nbs;
    const int g = lane >> 4, i16 = lane & 15, q = i16 >> 2, p = lane & 3;
    const int b = w >> 1, kh = w & 1;
    const bool act = w < 10 && b < nblk;
    const int nb = nbs + (act ? b : 0);
    if (w == 0 && *(const LAS unsigned*)(lds + LDS_FWOK) == 0u) wait_flags(ws, WS_FW, 0, 36, WS_FW, 0, 0, tag);
    BAR();
    bf16x8 W[9];
    f32x4 cs0, cs1;
    if (w < 10) {
        const rsrc_t rw = mk_rsrc(ws + WS_WF, WSUB * 2);
#pragma unroll
        for (int i = 0; i < 9; ++i) W[i] = bld16<SC1>(rw, lane * 16, ((kh * 9 + i) * 36 + nb) * 1024);
        const rsrc_t rc = mk_rsrc(ws + WS_CS, 2 * (D + F) * 4);
        cs0 = bldf4<SC1>(rc, g * 16, nb * 64); cs1 = bldf4<SC1>(rc, g * 16, nb * 64 + (D + F) * 4);
    }
    if (w == 11) wait_flags(ws, WS_FH, (blockIdx.x & (REP - 1)) * 256, 192, WS_FH, 0, 0, tag);
    BAR();
    STAMP(ws, 8);
    {
        const rsrc_t rh = mk_rsrc(ws + WS_HT, D * L * 2);
        const int fl = (lane >> 4) & 1;
        const int v0 = (lane >> 3) * 4096 + (((lane & 7) ^ (fl << 1)) * 16), v1 = (lane >> 3) * 4096 + (((lane & 7) ^ ((fl | 2) << 1)) * 16);
#pragma unroll
        for (int k = 0; k < 6; ++k) { const int idx = 6 * w + k; blds16<SC1>(rh, lds + idx * 1024, (k & 1) ? v1 : v0, idx * 32768 + tok0 * 2); }
    }
    VM0(); BAR();
    STAMP(ws, 9);
    const int f = ((q >> 1) & 1) | ((g & 1) << 1);
    const int lo_ = (8 * g + q) * 128 + 8 * (p & 1);
    f32x4 acc[4] = {};
    LAS f32x2* st = (LAS f32x2*)(lds + KC_ST);
    LAS f32x4* xb = (LAS f32x4*)(lds + KC_XB);
    if (w < 10) {
        const LAS char* hb[4];
#pragma unroll
        for (int mt = 0; mt < 4; ++mt) hb[mt] = lds + kh * 9 * 4096 + lo_ + (((2 * mt + (p >> 1)) ^ (f << 1)) * 16);
        bf16x8 H[2][2][4];
#pragma unroll
        for (int c = 0; c < 6; ++c) {
            if (c < 5) {
#pragma unroll
                for (int i = 0; i < 2; ++i)
                    if (2 * c + i < 9) {
#pragma unroll
                        for (int mt = 0; mt < 4; ++mt) H[c & 1][i][mt] = cat8(tr_read(hb[mt] + (2 * c + i) * 4096), tr_read(hb[mt] + (2 * c + i) * 4096 + 512));
                    }
            }
            __builtin_amdgcn_sched_barrier(0);
            if (c > 0) {
#pragma unroll
                for (int i = 0; i < 2; ++i)
                    if (2 * (c - 1) + i < 9) {
#pragma unroll
                        for (int mt = 0; mt < 4; ++mt) acc[mt] = MFMA16(W[2 * (c - 1) + i], H[(c - 1) & 1][i][mt], acc[mt]);
                    }
            }
            __builtin_amdgcn_sched_barrier(0);
        }
        xb[((w * 2 + 0) * 64) + lane] = sel4(kh, acc[0], acc[2]);
        xb[((w * 2 + 1) * 64) + lane] = sel4(kh, acc[1], acc[3]);
    } else {
        __builtin_amdgcn_s_setprio(3);
        const bf16x8 ones = {0x3f80, 0x3f80, 0x3f80, 0x3f80, 0x3f80, 0x3f80, 0x3f80, 0x3f80};
        const int m0 = 2 * (w - 10);
        const LAS char* h0 = lds + lo_ + (((2 * m0 + (p >> 1)) ^ (f << 1)) * 16);
        const LAS char* h1 = lds + lo_ + (((2 * m0 + 2 + (p >> 1)) ^ (f << 1)) * 16);
        bf16x8 H[2][3][2];
#pragma unroll
        for (int c = 0; c < 7; ++c) {
            if (c < 6) {
#pragma unroll
                for (int i = 0; i < 3; ++i) {
                    H[c & 1][i][0] = cat8(tr_read(h0 + (3 * c + i) * 4096), tr_read(h0 + (3 * c + i) * 4096 + 512));
                    H[c & 1][i][1] = cat8(tr_read(h1 + (3 * c + i) * 4096), tr_read(h1 + (3 * c + i) * 4096 + 512));
                }
            }
            __builtin_amdgcn_sched_barrier(0);
            if (c > 0) {
#pragma unroll
                for (int i = 0; i < 3; ++i) {
                    const bf16x8 H0 = H[(c - 1) & 1][i][0], H1 = H[(c - 1) & 1][i][1];
                    acc[0] = MFMA16(ones, H0, acc[0]); acc[1] = MFMA16(H0, H0, acc[1]);
                    acc[2] = MFMA16(ones, H1, acc[2]); acc[3] = MFMA16(H1, H1, acc[3]);
                }
            }
            __builtin_amdgcn_sched_barrier(0);
        }
        const float sm[2] = {acc[0][0], acc[2][0]};
        if (g == q) {
            const rsrc_t r2 = mk_rsrc(ws + WS_ST2, L * 8);
#pragma unroll
            for (int t = 0; t < 2; ++t) {
                const f32x4 d = acc[2 * t + 1];
                const float s2 = p == 0 ? d[0] : p == 1 ? d[1] : p == 2 ? d[2] : d[3];
                const float mean = sm[t] * (1.0f / D), var = fmaxf(s2 * (1.0f / D) - mean * mean, 0.f);
                const float mb = rbf(mean), rs = __builtin_amdgcn_rcpf(ln_sd(var));
                const float m2 = rbf((mean - mb) * rs), rs2 = __builtin_amdgcn_rcpf(ln_sd(var * rs * rs));
                st[(m0 + t) * 16 + i16] = (f32x2){rs * rs2, (mb * rs + m2) * rs2};
                if (j == 0) bst8((u32x2){__float_as_uint(mb), __float_as_uint(rs)}, r2, (tok0 + (m0 + t) * 16 + i16) * 8, 0);
            }
        }
    }
    __builtin_amdgcn_s_setprio(0);
    STAMP(ws, 24);
    LGKM0(); BAR();
    STAMP(ws, 10);
    const rsrc_t rg = mk_rsrc(ws + WS_G, (L + 4) * D * 2);
    LAS char* og = lds + KC_OUT;
    if (act) {
        const f32x4 cs = cs0 + cs1;
#pragma unroll
        for (int t = 0; t < 2; ++t) {
            const f32x4 a = sel4(kh, acc[2 + t], acc[t]) + xb[((w ^ 1) * 2 + t) * 64 + lane];
            const f32x2 ab = st[(2 * kh + t) * 16 + i16];
            float o[4];
#pragma unroll
            for (int k = 0; k < 4; ++k) o[k] = gelu_tanh(ab[0] * a[k] - ab[1] * cs[k]);
            *(LAS u32x2*)(og + (kh * 32 + t * 16 + i16) * OUT_ROW + b * 32 + g * 8) = (u32x2){pk_bf16(o[0], o[1]), pk_bf16(o[2], o[3])};
        }
    }
    LGKM0(); BAR();
    {
        const int nc = 2 * nblk, row = nblk == 5 ? tid / 10 : tid >> 3, c = tid - row * nc;
        if (row < 64) bst16(*(const LAS u32x4*)(og + row * OUT_ROW + c * 16), rg, row * (D * 2) + c * 16, ((2 + tok0) * D + nbs * 16) * 2);
        if ((panel == 0 || panel == 31) && tid >= 704 && tid < 704 + 2 * nc) {
            const int z = tid - 704, zr = z >= nc ? 1 : 0;
            bst16((u32x4){0u, 0u, 0u, 0u}, rg, zr * (D * 2) + (z - zr * nc) * 16, ((panel == 0 ? 0 : L + 2) * D + nbs * 16) * 2);
        }
    }
    STAMP(ws, 27);
    VM0(); BAR();
    publish(ws, WS_FG, panel * 32 + j, tag);
}

constexpr int KD_HT = 73728, KD_XB = KD_HT + 10240, KD_OUT = KD_XB + 20 * 1024;
__device__ __forceinline__ void phase_d(char* __restrict__ ws, LAS char* lds, int panel, int j, int je, unsigned tag STAMP_ARG) {
    const int tid = threadIdx.x, w = __builtin_amdgcn_readfirstlane(tid >> 6), lane = tid & 63;
    const int tok0 = panel * 64, nbs = nb0_of(j), nblk = nb0_of(j + 1) - nbs;
    const int g = lane >> 4, i16 = lane & 15, q = i16 >> 2, p = lane & 3;
    const int b = w >> 1, kh = w & 1;
    const bool act = w < 10 && b < nblk;
    const int nb = nbs + (act ? b : 0);
    bf16x8 W[9];
    if (w < 10) {
        const rsrc_t rw = mk_rsrc(ws + WS_WF + WSUB * 2, WSUB * 2);
#pragma unroll
        for (int i = 0; i < 9; ++i) W[i] = bld16<SC1>(rw, lane * 16, ((kh * 9 + i) * 36 + nb) * 1024);
    }
    if (w < 2 * nblk) {
        const rsrc_t rh = mk_rsrc(ws + WS_HT, D * L * 2);
        const int fl = ((lane >> 4) & 1) | ((w & 1) << 1);
        blds16<SC1>(rh, lds + KD_HT + w * 1024, (lane >> 3) * 4096 + (((lane & 7) ^ (fl << 1)) * 16), (nbs * 16 + w * 8) * 4096 + tok0 * 2);
    }
    if (w == 10) wait_flags(ws, WS_FW, (2 + 2 * je) * 18, 18, WS_FW, 0, 0, tag);
    if (w == 11) {
        const int p0 = panel > 0 ? panel - 1 : 0, p1 = panel < 31 ? panel + 1 : 31;
        wait_panels(ws, WS_FG, p0, WS_FG, panel, WS_FG, p1, tag);
    }
    BAR();
    STAMP(ws, 12);
    f32x2 st[2];
    {
        const rsrc_t rg = mk_rsrc(ws + WS_G, (L + 4) * D * 2);
        const int c7 = (lane & 7) ^ ((lane >> 3) & 6), vb = (lane >> 3) * 1152 + c7 * 16;
#pragma unroll
        for (int k = 0; k < 6; ++k) {
            const int idx = 6 * w + k, cg = idx >> 3, c = cg * 8 + c7;
            const int o2 = (0x4031 >> (((c * 57) >> 10) * 4)) & 15;
            blds16<SC1>(rg, lds + idx * 1024, vb + o2 * 1152, (tok0 + (idx & 7) * 8) * 1152 + cg * 128);
        }
        const rsrc_t r2 = mk_rsrc(ws + WS_ST2, L * 8);
#pragma unroll
        for (int t = 0; t < 2; ++t) st[t] = __builtin_bit_cast(f32x2, bldu2<SC1>(r2, i16 * 8, (tok0 + kh * 32 + t * 16) * 8));
    }
    VM0();
    asm volatile("" : "+v"(st[0]), "+v"(st[1]));
    BAR();
    STAMP(ws, 13);
    f32x4 acc[4] = {};
    LAS f32x4* xb = (LAS f32x4*)(lds + KD_XB);
    if (w < 10) {
        const int z = i16 & 6;
        const LAS char* a0 = lds + i16 * 128 + ((g ^ z) * 16), * a1 = lds + i16 * 128 + (((4 + g) ^ z) * 16);
        const LAS char* e0 = kh ? a1 + 4 * 8192 : a0;
        const LAS char* e1 = kh ? a0 + 5 * 8192 : a1;
        bf16x8 A[2][2][4];
#pragma unroll
        for (int c = 0; c < 6; ++c) {
            if (c < 5) {
#pragma unroll
                for (int ii = 0; ii < 2; ++ii) {
                    const int i = 2 * c + ii;
                    if (i < 9) {
#pragma unroll
                        for (int mt = 0; mt < 4; ++mt) A[c & 1][ii][mt] = *(const LAS bf16x8*)(((i & 1) ? e1 : e0) + (i >> 1) * 8192 + mt * 2048);
                    }
                }
            }
            __builtin_amdgcn_sched_barrier(0);
            if (c > 0) {
#pragma unroll
                for (int ii = 0; ii < 2; ++ii)
                    if (2 * (c - 1) + ii < 9) {
#pragma unroll
                        for (int mt = 0; mt < 4; ++mt) acc[mt] = MFMA16(W[2 * (c - 1) + ii], A[(c - 1) & 1][ii][mt], acc[mt]);
                    }
            }
            __builtin_amdgcn_sched_barrier(0);
        }
        xb[((w * 2 + 0) * 64) + lane] = sel4(kh, acc[0], acc[2]);
        xb[((w * 2 + 1) * 64) + lane] = sel4(kh, acc[1], acc[3]);
    }
    STAMP(ws, 25);
    LGKM0(); BAR();
    STAMP(ws, 14);
    LAS char* ox = lds + KD_OUT;
    if (w < 10) {
        const int bl = act ? b : 0;
        const int hrow = 16 * bl + 4 * g + q;
        const LAS char* hb = lds + KD_HT + hrow * 128 + 8 * (p & 1);
        const rsrc_t rp = mk_rsrc(ws + WS_PST, 32 * 36 * 64 * 8);
#pragma unroll
        for (int t = 0; t < 2; ++t) {
            const int mt = 2 * kh + t;
            const f32x4 a = sel4(kh, acc[2 + t], acc[t]) + xb[((w ^ 1) * 2 + t) * 64 + lane];
            const u32x2 hu = __builtin_bit_cast(u32x2, tr_read(hb + (((2 * mt + (p >> 1)) ^ (fsw(hrow) << 1)) * 16)));
            const float rs = st[t][1], nm = -st[t][0] * rs;
            u32x2 ov;
#pragma unroll
            for (int k = 0; k < 2; ++k)
                ov[k] = pk_bf16(__builtin_fmaf(bf_lo(hu[k]), rs, nm) + a[2 * k], __builtin_fmaf(bf_hi(hu[k]), rs, nm) + a[2 * k + 1]);
            float s1 = dot2bf(ov[1], 0x3f803f80u, dot2bf(ov[0], 0x3f803f80u, 0.f)), s2 = dot2bf(ov[1], ov[1], dot2bf(ov[0], ov[0], 0.f));
            s1 = rsum_g(s1); s2 = rsum_g(s2);
            if (act) {
                *(LAS u32x2*)(ox + (mt * 16 + i16) * OUT_ROW + b * 32 + g * 8) = ov;
                *(LAS u32x2*)(ox + 64 * OUT_ROW + (mt * 16 + i16) * OUT_ROW + b * 32 + g * 8) = hu;
                if (lane < 16) bst8((u32x2){__float_as_uint(s1), __float_as_uint(s2)}, rp, lane * 8, ((panel * 36 + nb) * 64 + mt * 16) * 8);
            }
        }
    }
    LGKM0(); BAR();
    {
        const int nc = 2 * nblk, row = nblk == 5 ? tid / 10 : tid >> 3, c = tid - row * nc;
        if (row < 64) {
            const int so = (tok0 * D + nbs * 16) * 2, vo = row * (D * 2) + c * 16;
            bst16(*(const LAS u32x4*)(ox + row * OUT_ROW + c * 16), mk_rsrc(ws + WS_X1, L * D * 2), vo, so);
            bst16(*(const LAS u32x4*)(ox + 64 * OUT_ROW + row * OUT_ROW + c * 16), mk_rsrc(ws + WS_HR, L * D * 2), vo, so);
        }
    }
    STAMP(ws, 26);
    VM0(); BAR();
    publish(ws, WS_FX, panel * 32 + j, tag);
}

constexpr int BG_LDS = 131072;
__device__ __forceinline__ void phase_cd_background(const Args& a, LAS char* lds, int panel, int job, unsigned tag STAMP_ARG) {
    char* ws = a.ws;
    const int tid = threadIdx.x, w = __builtin_amdgcn_readfirstlane(tid >> 6), lane = tid & 63, tok0 = panel * 64;
    BAR(); BAR();
    {
        const rsrc_t rh = mk_rsrc(ws + WS_HT, D * L * 2);
        const int fl = (lane >> 4) & 1;
        const int v0 = (lane >> 3) * 4096 + (((lane & 7) ^ (fl << 1)) * 16), v1 = (lane >> 3) * 4096 + (((lane & 7) ^ ((fl | 2) << 1)) * 16);
#pragma unroll
        for (int k = 0; k < 6; ++k) { const int idx = 6 * w + k; blds16<SC1>(rh, lds + idx * 1024, (k & 1) ? v1 : v0, idx * 32768 + tok0 * 2); }
    }
    VM0(); BAR();
    PrepRegs P;
    prep_issue(a, job, lane, P);
    BAR(); BAR(); BAR();
    prep_finish<false, false>(a, job, lds + BG_LDS + (w - 8) * 8192, lane, tag, P STAMP_PASS);
    BAR();
    {
        const rsrc_t rg = mk_rsrc(ws + WS_G, (L + 4) * D * 2);
        const int c7 = (lane & 7) ^ ((lane >> 3) & 6), vb = (lane >> 3) * 1152 + c7 * 16;
#pragma unroll
        for (int k = 0; k < 6; ++k) {
            const int idx = 6 * w + k, cg = idx >> 3, c = cg * 8 + c7;
            const int o2 = (0x4031 >> (((c * 57) >> 10) * 4)) & 15;
            blds16<SC1>(rg, lds + idx * 1024, vb + o2 * 1152, (tok0 + (idx & 7) * 8) * 1152 + cg * 128);
        }
    }
    VM0();
    if (lane == 0) __hip_atomic_store((gu32*)(ws + WS_FW) + job, tag, RLX_AGENT);
    BAR(); BAR(); BAR(); BAR();
}

constexpr int KE_HID = 73728, KE_ST = 2 * 73728, KE_FLG = KE_ST + 1024;
#define KE_PRIO 1
#define KE_A1 1
#define KE_NB0 3
#define KE_NB1 3
#define KE_NB2 3
#define KE_R0 4
#define KE_R1 4
#define KE_R2 4
template <int NB, int RING, int GRP>
__device__ __forceinline__ void e_body(char* __restrict__ ws, LAS char* lds, int panel, int j, unsigned tag, int w, int lane STAMP_ARG) {
    constexpr int S1 = 2 * KE_NB0, S2 = 2 * (KE_NB0 + KE_NB1);
    const int tok0 = panel * 64, g = lane >> 4, i16 = lane & 15;
    const int nbs = (GRP == 0 ? 0 : GRP == 1 ? 4 * KE_NB0 : 4 * (KE_NB0 + KE_NB1)) + NB * (w & 3);
    const rsrc_t rw = mk_rsrc(ws + WS_WF + (size_t)(2 + 2 * j) * WSUB * 2, 2 * WSUB * 2);
    const int wv = lane * 16, wsb = nbs * 1024;
    bf16x8 W[RING][NB];
#pragma unroll
    for (int s = 0; s < RING; ++s)
#pragma unroll
        for (int b = 0; b < NB; ++b) W[s][b] = bld16<SC1>(rw, wv + b * 1024, wsb + s * 36864);
    f32x4 acc[NB][4];
    {
        const rsrc_t rc = mk_rsrc(ws + WS_CS, 2 * (D + F) * 4);
#pragma unroll
        for (int b = 0; b < NB; ++b) {
            acc[b][0] = bldf4<SC1>(rc, g * 16 + b * 64, (D + j * D + nbs * 16) * 4);
            acc[b][1] = bldf4<SC1>(rc, g * 16 + b * 64, (D + j * D + nbs * 16 + D + F) * 4);
        }
    }
    __builtin_amdgcn_sched_barrier(0);
    unsigned fw4 = tag;
    if (GRP == 2) {
        if (w == 10 && lane < 18) fw4 = __hip_atomic_load((gu32*)(ws + WS_FW) + (3 + 2 * j) * 18 + lane, RLX_AGENT);
        if (w == 11) wait_panels(ws, WS_FX, panel, WS_FX, panel, WS_FX, panel, tag);
    }
    BAR();
    STAMP(ws, 16);
    u32x2 pv[36];
    if (GRP == 2 && w == 11) {
        const rsrc_t rp = mk_rsrc(ws + WS_PST + (size_t)panel * 36 * 512, 36 * 512);
#pragma unroll
        for (int k = 0; k < 36; ++k) pv[k] = bldu2<SC1>(rp, lane * 8, k * 512);
    }
    {
        const rsrc_t rx = mk_rsrc(ws + WS_X1 + (size_t)tok0 * D * 2, 64 * D * 2);
        const int vo = (lane >> 3) * 1152 + (((lane & 7) ^ ((lane >> 3) & 6)) * 16);
#pragma unroll
        for (int k = 0; k < 6; ++k) {
            const int idx = 6 * w + k;
            blds16<SC1>(rx, lds + idx * 1024, vo, (idx & 7) * 9216 + (idx >> 3) * 128);
        }
    }
    __builtin_amdgcn_sched_barrier(0);
    LAS f32x4* st = (LAS f32x4*)(lds + KE_ST);
    LAS unsigned* flg = (LAS unsigned*)(lds + KE_FLG);
    if (GRP == 2 && w == 11) {
        float s1 = 0.f, s2 = 0.f;
#pragma unroll
        for (int k = 0; k < 36; ++k) { s1 += __uint_as_float(pv[k][0]); s2 += __uint_as_float(pv[k][1]); }
        const float mean = s1 * (1.0f / D), var = fmaxf(s2 * (1.0f / D) - mean * mean, 0.f);
        const float mb = rbf(mean), sb = ln_sd(var), al = __builtin_amdgcn_rcpf(sb);
        const float k0 = -2.0f * 0.7978845608028654f * 1.4426950408889634f;
        st[lane] = (f32x4){k0 * al, k0 * 0.044715f * al * al * al, sb, -mb};
        if (lane < 3) flg[lane] = 0u;
    }
    if (GRP == 2 && w == 10) { const bool ok4 = __all(fw4 == tag); if (lane == 0) flg[3] = ok4 ? 1u : 0u; }
    VM0(); LGKM0(); BAR();
    STAMP(ws, 17);
    const int z = i16 & 6;
    const int o0 = i16 * 128 + (g ^ z) * 16, o1 = i16 * 128 + ((4 + g) ^ z) * 16;
    {
        float nm[4];
#pragma unroll
        for (int mt = 0; mt < 4; ++mt) nm[mt] = st[mt * 16 + i16][3];
#pragma unroll
        for (int b = 0; b < NB; ++b) {
            const f32x4 cs = acc[b][0] + acc[b][1];
#pragma unroll
            for (int mt = 0; mt < 4; ++mt) acc[b][mt] = cs * nm[mt];
        }
    }
#define E_SPIN(cond) do { while (cond) __builtin_amdgcn_s_sleep(1); __builtin_amdgcn_sched_barrier(0); asm volatile("" ::: "memory"); } while (0)
#define E_FLG(i) __hip_atomic_load(flg + (i), __ATOMIC_RELAXED, __HIP_MEMORY_SCOPE_WORKGROUP)
    constexpr bool ADB = !(NB >= 4 && KE_A1);
    bf16x8 A[2][4];
#pragma unroll
    for (int ph = 0; ph < 2; ++ph) {
        const LAS char* ab = lds + ph * KE_HID;
        if (ph == 1) E_SPIN(E_FLG(0) < 4u);
        if (ADB) {
#pragma unroll
            for (int mt = 0; mt < 4; ++mt) A[0][mt] = *(const LAS bf16x8*)(ab + o0 + mt * 2048);
        }
#pragma unroll
        for (int s = 0; s < 18; ++s) {
            if (ph == 1 && s + (ADB ? 1 : 0) == S1) E_SPIN(E_FLG(1) < 4u);
            if (ph == 1 && s + (ADB ? 1 : 0) == S2) E_SPIN(E_FLG(2) < 4u);
            if (GRP == 2 && ph == 0 && s == 2 && w == 8 && E_FLG(3) == 0u) {
                wait_flags(ws, WS_FW, (3 + 2 * j) * 18, 18, WS_FW, 0, 0, tag);
                if (lane == 0) __hip_atomic_store(flg + 3, 1u, __ATOMIC_RELAXED, __HIP_MEMORY_SCOPE_WORKGROUP);
            }
            if (ph == 0 && s == 18 - RING) E_SPIN(E_FLG(3) == 0u);
            if (!ADB) {
#pragma unroll
                for (int mt = 0; mt < 4; ++mt) A[0][mt] = *(const LAS bf16x8*)(ab + ((s & 1) ? o1 : o0) + mt * 2048 + (s >> 1) * 8192);
            } else if (s + 1 < 18) {
#pragma unroll
                for (int mt = 0; mt < 4; ++mt) A[(s + 1) & 1][mt] = *(const LAS bf16x8*)(ab + (((s + 1) & 1) ? o1 : o0) + mt * 2048 + ((s + 1) >> 1) * 8192);
            }
            const int t = ph * 18 + s;
#pragma unroll
            for (int b = 0; b < NB; ++b) {
#pragma unroll
                for (int mt = 0; mt < 4; ++mt) acc[b][mt] = MFMA16(W[t % RING][b], A[ADB ? (s & 1) : 0][mt], acc[b][mt]);
                if (t + RING < 36) W[t % RING][b] = bld16<0>(rw, wv + b * 1024, wsb + (t + RING) * 36864);
            }
            __builtin_amdgcn_sched_barrier(0);
        }
        if (ph == 0) {
            STAMP(ws, 18);
#pragma unroll
            for (int mt = 0; mt < 4; ++mt) {
                const f32x4 k = st[mt * 16 + i16];
#pragma unroll
                for (int b = 0; b < NB; ++b) {
                    float o[4];
#pragma unroll
                    for (int e = 0; e < 4; ++e) {
                        const float a = acc[b][mt][e];
                        const float ex = __builtin_amdgcn_exp2f(a * (k[0] + k[1] * (a * a)));
                        o[e] = a * __builtin_amdgcn_rcpf(k[2] + k[2] * ex);
                    }
                    const int nb = nbs + b;
                    *(LAS u32x2*)(lds + KE_HID + (nb >> 2) * 8192 + (mt * 16 + i16) * 128 + (((2 * (nb & 3) + (g >> 1)) ^ z) * 16) + 8 * (g & 1)) =
                        (u32x2){pk_bf16(o[0], o[1]), pk_bf16(o[2], o[3])};
                    acc[b][mt] = (f32x4){0.f, 0.f, 0.f, 0.f};
                }
            }
            LGKM0();
            if (lane == 0) __hip_atomic_fetch_add(flg + GRP, 1u, __ATOMIC_RELAXED, __HIP_MEMORY_SCOPE_WORKGROUP);
            STAMP(ws, 19);
            if (GRP == 2) __builtin_amdgcn_s_setprio(2); else if (GRP == 1) __builtin_amdgcn_s_setprio(1);
        }
    }
#undef E_SPIN
#undef E_FLG
    __builtin_amdgcn_s_setprio(0);
    STAMP(ws, 20);
    const rsrc_t rp = mk_rsrc(ws + WS_PART + (size_t)(panel * 8 + j) * 72 * 1024, 72 * 1024);
#pragma unroll
    for (int b = 0; b < NB; ++b)
#pragma unroll
        for (int m2 = 0; m2 < 2; ++m2) {
            const f32x4 a0 = acc[b][2 * m2], a1 = acc[b][2 * m2 + 1];
            const u32x4 v = {pk_bf16(a0[0], a0[1]), pk_bf16(a0[2], a0[3]), pk_bf16(a1[0], a1[1]), pk_bf16(a1[2], a1[3])};
            const int pr = (nbs + b) * 2 + m2;
            if (pr >= 9 * j && pr < 9 * j + 9) *(LAS u32x4*)(lds + (pr - 9 * j) * 1024 + lane * 16) = v;
            else bst16(v, rp, lane * 16, pr * 1024);
        }
}
__device__ __forceinline__ void phase_e(char* __restrict__ ws, LAS char* lds, int panel, int j, unsigned tag STAMP_ARG) {
    const int w = __builtin_amdgcn_readfirstlane(threadIdx.x >> 6), lane = threadIdx.x & 63;
    if (w < 4) e_body<KE_NB0, KE_R0, 0>(ws, lds, panel, j, tag, w, lane STAMP_PASS);
    else if (w < 8) e_body<KE_NB1, KE_R1, 1>(ws, lds, panel, j, tag, w, lane STAMP_PASS);
    else e_body<KE_NB2, KE_R2, 2>(ws, lds, panel, j, tag, w, lane STAMP_PASS);
    LGKM0();
    VM0(); BAR();
    publish(ws, WS_FP, panel * 32 + j, tag);
}

__device__ __forceinline__ void phase_f(char* __restrict__ ws, LAS char* lds, u16* __restrict__ out, int panel, int jj, unsigned tag STAMP_ARG) {
    const int tid = threadIdx.x, w = __builtin_amdgcn_readfirstlane(tid >> 6), lane = tid & 63;
    const int g = lane >> 4, i16 = lane & 15;
    const int pr = jj * 9 + (w < 9 ? w : 0), nb = pr >> 1, m2 = pr & 1;
    u32x2 xa[2], ha[2];
    const int so = ((panel * 64 + m2 * 32) * D + nb * 16) * 2, vo = (i16 * D + 4 * g) * 2;
    if (w < 9) {
        const rsrc_t r1 = mk_rsrc(ws + WS_X1, L * D * 2), rr = mk_rsrc(ws + WS_HR, L * D * 2);
#pragma unroll
        for (int t = 0; t < 2; ++t) { xa[t] = bldu2<SC1>(r1, vo, so + t * 16 * D * 2); ha[t] = bldu2<SC1>(rr, vo, so + t * 16 * D * 2); }
    }
    if (w == 11) wait_panels(ws, WS_FP, panel, WS_FP, panel, WS_FP, panel, tag);
    BAR();
    STAMP(ws, 22);
    if (w < 9) {
        const rsrc_t rp = mk_rsrc(ws + WS_PART + (size_t)(panel * 8) * 72 * 1024, 8 * 72 * 1024);
        u32x4 v[8];
#pragma unroll
        for (int k = 0; k < 7; ++k) v[k] = bldu4<SC1>(rp, lane * 16, ((k < jj ? k : k + 1) * 72 + pr) * 1024);
        v[7] = *(const LAS u32x4*)(lds + w * 1024 + lane * 16);
        float s[8] = {};
#pragma unroll
        for (int k = 0; k < 8; ++k)
#pragma unroll
            for (int e = 0; e < 4; ++e) { s[2 * e] += bf_lo(v[k][e]); s[2 * e + 1] += bf_hi(v[k][e]); }
#pragma unroll
        for (int t = 0; t < 2; ++t) {
            float o[4];
#pragma unroll
            for (int k = 0; k < 4; ++k) {
                const float x1 = (k & 1) ? bf_hi(xa[t][k >> 1]) : bf_lo(xa[t][k >> 1]);
                const float h = (k & 1) ? bf_hi(ha[t][k >> 1]) : bf_lo(ha[t][k >> 1]);
                o[k] = h + (x1 + s[4 * t + k]);
            }
            *(u32x2*)((char*)out + so + t * 16 * D * 2 + vo) = (u32x2){pk_bf16(o[0], o[1]), pk_bf16(o[2], o[3])};
        }
    }
}

constexpr int LDS_CTRL = 3 * CV_CH > KE_FLG + 16 ? 3 * CV_CH : KE_FLG + 16;
constexpr int LDS_BYTES = LDS_CTRL + 64;

NOPK __global__ __launch_bounds__(NTHR) void mega_kernel(Args a) {
    extern __shared__ __attribute__((aligned(16))) char smem[];
    LAS char* lds = (LAS char*)smem;
    const int tid = threadIdx.x, w = __builtin_amdgcn_readfirstlane(tid >> 6), lane = tid & 63, b = blockIdx.x;
    STAMP_DECL
    STAMP(a.ws, 0);
    const unsigned tagv = __hip_atomic_load((gu32*)(a.ws + WS_EPOCH), RLX_AGENT);
    const StatRow sr = row_stats_load(a, w, lane);
    __builtin_amdgcn_sched_barrier(0);
    unsigned tag;
    if (b < 192) phase_conv(a, lds, tagv, sr, tag STAMP_PASS);
    else {
        tag = __builtin_amdgcn_readfirstlane(tagv) + 1u;
        if (tid == 0) __hip_atomic_store((gu32*)(a.ws + WS_FA) + b, tag, RLX_AGENT);
        if (tid == 0) *(LAS unsigned*)(lds + LDS_FWOK) = 0u;
        row_stats(a, sr, tag, w, lane);
        LGKM0(); BAR();
        STAMP(a.ws, 1);
        const int i = w * 64 + (b - 192);
        if (i >= 36) {
            const unsigned long long t0 = __builtin_amdgcn_s_memrealtime();
            while (__builtin_amdgcn_s_memrealtime() - t0 < PREP_DELAY) __builtin_amdgcn_s_sleep(8);
        }
        if (i < 180) {
            const int job = i < 36 ? i : (2 + 2 * ((i - 36) / 18)) * 18 + (i - 36) % 18;
            PrepRegs P;
            prep_issue(a, job, lane, P);
            prep_finish<true, true>(a, job, lds + w * 8192, lane, tag, P STAMP_PASS);
        }
        BAR();
    }
    STAMP(a.ws, 7);
    const int je = 2 * (b & 3) + ((b >> 3) & 1);
    {
        const int panel = (b & 7) * 4 + (b >> 6), j = (b >> 3) & 7;
        const int m = (w - 8) * 128 + ((b & 7) | ((b >> 4) << 3));
        if ((j & 1) && (w == 8 || w == 9) && m < 144) phase_cd_background(a, lds, panel, (3 + 2 * (m / 18)) * 18 + m % 18, tag STAMP_PASS);
        else {
            phase_c(a.ws, lds, panel, j, tag STAMP_PASS);
            STAMP(a.ws, 11);
            phase_d(a.ws, lds, panel, j, je, tag STAMP_PASS);
        }
    }
    STAMP(a.ws, 15);
    phase_e(a.ws, lds, ((b >> 2) & 1) * 16 + (b >> 4), je, tag STAMP_PASS);
    STAMP(a.ws, 21);
    phase_f(a.ws, lds, a.out, ((b >> 2) & 1) * 16 + (b >> 4), je, tag STAMP_PASS);
    if (b == 0 && w == 10) {
        wait_flags(a.ws, WS_FA, 0, 256, WS_FA, 0, 0, tag);
        if (lane == 0) __hip_atomic_store((gu32*)(a.ws + WS_EPOCH), tag, RLX_AGENT);
    }
    STAMP(a.ws, 23);
    STAMP_FLUSH(a.ws);
}

extern "C" void kernel_launch(void* const* d_in, const int* in_sizes, int n_in,
                              void* d_out, int out_size, void* d_ws, size_t ws_size,
                              hipStream_t stream) {
    Args a;
    a.x = (const u16*)d_in[0];
    a.are = (const u16*)d_in[1]; a.aim = (const u16*)d_in[2];
    a.bre = (const u16*)d_in[3]; a.bim = (const u16*)d_in[4];
    a.cre = (const u16*)d_in[5]; a.cim = (const u16*)d_in[6];
    a.logdt = (const u16*)d_in[7]; a.dskip = (const u16*)d_in[8];
    a.w1 = (const u16*)d_in[10]; a.w2 = (const u16*)d_in[11];
    a.w3 = (const u16*)d_in[12]; a.w4 = (const u16*)d_in[13];
    a.ws = (char*)d_ws; a.out = (u16*)d_out;
    (void)hipFuncSetAttribute((const void*)mega_kernel, hipFuncAttributeMaxDynamicSharedMemorySize, LDS_BYTES);
    mega_kernel<<<NWG, NTHR, LDS_BYTES, stream>>>(a);
}
```

```cpp
#include <hip/hip_runtime.h>
#include <hip/hip_bf16.h>
#include <stdint.h>

typedef unsigned short u16;
typedef short bf16x8 __attribute__((ext_vector_type(8)));
typedef short s16x4 __attribute__((ext_vector_type(4)));
typedef float f32x2 __attribute__((ext_vector_type(2)));
typedef float f32x4 __attribute__((ext_vector_type(4)));
typedef float f32x16 __attribute__((ext_vector_type(16)));
typedef unsigned u32x2 __attribute__((ext_vector_type(2)));
typedef unsigned u32x4 __attribute__((ext_vector_type(4)));
typedef __bf16 bf16x2_t __attribute__((ext_vector_type(2)));
typedef _Float16 f16x2 __attribute__((ext_vector_type(2)));
typedef _Float16 f16x8 __attribute__((ext_vector_type(8)));
#define LAS __attribute__((address_space(3)))
#define GAS __attribute__((address_space(1)))
#if defined(__HIP_DEVICE_COMPILE__)
#define NOPK __attribute__((target("no-packed-fp32-ops")))
#else
#define NOPK
#endif

constexpr int L = 2048, D = 576, NS = 128, F = 4608;
constexpr int WSUB = D * D;
constexpr float LN_EPS = 1e-5f;
constexpr int NWG = 256, NTHR = 768;
constexpr int SC1 = 16;

constexpr size_t WS_EPOCH = 0;
constexpr int REP = 8;
constexpr size_t WS_FA = 256;
constexpr size_t WS_FW = WS_FA + 1024;
constexpr size_t WS_FG = WS_FW + 2048;
constexpr size_t WS_FX = WS_FG + 4096;
constexpr size_t WS_FP = WS_FX + 4096;
constexpr size_t WS_FH = WS_FP + 4096;
constexpr size_t WS_GR = 32768;
constexpr size_t WS_HT = WS_GR + (size_t)L * 8;
constexpr size_t WS_WF = WS_HT + (size_t)D * L * 2;
constexpr size_t WS_CS = WS_WF + (size_t)18 * WSUB * 2;
constexpr size_t WS_ST2 = WS_CS + (size_t)2 * (D + F) * 4;
constexpr size_t WS_G = WS_ST2 + (size_t)L * 8;
constexpr size_t WS_X1 = WS_G + (size_t)(L + 4) * D * 2;
constexpr size_t WS_HR = WS_X1 + (size_t)L * D * 2;
constexpr size_t WS_PST = WS_HR + (size_t)L * D * 2;
constexpr size_t WS_PART = WS_PST + (size_t)32 * 36 * 64 * 8;
constexpr size_t WS_END = WS_PART + (size_t)32 * 8 * 72 * 512 * 2;

#define STAMP(ws, i) do {} while (0)
#define STAMP_DECL
#define STAMP_ARG
#define STAMP_PASS
#define STAMP_FLUSH(ws) do {} while (0)

__device__ __forceinline__ unsigned pk_bf16(float lo, float hi) {
    f32x2 v = {lo, hi};
    return __builtin_bit_cast(unsigned, __builtin_convertvector(v, bf16x2_t));
}
__device__ __forceinline__ float bf_lo(unsigned p) { return __uint_as_float(p << 16); }
__device__ __forceinline__ float bf_hi(unsigned p) { return __uint_as_float(p & 0xffff0000u); }
__device__ __forceinline__ float bf2f(u16 x) { return __uint_as_float((unsigned)x << 16); }
__device__ __forceinline__ float rbf(float x) { return bf_lo(pk_bf16(x, x)); }
__device__ __forceinline__ float dot2bf(unsigned a, unsigned b, float c) {
    return __builtin_amdgcn_fdot2_f32_bf16(__builtin_bit_cast(bf16x2_t, a), __builtin_bit_cast(bf16x2_t, b), c, false);
}
__device__ __forceinline__ float gelu_tanh(float x) {
    const float c0 = -2.0f * 0.7978845608028654f * 1.4426950408889634f;
    const float c1 = c0 * 0.044715f;
    float e = __builtin_amdgcn_exp2f(x * (c0 + c1 * x * x));
    return x * __builtin_amdgcn_rcpf(1.0f + e);
}
__device__ __forceinline__ s16x4 tr_read(const LAS char* p) {
    return __builtin_amdgcn_ds_read_tr16_b64_v4i16((LAS s16x4*)p);
}
__device__ __forceinline__ bf16x8 cat8(s16x4 a, s16x4 b) {
    return __builtin_shufflevector(a, b, 0, 1, 2, 3, 4, 5, 6, 7);
}
__device__ __forceinline__ f32x4 sel4(bool c, f32x4 a, f32x4 b) {
    f32x4 r;
#pragma unroll
    for (int k = 0; k < 4; ++k) { float x = a[k], y = b[k]; asm volatile("" : "+v"(x), "+v"(y)); r[k] = c ? x : y; }
    return r;
}
__device__ __forceinline__ float rsum_g(float x) {
    const unsigned u = __float_as_uint(x);
    const auto a = __builtin_amdgcn_permlane16_swap(u, u, false, false);
    const float y = __uint_as_float(a[0]) + __uint_as_float(a[1]);
    const unsigned v = __float_as_uint(y);
    const auto b = __builtin_amdgcn_permlane32_swap(v, v, false, false);
    return __uint_as_float(b[0]) + __uint_as_float(b[1]);
}
template <int CTRL> __device__ __forceinline__ float dpp_f(float x) {
    return __uint_as_float(__builtin_amdgcn_update_dpp(0u, __float_as_uint(x), CTRL, 0xf, 0xf, false));
}
__device__ __forceinline__ float rsum_64(float x) {
    x += dpp_f<0xB1>(x);
    x += dpp_f<0x4E>(x);
    x += dpp_f<0x141>(x);
    x += dpp_f<0x140>(x);
    return rsum_g(x);
}
template <int CTRL> __device__ __forceinline__ unsigned dpp_u(unsigned x) { return __builtin_amdgcn_update_dpp(0u, x, CTRL, 0xf, 0xf, false); }
__device__ __forceinline__ unsigned umax2(unsigned a, unsigned b) { return a > b ? a : b; }
__device__ __forceinline__ float rmax_64(float xf) {
    unsigned x = __float_as_uint(xf);
    x = umax2(x, dpp_u<0xB1>(x)); x = umax2(x, dpp_u<0x4E>(x)); x = umax2(x, dpp_u<0x141>(x)); x = umax2(x, dpp_u<0x140>(x));
    const auto a = __builtin_amdgcn_permlane16_swap(x, x, false, false); x = umax2(a[0], a[1]);
    const auto b = __builtin_amdgcn_permlane32_swap(x, x, false, false); x = umax2(b[0], b[1]);
    return __uint_as_float(x);
}
__device__ __forceinline__ float ln_sd(float var) { return rbf(__builtin_amdgcn_sqrtf(rbf(rbf(var) + LN_EPS))); }
__device__ __forceinline__ int fsw(int row) { return ((row >> 1) & 1) | (((row >> 3) & 1) << 1); }
#define MFMA16(a, b, c) __builtin_amdgcn_mfma_f32_16x16x32_bf16((a), (b), (c), 0, 0, 0)
#define MFMA32(a, b, c) __builtin_amdgcn_mfma_f32_32x32x16_bf16((a), (b), (c), 0, 0, 0)
#define VM0() asm volatile("s_waitcnt vmcnt(0)" ::: "memory")
#define LGKM0() asm volatile("s_waitcnt lgkmcnt(0)" ::: "memory")
#define BAR() do { asm volatile("" ::: "memory"); __builtin_amdgcn_s_barrier(); asm volatile("" ::: "memory"); } while (0)

typedef __amdgpu_buffer_rsrc_t rsrc_t;
__device__ __forceinline__ rsrc_t mk_rsrc(const void* p, unsigned bytes) { return __builtin_amdgcn_make_buffer_rsrc((void*)p, 0, bytes, 0x00020000); }
template <int AUX> __device__ __forceinline__ bf16x8 bld16(rsrc_t r, int voff, int soff) {
    return __builtin_bit_cast(bf16x8, __builtin_amdgcn_raw_buffer_load_b128(r, voff, soff, AUX));
}
template <int AUX> __device__ __forceinline__ f32x4 bldf4(rsrc_t r, int voff, int soff) {
    return __builtin_bit_cast(f32x4, __builtin_amdgcn_raw_buffer_load_b128(r, voff, soff, AUX));
}
template <int AUX> __device__ __forceinline__ u32x4 bldu4(rsrc_t r, int voff, int soff) {
    return __builtin_amdgcn_raw_buffer_load_b128(r, voff, soff, AUX);
}
template <int AUX> __device__ __forceinline__ u32x2 bldu2(rsrc_t r, int voff, int soff) {
    return __builtin_amdgcn_raw_buffer_load_b64(r, voff, soff, AUX);
}
template <int AUX> __device__ __forceinline__ void blds16(rsrc_t r, LAS char* l, int voff, int soff) {
    __builtin_amdgcn_raw_ptr_buffer_load_lds(r, (LAS void*)l, 16, voff, soff, 0, AUX);
}
__device__ __forceinline__ void bst16(u32x4 v, rsrc_t r, int voff, int soff) { __builtin_amdgcn_raw_buffer_store_b128(v, r, voff, soff, SC1); }
__device__ __forceinline__ void bst8(u32x2 v, rsrc_t r, int voff, int soff) { __builtin_amdgcn_raw_buffer_store_b64(v, r, voff, soff, SC1); }
__device__ __forceinline__ void bst4(unsigned v, rsrc_t r, int voff, int soff) { __builtin_amdgcn_raw_buffer_store_b32(v, r, voff, soff, SC1); }

struct Args {
    const u16* x; const u16 *are, *aim, *bre, *bim, *cre, *cim, *logdt, *dskip;
    const u16 *w1, *w2, *w3, *w4;
    char* ws; u16* out;
};

typedef GAS unsigned gu32;
typedef GAS unsigned long long gu64;
#define RLX_AGENT __ATOMIC_RELAXED, __HIP_MEMORY_SCOPE_AGENT
#define WAUX 16
#define OAUX 0
#define PSLEEP 2
#define LATE_STATS 0
#define POLL_MODE 0
#define POLL_GAP 6
#define PREP_DELAY 100ull
constexpr unsigned long long WAIT_TICKS = 400000ull;
__device__ __forceinline__ void publish(char* ws, size_t flag_off, int idx, unsigned tag) {
    if (threadIdx.x == 0) __hip_atomic_store((gu32*)(ws + flag_off) + idx, tag, RLX_AGENT);
}
__device__ __forceinline__ void publish_rep(char* ws, size_t flag_off, int idx, unsigned tag, int lane) {
    if (lane < REP) __hip_atomic_store((gu32*)(ws + flag_off) + lane * 256 + idx, tag, RLX_AGENT);
}
__device__ __forceinline__ void wait_flags(char* ws, size_t offa, int a0, int na, size_t offb, int b0, int nb, unsigned tag) {
    const int lane = threadIdx.x & 63;
    const gu32* p[4]; bool on[4];
#pragma unroll
    for (int k = 0; k < 4; ++k) {
        const int i = lane + 64 * k;
        on[k] = i < na + nb;
        p[k] = i < na ? (const gu32*)(ws + offa) + a0 + i : (const gu32*)(ws + offb) + b0 + (on[k] ? i - na : 0);
    }
    const unsigned long long t0 = __builtin_amdgcn_s_memrealtime();
    for (;;) {
        bool ok = true;
#pragma unroll
        for (int k = 0; k < 4; ++k)
            if (64 * k < na + nb) { const unsigned v = __hip_atomic_load(p[k], RLX_AGENT); ok = ok && (!on[k] || v == tag); }
        if (__all(ok)) break;
        if (__builtin_amdgcn_s_memrealtime() - t0 > WAIT_TICKS) break;
        __builtin_amdgcn_s_sleep(PSLEEP);
    }
}
__device__ __forceinline__ void wait_panels(char* ws, size_t o0, int p0, size_t o1, int p1, size_t o2, int p2, unsigned tag) {
    const int lane = threadIdx.x & 63, k = (lane >> 3) % 3;
    const gu32* p = (const gu32*)(ws + (k == 0 ? o0 : k == 1 ? o1 : o2)) + (k == 0 ? p0 : k == 1 ? p1 : p2) * 32 + (lane & 7);
    const unsigned long long t0 = __builtin_amdgcn_s_memrealtime();
    for (;;) {
        if (__all(__hip_atomic_load(p, RLX_AGENT) == tag)) break;
        if (__builtin_amdgcn_s_memrealtime() - t0 > WAIT_TICKS) break;
        __builtin_amdgcn_s_sleep(PSLEEP);
    }
}
struct StatRow { u32x4 v0, v1; };
__device__ __forceinline__ StatRow row_stats_load(const Args& a, int w, int lane) {
    const int row = blockIdx.x * 8 + (w & 7);
    const rsrc_t rx = mk_rsrc(a.x + (size_t)row * D, w < 8 ? D * 2 : 0);
    StatRow r;
    r.v0 = bldu4<0>(rx, lane * 16, 0); r.v1 = bldu4<0>(rx, 1024 + lane * 16, 0);
    return r;
}
__device__ __forceinline__ void row_stats(const Args& a, const StatRow& r, unsigned tag, int w, int lane) {
    if (w < 8) {
        float s1 = 0.f, s2 = 0.f;
#pragma unroll
        for (int k = 0; k < 4; ++k) {
            s1 = dot2bf(r.v0[k], 0x3f803f80u, s1); s2 = dot2bf(r.v0[k], r.v0[k], s2);
            s1 = dot2bf(r.v1[k], 0x3f803f80u, s1); s2 = dot2bf(r.v1[k], r.v1[k], s2);
        }
        s1 = rsum_64(s1); s2 = rsum_64(s2);
        const float mean = s1 * (1.0f / D), var = fmaxf(s2 * (1.0f / D) - mean * mean, 0.f);
        const unsigned val = (pk_bf16(mean, ln_sd(var)));
        if (lane == 0) __hip_atomic_store((gu64*)(a.ws + WS_GR) + blockIdx.x * 8 + w, ((unsigned long long)tag << 32) | val, RLX_AGENT);
    }
}

struct PrepRegs { u32x4 R[36]; };
__device__ __forceinline__ void prep_issue(const Args& a, int job, int lane, PrepRegs& P) {
    const int sub = job / 18, rem = job % 18, kh = rem / 9, ns = rem % 9;
    const u16* src; int ld;
    if (sub == 0) { src = a.w1; ld = D; }
    else if (sub == 1) { src = a.w2; ld = D; }
    else if ((sub & 1) == 0) { src = a.w3 + ((sub - 2) >> 1) * D; ld = F; }
    else { src = a.w4 + (size_t)((sub - 2) >> 1) * D * D; ld = D; }
    src += (size_t)(kh * 288) * ld + ns * 64;
    const rsrc_t rs = mk_rsrc(src, 288 * ld * 2);
    const int ldb = ld * 2;
    const int vo = (lane >> 3) * ldb + (lane & 7) * 16;
#pragma unroll
    for (int i = 0; i < 36; ++i) P.R[i] = bldu4<0>(rs, vo, i * 8 * ldb);
    __builtin_amdgcn_sched_barrier(0);
}
template <bool PUBLISH, bool COLSUM>
__device__ __forceinline__ void prep_finish(const Args& a, int job, LAS char* lds, int lane, unsigned tag, const PrepRegs& P STAMP_ARG) {
    const int sub = job / 18, rem = job % 18, kh = rem / 9, ns = rem % 9;
    const rsrc_t rd = mk_rsrc(a.ws + WS_WF + (size_t)sub * WSUB * 2, WSUB * 2);
    const int fl = (lane >> 4) & 1;
    const int w0 = (lane >> 3) * 128 + (((lane & 7) ^ (fl << 1)) * 16), w1 = (lane >> 3) * 128 + (((lane & 7) ^ ((fl | 2) << 1)) * 16);
    const int g = lane >> 4, i16 = lane & 15, q = i16 >> 2, p = lane & 3;
    const int f = ((q >> 1) & 1) | ((g & 1) << 1);
    int rb[4];
#pragma unroll
    for (int n = 0; n < 4; ++n) rb[n] = (8 * g + q) * 128 + (((2 * n + (p >> 1)) ^ (f << 1)) * 16) + 8 * (p & 1);
    float cs[4] = {0.f, 0.f, 0.f, 0.f};
#pragma unroll
    for (int u = 0; u < 9; ++u) {
        LAS char* sl = lds + (u & 1) * 4096;
#pragma unroll
        for (int i = 0; i < 4; ++i) *(LAS u32x4*)(sl + i * 1024 + ((i & 1) ? w1 : w0)) = P.R[4 * u + i];
        if (u == 0) STAMP(a.ws, 30);
        if (u == 8) STAMP(a.ws, 31);
#pragma unroll
        for (int n = 0; n < 4; ++n) {
            const u32x4 x = __builtin_bit_cast(u32x4, cat8(tr_read(sl + rb[n]), tr_read(sl + rb[n] + 512)));
            bst16(x, rd, lane * 16, ((kh * 9 + u) * 36 + ns * 4 + n) * 1024);
            if (COLSUM) {
#pragma unroll
                for (int k = 0; k < 4; ++k) cs[n] = dot2bf(x[k], 0x3f803f80u, cs[n]);
            }
        }
    }
    if (COLSUM && (sub & 1) == 0) {
        const rsrc_t rc = mk_rsrc(a.ws + WS_CS, 2 * (D + F) * 4);
#pragma unroll
        for (int n = 0; n < 4; ++n) {
            const float c = rsum_g(cs[n]);
            if (lane < 16) bst4(__float_as_uint(c), rc, lane * 4, (kh * (D + F) + (sub == 0 ? 0 : D + ((sub - 2) >> 1) * D) + ns * 64 + n * 16) * 4);
        }
    }
    STAMP(a.ws, 32);
    if (PUBLISH) {
        VM0();
        STAMP(a.ws, 33);
        if (lane == 0) __hip_atomic_store((gu32*)(a.ws + WS_FW) + job, tag, RLX_AGENT);
    }
}

constexpr int CV_RC = 4160;
constexpr int CV_U = 4 * CV_RC;
constexpr int CV_X = CV_U + 2288 * 2;
constexpr int CV_RED = CV_X + 4096;
constexpr int CV_CH = CV_RED + 24576;
constexpr int KG_ROW = 544;
constexpr int KG_XI = 32 * KG_ROW, KG_T = CV_RED + 4096;

struct cpx { float re, im; };
__device__ __forceinline__ cpx cmul(cpx a, cpx b) { return {a.re * b.re - a.im * b.im, a.re * b.im + a.im * b.re}; }
__device__ __forceinline__ cpx cexp_rev(float a2, float rev, float m) {
    const float e = __builtin_amdgcn_exp2f(a2 * m), p = __builtin_amdgcn_fractf(rev * m);
    return {e * __builtin_amdgcn_cosf(p), e * __builtin_amdgcn_sinf(p)};
}
__device__ __forceinline__ unsigned pk_f16(cpx c) { f32x2 v = {c.re, c.im}; return __builtin_bit_cast(unsigned, __builtin_convertvector(v, f16x2)); }

__device__ __forceinline__ void conv_put(LAS char* lds, int m, u32x4 A, u32x2 B) {
    u32x4 c1 = {__builtin_amdgcn_alignbit(A[1], A[0], 16), __builtin_amdgcn_alignbit(A[2], A[1], 16), __builtin_amdgcn_alignbit(A[3], A[2], 16), __builtin_amdgcn_alignbit(B[0], A[3], 16)};
    u32x4 c2 = {A[1], A[2], A[3], B[0]};
    u32x4 c3 = {c1[1], c1[2], c1[3], __builtin_amdgcn_alignbit(B[1], B[0], 16)};
    *(LAS u32x4*)(lds + 0 * CV_RC + m * 16) = A;
    *(LAS u32x4*)(lds + 1 * CV_RC + m * 16) = c1;
    *(LAS u32x4*)(lds + 2 * CV_RC + m * 16) = c2;
    *(LAS u32x4*)(lds + 3 * CV_RC + m * 16) = c3;
}

constexpr int LDS_FWOK = 3 * CV_CH;
__device__ __forceinline__ void phase_conv(const Args& a, LAS char* lds0, unsigned tagv, const StatRow& sr, unsigned& tag_out STAMP_ARG) {
    const int tid = threadIdx.x, w = __builtin_amdgcn_readfirstlane(tid >> 6), lane = tid & 63;
    const int b = blockIdx.x, chl = w >> 2, wq = w & 3, t8 = tid & 255, ch = 3 * b + chl;
    LAS char* lds = lds0 + chl * CV_CH;
    const int part = wq >> 1, n = (wq & 1) * 64 + lane;
    const int o = ch * NS + n, o2 = ch * NS + (n ^ 64);
    const u16 r_ld = a.logdt[ch], r_ar = a.are[o], r_ai = a.aim[o], r_ar2 = a.are[o2];
    u16 r_b[4] = {}, r_c[4] = {};
    if (part) { r_b[0] = a.bre[o]; r_b[1] = a.bim[o]; r_c[0] = a.cre[o]; r_c[1] = a.cim[o]; r_b[2] = a.bre[o2]; r_b[3] = a.bim[o2]; r_c[2] = a.cre[o2]; r_c[3] = a.cim[o2]; }
    const u16 r_ds = a.dskip[ch];
    __builtin_amdgcn_sched_barrier(0);
    const unsigned tag = __builtin_amdgcn_readfirstlane(tagv) + 1u;
    tag_out = tag;
    if (tid == 0) __hip_atomic_store((gu32*)(a.ws + WS_FA) + b, tag, RLX_AGENT);
    row_stats(a, sr, tag, w, lane);
    STAMP(a.ws, 1);
    __builtin_amdgcn_sched_barrier(0);
    u32x2 xr[4];
    const rsrc_t rx = mk_rsrc(a.x, L * D * 2);
    const int xvo = tid * (2 * D * 2) + ((6 * b) & ~3);
#define GATHER(i) do { __builtin_amdgcn_sched_barrier(0); if ((i) < 2 || w < 4) xr[i] = bldu2<0>(rx, xvo + ((i) & 1) * D * 2, ((i) >> 1) * 1536 * D * 2); __builtin_amdgcn_sched_barrier(0); } while (0)
    GATHER(0);
    const float dt = rbf(expf(bf2f(r_ld)));
    const float are = dt * bf2f(r_ar);
    const float ap = rmax_64(fmaxf(fmaxf(are, dt * bf2f(r_ar2)), 0.f));
    const float a2 = (are - ap) * 1.4426950408889634f, rev = dt * bf2f(r_ai) * 0.15915494309189535f;
    float kscale = 1.f;
    if (part == 0) {
        cpx e[4];
        const cpx g8 = cexp_rev(a2, rev, 8.f), g32 = cexp_rev(a2, rev, 32.f);
        e[0] = {1.f, 0.f}; e[1] = g8; e[2] = cmul(g8, g8); e[3] = cmul(e[2], g8);
        LAS char* er = lds + n * 4;
#pragma unroll
        for (int t = 0; t < 8; ++t) {
            if (t == 0) GATHER(1);
            if (t == 3) GATHER(2);
            if (t == 6) GATHER(3);
#pragma unroll
            for (int c = 0; c < 4; ++c) {
                *(LAS unsigned*)(er + (4 * t + c) * KG_ROW) = pk_f16(e[c]);
                if (t < 7) e[c] = (t == 0 && c == 0) ? g32 : cmul(e[c], g32);
            }
        }
    } else {
        const float b0 = dt * bf2f(r_b[0]), b1 = dt * bf2f(r_b[1]), c0 = bf2f(r_c[0]), c1 = bf2f(r_c[1]);
        const float b2 = dt * bf2f(r_b[2]), b3 = dt * bf2f(r_b[3]), c2 = bf2f(r_c[2]), c3 = bf2f(r_c[3]);
        const float wr = c0 * b0 - c1 * b1, wi = c0 * b1 + c1 * b0, vr = c2 * b2 - c3 * b3, vi = c2 * b3 + c3 * b2;
        const float wm = rmax_64(fmaxf(fmaxf(fabsf(wr), fabsf(wi)), fmaxf(fabsf(vr), fabsf(vi))));
        const unsigned ex = (__float_as_uint(wm) >> 23) & 0xffu;
        const unsigned exc = ex < 2u ? 2u : (ex > 252u ? 252u : ex);
        const float sc = __uint_as_float((254u - exc) << 23);
        kscale = __uint_as_float(exc << 23);
        const cpx W = {wr * sc, -wi * sc};
        const cpx g1 = cexp_rev(a2, -rev, 1.f), p256 = cexp_rev(a2, -rev, 256.f);
        const cpx g2 = cmul(g1, g1), g4 = cmul(g2, g2), p512 = cmul(p256, p256);
        LAS char* xr_ = lds + KG_XI + n * 4;
#define XST(i, v) *(LAS unsigned*)(xr_ + (i) * KG_ROW) = pk_f16(v)
        GATHER(1);
        cpx x = W; XST(0, x);
#pragma unroll
        for (int i = 1; i < 8; ++i) { x = cmul(x, g1); XST(i, x); }
        GATHER(2);
        x = cmul(W, p256); XST(8, x);
#pragma unroll
        for (int i = 9; i < 12; ++i) { x = cmul(x, g2); XST(i, x); }
        cpx y = cmul(W, p512); XST(12, y); XST(13, cmul(y, g4));
        x = cmul(y, p256); XST(14, x); XST(15, cmul(x, g4));
        GATHER(3);
        y = cmul(y, p512); XST(16, y);
#pragma unroll
        for (int i = 17; i < 21; ++i) { y = cmul(y, p256); XST(i, y); }
#undef XST
#undef GATHER
        if (lane == 0 && wq == 2) *(LAS float*)(lds + KG_T - 16) = kscale;
    }
    const int p = lane & 15, g = lane >> 4;
    LAS float* T = (LAS float*)(lds + KG_T);
    int base, idx0, istr;
    if (wq & 1) {
        base = p < 4 ? 1024 + 256 * p : (p == 4 ? 2048 : 0);
        idx0 = p < 4 ? 512 + 32 * p : (p == 4 ? 640 : 680);
        istr = 1;
    } else {
        const int q4 = p & 3;
        base = p < 8 ? p : (p < 12 ? 256 + 2 * q4 : 512 + 4 * (p & 1) + 128 * (p & 2));
        idx0 = p < 8 ? p : (p < 12 ? 256 + q4 : 384 + (p & 1) + 32 * (p & 2));
        istr = p < 8 ? 8 : (p < 12 ? 4 : 2);
    }
    const int j0 = (wq >> 1) * 16 + 4 * g;
    LAS float* tw = T + idx0 + j0 * istr;
    const LAS char* ea = lds + ((wq >> 1) * 16 + p) * KG_ROW + g * 16;
    const LAS char* xa = lds + KG_XI + ((wq & 1) * 16 + p) * KG_ROW + g * 16;
    STAMP(a.ws, 29);
    LGKM0(); BAR();
    STAMP(a.ws, 2);
    {
        f32x4 acc = {};
        f16x8 A[8], B[8];
#pragma unroll
        for (int t = 0; t < 8; ++t) { A[t] = *(const LAS f16x8*)(ea + t * 64); B[t] = *(const LAS f16x8*)(xa + t * 64); }
        float ks = *(const LAS float*)(lds + KG_T - 16);
        __builtin_amdgcn_sched_barrier(0);
#pragma unroll
        for (int t = 0; t < 8; ++t) acc = __builtin_amdgcn_mfma_f32_16x16x32_f16(A[t], B[t], acc, 0, 0, 0);
        if (ap > 0.f) {
#pragma unroll
            for (int q = 0; q < 4; ++q) acc[q] *= __builtin_amdgcn_exp2f(ap * 1.4426950408889634f * (float)(base + 8 * (j0 + q)));
        }
#pragma unroll
        for (int q = 0; q < 4; ++q) tw[q * istr] = acc[q] * ks;
    }
    LGKM0(); BAR();
    STAMP(a.ws, 3);
    {
        float v[12];
        float lf = (float)(2047 - t8 * 8);
        asm volatile("" : "+v"(lf));
        const unsigned tb = (unsigned)(size_t)T - 17024u * 4u;
        if (wq < 3) {
#pragma unroll
            for (int e = 0; e < 12; e += 2) {
                const unsigned bits = pk_bf16(lf - (float)e, lf - (float)(e + 1));
                v[e] = *(const LAS float*)(size_t)(((bits & 0xffffu) << 2) + tb);
                v[e + 1] = *(const LAS float*)(size_t)(((bits >> 16) << 2) + tb);
            }
        } else {
#pragma unroll
            for (int e = 0; e < 12; ++e) {
                const int l = 2047 - (t8 * 8 + e);
                const int idx = l < 128 ? (l < 0 ? 0 : l) : (int)(pk_bf16((float)l, 0.f) & 0xffffu) - 17024;
                const float t = T[idx];
                v[e] = l < 0 ? 0.f : t;
            }
        }
        conv_put(lds, t8, (u32x4){pk_bf16(v[0], v[1]), pk_bf16(v[2], v[3]), pk_bf16(v[4], v[5]), pk_bf16(v[6], v[7])}, (u32x2){pk_bf16(v[8], v[9]), pk_bf16(v[10], v[11])});
        if (t8 < 4) conv_put(lds, 256 + t8, (u32x4){0u, 0u, 0u, 0u}, (u32x2){0u, 0u});
        if (t8 >= 64 && t8 < 94) *(LAS u32x4*)(lds + CV_U + (t8 - 64) * 16) = (u32x4){0u, 0u, 0u, 0u};
    }
    STAMP(a.ws, 4);
    {
        u32x4 gv[2];
        const rsrc_t rgn = mk_rsrc(a.ws + WS_GR, L * 8);
        const unsigned long long t0 = __builtin_amdgcn_s_memrealtime();
        for (;;) {
            gv[0] = bldu4<SC1>(rgn, tid * 16, 0);
            gv[1] = w < 4 ? bldu4<SC1>(rgn, tid * 16, 1536 * 8) : gv[0];
            const bool ok = gv[0][1] == tag && gv[0][3] == tag && gv[1][1] == tag && gv[1][3] == tag;
            if (__all(ok)) break;
            if (__builtin_amdgcn_s_memrealtime() - t0 > WAIT_TICKS) break;
            __builtin_amdgcn_s_sleep(PSLEEP);
            asm volatile("" ::: "memory");
        }
#pragma unroll
        for (int i = 0; i < 2; ++i) {
            if (i == 0 || w < 4) {
                const int row = 2 * tid + 1536 * i;
                u32x2 x0 = xr[2 * i], x1 = xr[2 * i + 1];
                if (b & 1) {
                    x0 = (u32x2){__builtin_amdgcn_alignbit(x0[1], x0[0], 16), x0[1] >> 16};
                    x1 = (u32x2){__builtin_amdgcn_alignbit(x1[1], x1[0], 16), x1[1] >> 16};
                }
                const float m0 = bf_lo(gv[i][0]), m1 = bf_lo(gv[i][2]);
                const float s0 = __builtin_amdgcn_rcpf(bf_hi(gv[i][0])), s1 = __builtin_amdgcn_rcpf(bf_hi(gv[i][2]));
#pragma unroll
                for (int c = 0; c < 3; ++c) {
                    const unsigned xp = c == 0 ? __builtin_amdgcn_perm(x1[0], x0[0], 0x05040100u) : c == 1 ? __builtin_amdgcn_perm(x1[0], x0[0], 0x07060302u)
                                                                                                            : __builtin_amdgcn_perm(x1[1], x0[1], 0x05040100u);
                    const unsigned dd = pk_bf16(bf_lo(xp) - m0, bf_hi(xp) - m1);
                    *(LAS unsigned*)(lds0 + c * CV_CH + CV_U + 480 + row * 2) = pk_bf16(bf_lo(dd) * s0, bf_hi(dd) * s1);
                    *(LAS unsigned*)(lds0 + c * CV_CH + CV_X + row * 2) = xp;
                }
            }
        }
    }
    LGKM0(); BAR();
    STAMP(a.ws, 5);
    unsigned fwv = tag;
    if (w == 11 && lane < 36) fwv = __hip_atomic_load((gu32*)(a.ws + WS_FW) + lane, RLX_AGENT);
    f32x4 acc[8];
#pragma unroll
    for (int c = 0; c < 8; ++c) acc[c] = (f32x4){0.f, 0.f, 0.f, 0.f};
    const int cpy = (3 - p) & 3;
#pragma unroll
    for (int rr = 0; rr < 2; ++rr) {
        const int r = 2 * wq + rr;
        const LAS char* ab = lds + cpy * CV_RC + 2 * (2031 - 32 * r + 8 * g - p - cpy) - 512 * 7;
        const LAS char* fb = lds + CV_U + 2 * (240 - 32 * r + 16 * p - 16 + 8 * g);
        bf16x8 A[8], Fm[8];
#pragma unroll
        for (int s = 0; s < 8; ++s) {
            const s16x4 lo = *(const volatile LAS s16x4*)(ab + 512 * (7 - s));
            const s16x4 hi = *(const volatile LAS s16x4*)(ab + 512 * (7 - s) + 8);
            A[s] = cat8(lo, hi);
        }
#pragma unroll
        for (int e = 0; e < 8; ++e) Fm[e] = *(const LAS bf16x8*)(fb + 512 * e);
#pragma unroll
        for (int c = 0; c < 8; ++c)
#pragma unroll
            for (int s = 0; s <= c; ++s) acc[c] = MFMA16(A[s], Fm[c - s], acc[c]);
    }
    STAMP(a.ws, 6);
    LAS f32x4* red = (LAS f32x4*)(lds + CV_RED);
#pragma unroll
    for (int c = 0; c < 8; ++c) {
        const int ow = c >> 1;
        if (ow != wq) red[(ow * 6 + (c & 1) * 3 + (wq > ow ? wq - 1 : wq)) * 64 + lane] = acc[c];
    }
    if (w == 11) { const bool okw = __all(fwv == tag); if (lane == 0) *(LAS unsigned*)(lds0 + LDS_FWOK) = okw ? 1u : 0u; }
    LGKM0(); BAR();
    const rsrc_t rh = mk_rsrc(a.ws + WS_HT + (size_t)ch * L * 2, L * 2);
    const float dsk = bf2f(r_ds);
#pragma unroll
    for (int i = 0; i < 2; ++i) {
        f32x4 y;
#pragma unroll
        for (int k = 0; k < 4; ++k) {
            const float y01 = (wq & 1) ? acc[2 + i][k] : acc[i][k], y23 = (wq & 1) ? acc[6 + i][k] : acc[4 + i][k];
            y[k] = (wq & 2) ? y23 : y01;
        }
#pragma unroll
        for (int k = 0; k < 3; ++k) y += red[(wq * 6 + i * 3 + k) * 64 + lane];
        const int l0 = 256 * (2 * wq + i) + 16 * p + 4 * g;
        const u32x2 uu = *(const LAS u32x2*)(lds + CV_U + 480 + 2 * l0), xv = *(const LAS u32x2*)(lds + CV_X + 2 * l0);
        float o[4];
#pragma unroll
        for (int k = 0; k < 4; ++k) {
            const float u = (k & 1) ? bf_hi(uu[k >> 1]) : bf_lo(uu[k >> 1]);
            const float x = (k & 1) ? bf_hi(xv[k >> 1]) : bf_lo(xv[k >> 1]);
            o[k] = x + __builtin_fmaf(u, dsk, y[k]);
        }
        bst8((u32x2){pk_bf16(o[0], o[1]), pk_bf16(o[2], o[3])}, rh, 2 * l0, 0);
    }
    STAMP(a.ws, 28);
    VM0(); BAR();
    if (w == 0) publish_rep(a.ws, WS_FH, b, tag, lane);
}

__device__ __forceinline__ int nb0_of(int j) { return (9 * j + 1) >> 1; }

constexpr int OUT_ROW = 176;
constexpr int KC_XB = 73728, KC_ST = KC_XB + 20 * 1024, KC_OUT = KC_ST + 512;
__device__ __forceinline__ void phase_c(char* __restrict__ ws, LAS char* lds, int panel, int j, unsigned tag STAMP_ARG) {
    const int tid = threadIdx.x, w = __builtin_amdgcn_readfirstlane(tid >> 6), lane = tid & 63;
    const int tok0 = panel * 64, nbs = nb0_of(j), nblk = nb0_of(j + 1) - nbs;
    const int g = lane >> 4, i16 = lane & 15, q = i16 >> 2, p = lane & 3;
    const int b = w >> 1, kh = w & 1;
    const bool act = w < 10 && b < nblk;
    const int nb = nbs + (act ? b : 0);
    if (w == 0 && *(const LAS unsigned*)(lds + LDS_FWOK) == 0u) wait_flags(ws, WS_FW, 0, 36, WS_FW, 0, 0, tag);
    BAR();
    bf16x8 W[9];
    f32x4 cs0, cs1;
    if (w < 10) {
        const rsrc_t rw = mk_rsrc(ws + WS_WF, WSUB * 2);
#pragma unroll
        for (int i = 0; i < 9; ++i) W[i] = bld16<SC1>(rw, lane * 16, ((kh * 9 + i) * 36 + nb) * 1024);
        const rsrc_t rc = mk_rsrc(ws + WS_CS, 2 * (D + F) * 4);
        cs0 = bldf4<SC1>(rc, g * 16, nb * 64); cs1 = bldf4<SC1>(rc, g * 16, nb * 64 + (D + F) * 4);
    }
    if (w == 11) wait_flags(ws, WS_FH, (blockIdx.x & (REP - 1)) * 256, 192, WS_FH, 0, 0, tag);
    BAR();
    STAMP(ws, 8);
    {
        const rsrc_t rh = mk_rsrc(ws + WS_HT, D * L * 2);
        const int fl = (lane >> 4) & 1;
        const int v0 = (lane >> 3) * 4096 + (((lane & 7) ^ (fl << 1)) * 16), v1 = (lane >> 3) * 4096 + (((lane & 7) ^ ((fl | 2) << 1)) * 16);
#pragma unroll
        for (int k = 0; k < 6; ++k) { const int idx = 6 * w + k; blds16<SC1>(rh, lds + idx * 1024, (k & 1) ? v1 : v0, idx * 32768 + tok0 * 2); }
    }
    VM0(); BAR();
    STAMP(ws, 9);
    const int f = ((q >> 1) & 1) | ((g & 1) << 1);
    const int lo_ = (8 * g + q) * 128 + 8 * (p & 1);
    f32x4 acc[4] = {};
    LAS f32x2* st = (LAS f32x2*)(lds + KC_ST);
    LAS f32x4* xb = (LAS f32x4*)(lds + KC_XB);
    if (w < 10) {
        const LAS char* hb[4];
#pragma unroll
        for (int mt = 0; mt < 4; ++mt) hb[mt] = lds + kh * 9 * 4096 + lo_ + (((2 * mt + (p >> 1)) ^ (f << 1)) * 16);
        bf16x8 H[2][2][4];
#pragma unroll
        for (int c = 0; c < 6; ++c) {
            if (c < 5) {
#pragma unroll
                for (int i = 0; i < 2; ++i)
                    if (2 * c + i < 9) {
#pragma unroll
                        for (int mt = 0; mt < 4; ++mt) H[c & 1][i][mt] = cat8(tr_read(hb[mt] + (2 * c + i) * 4096), tr_read(hb[mt] + (2 * c + i) * 4096 + 512));
                    }
            }
            __builtin_amdgcn_sched_barrier(0);
            if (c > 0) {
#pragma unroll
                for (int i = 0; i < 2; ++i)
                    if (2 * (c - 1) + i < 9) {
#pragma unroll
                        for (int mt = 0; mt < 4; ++mt) acc[mt] = MFMA16(W[2 * (c - 1) + i], H[(c - 1) & 1][i][mt], acc[mt]);
                    }
            }
            __builtin_amdgcn_sched_barrier(0);
        }
        xb[((w * 2 + 0) * 64) + lane] = sel4(kh, acc[0], acc[2]);
        xb[((w * 2 + 1) * 64) + lane] = sel4(kh, acc[1], acc[3]);
    } else {
        __builtin_amdgcn_s_setprio(3);
        const bf16x8 ones = {0x3f80, 0x3f80, 0x3f80, 0x3f80, 0x3f80, 0x3f80, 0x3f80, 0x3f80};
        const int m0 = 2 * (w - 10);
        const LAS char* h0 = lds + lo_ + (((2 * m0 + (p >> 1)) ^ (f << 1)) * 16);
        const LAS char* h1 = lds + lo_ + (((2 * m0 + 2 + (p >> 1)) ^ (f << 1)) * 16);
        bf16x8 H[2][3][2];
#pragma unroll
        for (int c = 0; c < 7; ++c) {
            if (c < 6) {
#pragma unroll
                for (int i = 0; i < 3; ++i) {
                    H[c & 1][i][0] = cat8(tr_read(h0 + (3 * c + i) * 4096), tr_read(h0 + (3 * c + i) * 4096 + 512));
                    H[c & 1][i][1] = cat8(tr_read(h1 + (3 * c + i) * 4096), tr_read(h1 + (3 * c + i) * 4096 + 512));
                }
            }
            __builtin_amdgcn_sched_barrier(0);
            if (c > 0) {
#pragma unroll
                for (int i = 0; i < 3; ++i) {
                    const bf16x8 H0 = H[(c - 1) & 1][i][0], H1 = H[(c - 1) & 1][i][1];
                    acc[0] = MFMA16(ones, H0, acc[0]); acc[1] = MFMA16(H0, H0, acc[1]);
                    acc[2] = MFMA16(ones, H1, acc[2]); acc[3] = MFMA16(H1, H1, acc[3]);
                }
            }
            __builtin_amdgcn_sched_barrier(0);
        }
        const float sm[2] = {acc[0][0], acc[2][0]};
        if (g == q) {
            const rsrc_t r2 = mk_rsrc(ws + WS_ST2, L * 8);
#pragma unroll
            for (int t = 0; t < 2; ++t) {
                const f32x4 d = acc[2 * t + 1];
                const float s2 = p == 0 ? d[0] : p == 1 ? d[1] : p == 2 ? d[2] : d[3];
                const float mean = sm[t] * (1.0f / D), var = fmaxf(s2 * (1.0f / D) - mean * mean, 0.f);
                const float mb = rbf(mean), rs = __builtin_amdgcn_rcpf(ln_sd(var));
                const float m2 = rbf((mean - mb) * rs), rs2 = __builtin_amdgcn_rcpf(ln_sd(var * rs * rs));
                st[(m0 + t) * 16 + i16] = (f32x2){rs * rs2, (mb * rs + m2) * rs2};
                if (j == 0) bst8((u32x2){__float_as_uint(mb), __float_as_uint(rs)}, r2, (tok0 + (m0 + t) * 16 + i16) * 8, 0);
            }
        }
    }
    __builtin_amdgcn_s_setprio(0);
    STAMP(ws, 24);
    LGKM0(); BAR();
    STAMP(ws, 10);
    const rsrc_t rg = mk_rsrc(ws + WS_G, (L + 4) * D * 2);
    LAS char* og = lds + KC_OUT;
    if (act) {
        const f32x4 cs = cs0 + cs1;
#pragma unroll
        for (int t = 0; t < 2; ++t) {
            const f32x4 a = sel4(kh, acc[2 + t], acc[t]) + xb[((w ^ 1) * 2 + t) * 64 + lane];
            const f32x2 ab = st[(2 * kh + t) * 16 + i16];
            float o[4];
#pragma unroll
            for (int k = 0; k < 4; ++k) o[k] = gelu_tanh(ab[0] * a[k] - ab[1] * cs[k]);
            *(LAS u32x2*)(og + (kh * 32 + t * 16 + i16) * OUT_ROW + b * 32 + g * 8) = (u32x2){pk_bf16(o[0], o[1]), pk_bf16(o[2], o[3])};
        }
    }
    LGKM0(); BAR();
    {
        const int nc = 2 * nblk, row = nblk == 5 ? tid / 10 : tid >> 3, c = tid - row * nc;
        if (row < 64) bst16(*(const LAS u32x4*)(og + row * OUT_ROW + c * 16), rg, row * (D * 2) + c * 16, ((2 + tok0) * D + nbs * 16) * 2);
        if ((panel == 0 || panel == 31) && tid >= 704 && tid < 704 + 2 * nc) {
            const int z = tid - 704, zr = z >= nc ? 1 : 0;
            bst16((u32x4){0u, 0u, 0u, 0u}, rg, zr * (D * 2) + (z - zr * nc) * 16, ((panel == 0 ? 0 : L + 2) * D + nbs * 16) * 2);
        }
    }
    STAMP(ws, 27);
    VM0(); BAR();
    publish(ws, WS_FG, panel * 32 + j, tag);
}

constexpr int KD_HT = 73728, KD_XB = KD_HT + 10240, KD_OUT = KD_XB + 20 * 1024;
__device__ __forceinline__ void phase_d(char* __restrict__ ws, LAS char* lds, int panel, int j, int je, unsigned tag STAMP_ARG) {
    const int tid = threadIdx.x, w = __builtin_amdgcn_readfirstlane(tid >> 6), lane = tid & 63;
    const int tok0 = panel * 64, nbs = nb0_of(j), nblk = nb0_of(j + 1) - nbs;
    const int g = lane >> 4, i16 = lane & 15, q = i16 >> 2, p = lane & 3;
    const int b = w >> 1, kh = w & 1;
    const bool act = w < 10 && b < nblk;
    const int nb = nbs + (act ? b : 0);
    bf16x8 W[9];
    if (w < 10) {
        const rsrc_t rw = mk_rsrc(ws + WS_WF + WSUB * 2, WSUB * 2);
#pragma unroll
        for (int i = 0; i < 9; ++i) W[i] = bld16<SC1>(rw, lane * 16, ((kh * 9 + i) * 36 + nb) * 1024);
    }
    if (w < 2 * nblk) {
        const rsrc_t rh = mk_rsrc(ws + WS_HT, D * L * 2);
        const int fl = ((lane >> 4) & 1) | ((w & 1) << 1);
        blds16<SC1>(rh, lds + KD_HT + w * 1024, (lane >> 3) * 4096 + (((lane & 7) ^ (fl << 1)) * 16), (nbs * 16 + w * 8) * 4096 + tok0 * 2);
    }
    if (w == 10) wait_flags(ws, WS_FW, (2 + 2 * je) * 18, 18, WS_FW, 0, 0, tag);
    if (w == 11) {
        const int p0 = panel > 0 ? panel - 1 : 0, p1 = panel < 31 ? panel + 1 : 31;
        wait_panels(ws, WS_FG, p0, WS_FG, panel, WS_FG, p1, tag);
    }
    BAR();
    STAMP(ws, 12);
    f32x2 st[2];
    {
        const rsrc_t rg = mk_rsrc(ws + WS_G, (L + 4) * D * 2);
        const int c7 = (lane & 7) ^ ((lane >> 3) & 6), vb = (lane >> 3) * 1152 + c7 * 16;
#pragma unroll
        for (int k = 0; k < 6; ++k) {
            const int idx = 6 * w + k, cg = idx >> 3, c = cg * 8 + c7;
            const int o2 = (0x4031 >> (((c * 57) >> 10) * 4)) & 15;
            blds16<SC1>(rg, lds + idx * 1024, vb + o2 * 1152, (tok0 + (idx & 7) * 8) * 1152 + cg * 128);
        }
        const rsrc_t r2 = mk_rsrc(ws + WS_ST2, L * 8);
#pragma unroll
        for (int t = 0; t < 2; ++t) st[t] = __builtin_bit_cast(f32x2, bldu2<SC1>(r2, i16 * 8, (tok0 + kh * 32 + t * 16) * 8));
    }
    VM0();
    asm volatile("" : "+v"(st[0]), "+v"(st[1]));
    BAR();
    STAMP(ws, 13);
    f32x4 acc[4] = {};
    LAS f32x4* xb = (LAS f32x4*)(lds + KD_XB);
    if (w < 10) {
        const int z = i16 & 6;
        const LAS char* a0 = lds + i16 * 128 + ((g ^ z) * 16), * a1 = lds + i16 * 128 + (((4 + g) ^ z) * 16);
        const LAS char* e0 = kh ? a1 + 4 * 8192 : a0;
        const LAS char* e1 = kh ? a0 + 5 * 8192 : a1;
        bf16x8 A[2][2][4];
#pragma unroll
        for (int c = 0; c < 6; ++c) {
            if (c < 5) {
#pragma unroll
                for (int ii = 0; ii < 2; ++ii) {
                    const int i = 2 * c + ii;
                    if (i < 9) {
#pragma unroll
                        for (int mt = 0; mt < 4; ++mt) A[c & 1][ii][mt] = *(const LAS bf16x8*)(((i & 1) ? e1 : e0) + (i >> 1) * 8192 + mt * 2048);
                    }
                }
            }
            __builtin_amdgcn_sched_barrier(0);
            if (c > 0) {
#pragma unroll
                for (int ii = 0; ii < 2; ++ii)
                    if (2 * (c - 1) + ii < 9) {
#pragma unroll
                        for (int mt = 0; mt < 4; ++mt) acc[mt] = MFMA16(W[2 * (c - 1) + ii], A[(c - 1) & 1][ii][mt], acc[mt]);
                    }
            }
            __builtin_amdgcn_sched_barrier(0);
        }
        xb[((w * 2 + 0) * 64) + lane] = sel4(kh, acc[0], acc[2]);
        xb[((w * 2 + 1) * 64) + lane] = sel4(kh, acc[1], acc[3]);
    }
    STAMP(ws, 25);
    LGKM0(); BAR();
    STAMP(ws, 14);
    LAS char* ox = lds + KD_OUT;
    if (w < 10) {
        const int bl = act ? b : 0;
        const int hrow = 16 * bl + 4 * g + q;
        const LAS char* hb = lds + KD_HT + hrow * 128 + 8 * (p & 1);
        const rsrc_t rp = mk_rsrc(ws + WS_PST, 32 * 36 * 64 * 8);
#pragma unroll
        for (int t = 0; t < 2; ++t) {
            const int mt = 2 * kh + t;
            const f32x4 a = sel4(kh, acc[2 + t], acc[t]) + xb[((w ^ 1) * 2 + t) * 64 + lane];
            const u32x2 hu = __builtin_bit_cast(u32x2, tr_read(hb + (((2 * mt + (p >> 1)) ^ (fsw(hrow) << 1)) * 16)));
            const float rs = st[t][1], nm = -st[t][0] * rs;
            u32x2 ov;
#pragma unroll
            for (int k = 0; k < 2; ++k)
                ov[k] = pk_bf16(__builtin_fmaf(bf_lo(hu[k]), rs, nm) + a[2 * k], __builtin_fmaf(bf_hi(hu[k]), rs, nm) + a[2 * k + 1]);
            float s1 = dot2bf(ov[1], 0x3f803f80u, dot2bf(ov[0], 0x3f803f80u, 0.f)), s2 = dot2bf(ov[1], ov[1], dot2bf(ov[0], ov[0], 0.f));
            s1 = rsum_g(s1); s2 = rsum_g(s2);
            if (act) {
                *(LAS u32x2*)(ox + (mt * 16 + i16) * OUT_ROW + b * 32 + g * 8) = ov;
                *(LAS u32x2*)(ox + 64 * OUT_ROW + (mt * 16 + i16) * OUT_ROW + b * 32 + g * 8) = hu;
                if (lane < 16) bst8((u32x2){__float_as_uint(s1), __float_as_uint(s2)}, rp, lane * 8, ((panel * 36 + nb) * 64 + mt * 16) * 8);
            }
        }
    }
    LGKM0(); BAR();
    {
        const int nc = 2 * nblk, row = nblk == 5 ? tid / 10 : tid >> 3, c = tid - row * nc;
        if (row < 64) {
            const int so = (tok0 * D + nbs * 16) * 2, vo = row * (D * 2) + c * 16;
            bst16(*(const LAS u32x4*)(ox + row * OUT_ROW + c * 16), mk_rsrc(ws + WS_X1, L * D * 2), vo, so);
            bst16(*(const LAS u32x4*)(ox + 64 * OUT_ROW + row * OUT_ROW + c * 16), mk_rsrc(ws + WS_HR, L * D * 2), vo, so);
        }
    }
    STAMP(ws, 26);
    VM0(); BAR();
    publish(ws, WS_FX, panel * 32 + j, tag);
}

constexpr int BG_LDS = 131072;
__device__ __forceinline__ void phase_cd_background(const Args& a, LAS char* lds, int panel, int job, unsigned tag STAMP_ARG) {
    char* ws = a.ws;
    const int tid = threadIdx.x, w = __builtin_amdgcn_readfirstlane(tid >> 6), lane = tid & 63, tok0 = panel * 64;
    BAR(); BAR();
    {
        const rsrc_t rh = mk_rsrc(ws + WS_HT, D * L * 2);
        const int fl = (lane >> 4) & 1;
        const int v0 = (lane >> 3) * 4096 + (((lane & 7) ^ (fl << 1)) * 16), v1 = (lane >> 3) * 4096 + (((lane & 7) ^ ((fl | 2) << 1)) * 16);
#pragma unroll
        for (int k = 0; k < 6; ++k) { const int idx = 6 * w + k; blds16<SC1>(rh, lds + idx * 1024, (k & 1) ? v1 : v0, idx * 32768 + tok0 * 2); }
    }
    VM0(); BAR();
    PrepRegs P;
    prep_issue(a, job, lane, P);
    BAR(); BAR(); BAR();
    prep_finish<false, false>(a, job, lds + BG_LDS + (w - 8) * 8192, lane, tag, P STAMP_PASS);
    BAR();
    {
        const rsrc_t rg = mk_rsrc(ws + WS_G, (L + 4) * D * 2);
        const int c7 = (lane & 7) ^ ((lane >> 3) & 6), vb = (lane >> 3) * 1152 + c7 * 16;
#pragma unroll
        for (int k = 0; k < 6; ++k) {
            const int idx = 6 * w + k, cg = idx >> 3, c = cg * 8 + c7;
            const int o2 = (0x4031 >> (((c * 57) >> 10) * 4)) & 15;
            blds16<SC1>(rg, lds + idx * 1024, vb + o2 * 1152, (tok0 + (idx & 7) * 8) * 1152 + cg * 128);
        }
    }
    VM0();
    if (lane == 0) __hip_atomic_store((gu32*)(ws + WS_FW) + job, tag, RLX_AGENT);
    BAR(); BAR(); BAR(); BAR();
}

constexpr int KE_HID = 73728, KE_ST = 2 * 73728, KE_FLG = KE_ST + 1024;
#define KE_PRIO 1
#define KE_A1 1
#define KE_NB0 3
#define KE_NB1 3
#define KE_NB2 3
#define KE_R0 4
#define KE_R1 4
#define KE_R2 4
#define STG 2
#define STW 11
#define E_STAGED 0
template <int NB, int RING, int GRP>
__device__ __forceinline__ void e_body(char* __restrict__ ws, LAS char* lds, int panel, int j, unsigned tag, int w, int lane STAMP_ARG) {
    constexpr int S1 = 2 * KE_NB0, S2 = 2 * (KE_NB0 + KE_NB1);
    const int tok0 = panel * 64, g = lane >> 4, i16 = lane & 15;
    const int nbs = (GRP == 0 ? 0 : GRP == 1 ? 4 * KE_NB0 : 4 * (KE_NB0 + KE_NB1)) + NB * (w & 3);
    const rsrc_t rw = mk_rsrc(ws + WS_WF + (size_t)(2 + 2 * j) * WSUB * 2, 2 * WSUB * 2);
    const int wv = lane * 16, wsb = nbs * 1024;
    bf16x8 W[RING][NB];
#pragma unroll
    for (int s = 0; s < RING; ++s)
#pragma unroll
        for (int b = 0; b < NB; ++b) W[s][b] = bld16<SC1>(rw, wv + b * 1024, wsb + s * 36864);
    f32x4 acc[NB][4];
    {
        const rsrc_t rc = mk_rsrc(ws + WS_CS, 2 * (D + F) * 4);
#pragma unroll
        for (int b = 0; b < NB; ++b) {
            acc[b][0] = bldf4<SC1>(rc, g * 16 + b * 64, (D + j * D + nbs * 16) * 4);
            acc[b][1] = bldf4<SC1>(rc, g * 16 + b * 64, (D + j * D + nbs * 16 + D + F) * 4);
        }
    }
    __builtin_amdgcn_sched_barrier(0);
    unsigned fw4 = tag;
    if (GRP == 2) {
        if (w == 10 && lane < 18) fw4 = __hip_atomic_load((gu32*)(ws + WS_FW) + (3 + 2 * j) * 18 + lane, RLX_AGENT);
        if (w == 11) wait_panels(ws, WS_FX, panel, WS_FX, panel, WS_FX, panel, tag);
    }
    BAR();
    STAMP(ws, 16);
    u32x2 pv[36];
    if (GRP == STG && w == STW) {
        const rsrc_t rp = mk_rsrc(ws + WS_PST + (size_t)panel * 36 * 512, 36 * 512);
#pragma unroll
        for (int k = 0; k < 36; ++k) pv[k] = bldu2<SC1>(rp, lane * 8, k * 512);
    }
    {
        const rsrc_t rx = mk_rsrc(ws + WS_X1 + (size_t)tok0 * D * 2, 64 * D * 2);
        const int vo = (lane >> 3) * 1152 + (((lane & 7) ^ ((lane >> 3) & 6)) * 16);
#pragma unroll
        for (int k = 0; k < 6; ++k) {
            const int idx = E_STAGED ? w + 12 * k : 6 * w + k;
            blds16<SC1>(rx, lds + idx * 1024, vo, (idx & 7) * 9216 + (idx >> 3) * 128);
        }
    }
    __builtin_amdgcn_sched_barrier(0);
    LAS f32x4* st = (LAS f32x4*)(lds + KE_ST);
    LAS unsigned* flg = (LAS unsigned*)(lds + KE_FLG);
    if (GRP == STG && w == STW) {
        float a1[4] = {}, a2[4] = {};
#pragma unroll
        for (int k = 0; k < 36; ++k) { a1[k & 3] += __uint_as_float(pv[k][0]); a2[k & 3] += __uint_as_float(pv[k][1]); }
        const float s1 = (a1[0] + a1[1]) + (a1[2] + a1[3]), s2 = (a2[0] + a2[1]) + (a2[2] + a2[3]);
        const float mean = s1 * (1.0f / D), var = fmaxf(s2 * (1.0f / D) - mean * mean, 0.f);
        const float mb = rbf(mean), sb = ln_sd(var), al = __builtin_amdgcn_rcpf(sb);
        const float k0 = -2.0f * 0.7978845608028654f * 1.4426950408889634f;
        st[lane] = (f32x4){k0 * al, k0 * 0.044715f * al * al * al, sb, -mb};
        if (lane < 3) flg[lane] = 0u;
        if (lane == 4 || lane == 5) flg[lane] = 0u;
    }
    if (GRP == 2 && w == 10) { const bool ok4 = __all(fw4 == tag); if (lane == 0) flg[3] = ok4 ? 1u : 0u; }
    if (E_STAGED) asm volatile("s_waitcnt vmcnt(4)" ::: "memory"); else VM0();
    LGKM0(); BAR();
    STAMP(ws, 17);
    const int z = i16 & 6;
    const int o0 = i16 * 128 + (g ^ z) * 16, o1 = i16 * 128 + ((4 + g) ^ z) * 16;
    {
        float nm[4];
#pragma unroll
        for (int mt = 0; mt < 4; ++mt) nm[mt] = st[mt * 16 + i16][3];
#pragma unroll
        for (int b = 0; b < NB; ++b) {
            const f32x4 cs = acc[b][0] + acc[b][1];
#pragma unroll
            for (int mt = 0; mt < 4; ++mt) acc[b][mt] = cs * nm[mt];
        }
    }
#define E_SPIN(cond) do { while (cond) __builtin_amdgcn_s_sleep(1); __builtin_amdgcn_sched_barrier(0); asm volatile("" ::: "memory"); } while (0)
#define E_FLG(i) __hip_atomic_load(flg + (i), __ATOMIC_RELAXED, __HIP_MEMORY_SCOPE_WORKGROUP)
    constexpr bool ADB = !(NB >= 4 && KE_A1);
    bf16x8 A[2][4];
#pragma unroll
    for (int ph = 0; ph < 2; ++ph) {
        const LAS char* ab = lds + ph * KE_HID;
        if (ph == 1) E_SPIN(E_FLG(0) < 4u);
        if (ADB) {
#pragma unroll
            for (int mt = 0; mt < 4; ++mt) A[0][mt] = *(const LAS bf16x8*)(ab + o0 + mt * 2048);
        }
#pragma unroll
        for (int s = 0; s < 18; ++s) {
            if (ph == 1 && s + (ADB ? 1 : 0) == S1) E_SPIN(E_FLG(1) < 4u);
            if (ph == 1 && s + (ADB ? 1 : 0) == S2) E_SPIN(E_FLG(2) < 4u);
            if (GRP == 2 && ph == 0 && s == 2 && w == 8 && E_FLG(3) == 0u) {
                wait_flags(ws, WS_FW, (3 + 2 * j) * 18, 18, WS_FW, 0, 0, tag);
                if (lane == 0) __hip_atomic_store(flg + 3, 1u, __ATOMIC_RELAXED, __HIP_MEMORY_SCOPE_WORKGROUP);
            }
            if (ph == 0 && s == 18 - RING) E_SPIN(E_FLG(3) == 0u);
            if (E_STAGED && ph == 0) {
                if (s == 1) { asm volatile("s_waitcnt vmcnt(%0)" :: "n"(2 + NB) : "memory"); if (lane == 0) __hip_atomic_fetch_add(flg + 4, 1u, __ATOMIC_RELAXED, __HIP_MEMORY_SCOPE_WORKGROUP); }
                if (s == 3) { asm volatile("s_waitcnt vmcnt(%0)" :: "n"(3 * NB) : "memory"); if (lane == 0) __hip_atomic_fetch_add(flg + 5, 1u, __ATOMIC_RELAXED, __HIP_MEMORY_SCOPE_WORKGROUP); }
                if (s == 5) E_SPIN(E_FLG(4) < 12u);
                if (s == 11) E_SPIN(E_FLG(5) < 12u);
            }
            if (!ADB) {
#pragma unroll
                for (int mt = 0; mt < 4; ++mt) A[0][mt] = *(const LAS bf16x8*)(ab + ((s & 1) ? o1 : o0) + mt * 2048 + (s >> 1) * 8192);
            } else if (s + 1 < 18) {
#pragma unroll
                for (int mt = 0; mt < 4; ++mt) A[(s + 1) & 1][mt] = *(const LAS bf16x8*)(ab + (((s + 1) & 1) ? o1 : o0) + mt * 2048 + ((s + 1) >> 1) * 8192);
            }
            const int t = ph * 18 + s;
#pragma unroll
            for (int b = 0; b < NB; ++b) {
#pragma unroll
                for (int mt = 0; mt < 4; ++mt) acc[b][mt] = MFMA16(W[t % RING][b], A[ADB ? (s & 1) : 0][mt], acc[b][mt]);
                if (t + RING < 36) W[t % RING][b] = bld16<WAUX>(rw, wv + b * 1024, wsb + (t + RING) * 36864);
            }
            __builtin_amdgcn_sched_barrier(0);
        }
        if (ph == 0) {
            STAMP(ws, 18);
#pragma unroll
            for (int mt = 0; mt < 4; ++mt) {
                const f32x4 k = st[mt * 16 + i16];
#pragma unroll
                for (int b = 0; b < NB; ++b) {
                    float o[4];
#pragma unroll
                    for (int e = 0; e < 4; ++e) {
                        const float a = acc[b][mt][e];
                        const float ex = __builtin_amdgcn_exp2f(a * (k[0] + k[1] * (a * a)));
                        o[e] = a * __builtin_amdgcn_rcpf(k[2] + k[2] * ex);
                    }
                    const int nb = nbs + b;
                    *(LAS u32x2*)(lds + KE_HID + (nb >> 2) * 8192 + (mt * 16 + i16) * 128 + (((2 * (nb & 3) + (g >> 1)) ^ z) * 16) + 8 * (g & 1)) =
                        (u32x2){pk_bf16(o[0], o[1]), pk_bf16(o[2], o[3])};
                    acc[b][mt] = (f32x4){0.f, 0.f, 0.f, 0.f};
                }
            }
            LGKM0();
            if (lane == 0) __hip_atomic_fetch_add(flg + GRP, 1u, __ATOMIC_RELAXED, __HIP_MEMORY_SCOPE_WORKGROUP);
            STAMP(ws, 19);
            if (GRP == 2) __builtin_amdgcn_s_setprio(2); else if (GRP == 1) __builtin_amdgcn_s_setprio(1);
        }
    }
#undef E_SPIN
#undef E_FLG
    __builtin_amdgcn_s_setprio(0);
    STAMP(ws, 20);
    const rsrc_t rp = mk_rsrc(ws + WS_PART + (size_t)(panel * 8 + j) * 72 * 1024, 72 * 1024);
#pragma unroll
    for (int b = 0; b < NB; ++b)
#pragma unroll
        for (int m2 = 0; m2 < 2; ++m2) {
            const f32x4 a0 = acc[b][2 * m2], a1 = acc[b][2 * m2 + 1];
            const u32x4 v = {pk_bf16(a0[0], a0[1]), pk_bf16(a0[2], a0[3]), pk_bf16(a1[0], a1[1]), pk_bf16(a1[2], a1[3])};
            const int pr = (nbs + b) * 2 + m2;
            if (pr >= 9 * j && pr < 9 * j + 9) *(LAS u32x4*)(lds + (pr - 9 * j) * 1024 + lane * 16) = v;
            else bst16(v, rp, lane * 16, pr * 1024);
        }
}
__device__ __forceinline__ void phase_e(char* __restrict__ ws, LAS char* lds, int panel, int j, unsigned tag STAMP_ARG) {
    const int w = __builtin_amdgcn_readfirstlane(threadIdx.x >> 6), lane = threadIdx.x & 63;
    if (w < 4) e_body<KE_NB0, KE_R0, 0>(ws, lds, panel, j, tag, w, lane STAMP_PASS);
    else if (w < 8) e_body<KE_NB1, KE_R1, 1>(ws, lds, panel, j, tag, w, lane STAMP_PASS);
    else e_body<KE_NB2, KE_R2, 2>(ws, lds, panel, j, tag, w, lane STAMP_PASS);
    LGKM0();
    VM0(); BAR();
    publish(ws, WS_FP, panel * 32 + j, tag);
}

__device__ __forceinline__ void phase_f(char* __restrict__ ws, LAS char* lds, u16* __restrict__ out, int panel, int jj, unsigned tag STAMP_ARG) {
    const int tid = threadIdx.x, w = __builtin_amdgcn_readfirstlane(tid >> 6), lane = tid & 63;
    const int g = lane >> 4, i16 = lane & 15;
    const int pr = jj * 9 + (w < 9 ? w : 0), nb = pr >> 1, m2 = pr & 1;
    u32x2 xa[2], ha[2];
    const int so = ((panel * 64 + m2 * 32) * D + nb * 16) * 2, vo = (i16 * D + 4 * g) * 2;
    if (w < 9) {
        const rsrc_t r1 = mk_rsrc(ws + WS_X1, L * D * 2), rr = mk_rsrc(ws + WS_HR, L * D * 2);
#pragma unroll
        for (int t = 0; t < 2; ++t) { xa[t] = bldu2<SC1>(r1, vo, so + t * 16 * D * 2); ha[t] = bldu2<SC1>(rr, vo, so + t * 16 * D * 2); }
    }
    if (w == 11) wait_panels(ws, WS_FP, panel, WS_FP, panel, WS_FP, panel, tag);
    BAR();
    STAMP(ws, 22);
    if (w < 9) {
        const rsrc_t rp = mk_rsrc(ws + WS_PART + (size_t)(panel * 8) * 72 * 1024, 8 * 72 * 1024);
        u32x4 v[8];
#pragma unroll
        for (int k = 0; k < 7; ++k) v[k] = bldu4<SC1>(rp, lane * 16, ((k < jj ? k : k + 1) * 72 + pr) * 1024);
        v[7] = *(const LAS u32x4*)(lds + w * 1024 + lane * 16);
        float s[8] = {};
#pragma unroll
        for (int k = 0; k < 8; ++k)
#pragma unroll
            for (int e = 0; e < 4; ++e) { s[2 * e] += bf_lo(v[k][e]); s[2 * e + 1] += bf_hi(v[k][e]); }
#pragma unroll
        for (int t = 0; t < 2; ++t) {
            float o[4];
#pragma unroll
            for (int k = 0; k < 4; ++k) {
                const float x1 = (k & 1) ? bf_hi(xa[t][k >> 1]) : bf_lo(xa[t][k >> 1]);
                const float h = (k & 1) ? bf_hi(ha[t][k >> 1]) : bf_lo(ha[t][k >> 1]);
                o[k] = h + (x1 + s[4 * t + k]);
            }
            __builtin_amdgcn_raw_buffer_store_b64((u32x2){pk_bf16(o[0], o[1]), pk_bf16(o[2], o[3])}, mk_rsrc(out, L * D * 2), vo, so + t * 16 * D * 2, OAUX);
        }
    }
}

constexpr int LDS_CTRL = 3 * CV_CH > KE_FLG + 32 ? 3 * CV_CH : KE_FLG + 32;
constexpr int LDS_BYTES = LDS_CTRL + 64;

NOPK __global__ __launch_bounds__(NTHR) void mega_kernel(Args a) {
    extern __shared__ __attribute__((aligned(16))) char smem[];
    LAS char* lds = (LAS char*)smem;
    const int tid = threadIdx.x, w = __builtin_amdgcn_readfirstlane(tid >> 6), lane = tid & 63, b = blockIdx.x;
    STAMP_DECL
    STAMP(a.ws, 0);
    const unsigned tagv = __hip_atomic_load((gu32*)(a.ws + WS_EPOCH), RLX_AGENT);
    const StatRow sr = row_stats_load(a, w, lane);
    __builtin_amdgcn_sched_barrier(0);
    unsigned tag;
    if (b < 192) phase_conv(a, lds, tagv, sr, tag STAMP_PASS);
    else {
        tag = __builtin_amdgcn_readfirstlane(tagv) + 1u;
        if (tid == 0) __hip_atomic_store((gu32*)(a.ws + WS_FA) + b, tag, RLX_AGENT);
        if (tid == 0) *(LAS unsigned*)(lds + LDS_FWOK) = 0u;
        row_stats(a, sr, tag, w, lane);
        LGKM0(); BAR();
        STAMP(a.ws, 1);
        const int i = w * 64 + (b - 192);
        if (i >= 36) {
            const unsigned long long t0 = __builtin_amdgcn_s_memrealtime();
            while (__builtin_amdgcn_s_memrealtime() - t0 < PREP_DELAY) __builtin_amdgcn_s_sleep(8);
        }
        if (i < 180) {
            const int job = i < 36 ? i : (2 + 2 * ((i - 36) / 18)) * 18 + (i - 36) % 18;
            PrepRegs P;
            prep_issue(a, job, lane, P);
            prep_finish<true, true>(a, job, lds + w * 8192, lane, tag, P STAMP_PASS);
        }
        BAR();
    }
    STAMP(a.ws, 7);
    const int je = 2 * (b & 3) + ((b >> 3) & 1);
    {
        const int panel = (b & 7) * 4 + (b >> 6), j = (b >> 3) & 7;
        const int m = (w - 8) * 128 + ((b & 7) | ((b >> 4) << 3));
        if ((j & 1) && (w == 8 || w == 9) && m < 144) phase_cd_background(a, lds, panel, (3 + 2 * (m / 18)) * 18 + m % 18, tag STAMP_PASS);
        else {
            phase_c(a.ws, lds, panel, j, tag STAMP_PASS);
            STAMP(a.ws, 11);
            phase_d(a.ws, lds, panel, j, je, tag STAMP_PASS);
        }
    }
    STAMP(a.ws, 15);
    phase_e(a.ws, lds, ((b >> 2) & 1) * 16 + (b >> 4), je, tag STAMP_PASS);
    STAMP(a.ws, 21);
    phase_f(a.ws, lds, a.out, ((b >> 2) & 1) * 16 + (b >> 4), je, tag STAMP_PASS);
    if (b == 0 && w == 10) {
        wait_flags(a.ws, WS_FA, 0, 256, WS_FA, 0, 0, tag);
        if (lane == 0) __hip_atomic_store((gu32*)(a.ws + WS_EPOCH), tag, RLX_AGENT);
    }
    STAMP(a.ws, 23);
    STAMP_FLUSH(a.ws);
}

extern "C" void kernel_launch(void* const* d_in, const int* in_sizes, int n_in,
                              void* d_out, int out_size, void* d_ws, size_t ws_size,
                              hipStream_t stream) {
    Args a;
    a.x = (const u16*)d_in[0];
    a.are = (const u16*)d_in[1]; a.aim = (const u16*)d_in[2];
    a.bre = (const u16*)d_in[3]; a.bim = (const u16*)d_in[4];
    a.cre = (const u16*)d_in[5]; a.cim = (const u16*)d_in[6];
    a.logdt = (const u16*)d_in[7]; a.dskip = (const u16*)d_in[8];
    a.w1 = (const u16*)d_in[10]; a.w2 = (const u16*)d_in[11];
    a.w3 = (const u16*)d_in[12]; a.w4 = (const u16*)d_in[13];
    a.ws = (char*)d_ws; a.out = (u16*)d_out;
    (void)hipFuncSetAttribute((const void*)mega_kernel, hipFuncAttributeMaxDynamicSharedMemorySize, LDS_BYTES);
    mega_kernel<<<NWG, NTHR, LDS_BYTES, stream>>>(a);
}
```

```cpp
#include <hip/hip_runtime.h>
#include <hip/hip_bf16.h>
#include <stdint.h>

typedef unsigned short u16;
typedef short bf16x8 __attribute__((ext_vector_type(8)));
typedef short s16x4 __attribute__((ext_vector_type(4)));
typedef float f32x2 __attribute__((ext_vector_type(2)));
typedef float f32x4 __attribute__((ext_vector_type(4)));
typedef float f32x16 __attribute__((ext_vector_type(16)));
typedef unsigned u32x2 __attribute__((ext_vector_type(2)));
typedef unsigned u32x4 __attribute__((ext_vector_type(4)));
typedef __bf16 bf16x2_t __attribute__((ext_vector_type(2)));
typedef _Float16 f16x2 __attribute__((ext_vector_type(2)));
typedef _Float16 f16x8 __attribute__((ext_vector_type(8)));
#define LAS __attribute__((address_space(3)))
#define GAS __attribute__((address_space(1)))
#if defined(__HIP_DEVICE_COMPILE__)
#define NOPK __attribute__((target("no-packed-fp32-ops")))
#else
#define NOPK
#endif

constexpr int L = 2048, D = 576, NS = 128, F = 4608;
constexpr int WSUB = D * D;
constexpr float LN_EPS = 1e-5f;
constexpr int NWG = 256, NTHR = 768;
constexpr int SC1 = 16;

constexpr size_t WS_EPOCH = 0;
constexpr int REP = 8;
constexpr size_t WS_FA = 256;
constexpr size_t WS_FW = WS_FA + 1024;
constexpr size_t WS_FG = WS_FW + 2048;
constexpr size_t WS_FX = WS_FG + 4096;
constexpr size_t WS_FP = WS_FX + 4096;
constexpr size_t WS_FH = WS_FP + 4096;
constexpr size_t WS_GR = 32768;
constexpr size_t WS_HT = WS_GR + (size_t)L * 8;
constexpr size_t WS_WF = WS_HT + (size_t)D * L * 2;
constexpr size_t WS_CS = WS_WF + (size_t)18 * WSUB * 2;
constexpr size_t WS_ST2 = WS_CS + (size_t)2 * (D + F) * 4;
constexpr size_t WS_G = WS_ST2 + (size_t)L * 8;
constexpr size_t WS_X1 = WS_G + (size_t)(L + 4) * D * 2;
constexpr size_t WS_HR = WS_X1 + (size_t)L * D * 2;
constexpr size_t WS_PST = WS_HR + (size_t)L * D * 2;
constexpr size_t WS_PART = WS_PST + (size_t)32 * 36 * 64 * 8;
constexpr size_t WS_END = WS_PART + (size_t)32 * 8 * 72 * 512 * 2;

#define STAMP(ws, i) do {} while (0)
#define STAMP_DECL
#define STAMP_ARG
#define STAMP_PASS
#define STAMP_FLUSH(ws) do {} while (0)

__device__ __forceinline__ unsigned pk_bf16(float lo, float hi) {
    f32x2 v = {lo, hi};
    return __builtin_bit_cast(unsigned, __builtin_convertvector(v, bf16x2_t));
}
__device__ __forceinline__ float bf_lo(unsigned p) { return __uint_as_float(p << 16); }
__device__ __forceinline__ float bf_hi(unsigned p) { return __uint_as_float(p & 0xffff0000u); }
__device__ __forceinline__ float bf2f(u16 x) { return __uint_as_float((unsigned)x << 16); }
__device__ __forceinline__ float rbf(float x) { return bf_lo(pk_bf16(x, x)); }
__device__ __forceinline__ float dot2bf(unsigned a, unsigned b, float c) {
    return __builtin_amdgcn_fdot2_f32_bf16(__builtin_bit_cast(bf16x2_t, a), __builtin_bit_cast(bf16x2_t, b), c, false);
}
__device__ __forceinline__ float gelu_tanh(float x) {
    const float c0 = -2.0f * 0.7978845608028654f * 1.4426950408889634f;
    const float c1 = c0 * 0.044715f;
    float e = __builtin_amdgcn_exp2f(x * (c0 + c1 * x * x));
    return x * __builtin_amdgcn_rcpf(1.0f + e);
}
__device__ __forceinline__ s16x4 tr_read(const LAS char* p) {
    return __builtin_amdgcn_ds_read_tr16_b64_v4i16((LAS s16x4*)p);
}
__device__ __forceinline__ bf16x8 cat8(s16x4 a, s16x4 b) {
    return __builtin_shufflevector(a, b, 0, 1, 2, 3, 4, 5, 6, 7);
}
__device__ __forceinline__ f32x4 sel4(bool c, f32x4 a, f32x4 b) {
    f32x4 r;
#pragma unroll
    for (int k = 0; k < 4; ++k) { float x = a[k], y = b[k]; asm volatile("" : "+v"(x), "+v"(y)); r[k] = c ? x : y; }
    return r;
}
__device__ __forceinline__ float rsum_g(float x) {
    const unsigned u = __float_as_uint(x);
    const auto a = __builtin_amdgcn_permlane16_swap(u, u, false, false);
    const float y = __uint_as_float(a[0]) + __uint_as_float(a[1]);
    const unsigned v = __float_as_uint(y);
    const auto b = __builtin_amdgcn_permlane32_swap(v, v, false, false);
    return __uint_as_float(b[0]) + __uint_as_float(b[1]);
}
template <int CTRL> __device__ __forceinline__ float dpp_f(float x) {
    return __uint_as_float(__builtin_amdgcn_update_dpp(0u, __float_as_uint(x), CTRL, 0xf, 0xf, false));
}
__device__ __forceinline__ float rsum_64(float x) {
    x += dpp_f<0xB1>(x);
    x += dpp_f<0x4E>(x);
    x += dpp_f<0x141>(x);
    x += dpp_f<0x140>(x);
    return rsum_g(x);
}
template <int CTRL> __device__ __forceinline__ unsigned dpp_u(unsigned x) { return __builtin_amdgcn_update_dpp(0u, x, CTRL, 0xf, 0xf, false); }
__device__ __forceinline__ unsigned umax2(unsigned a, unsigned b) { return a > b ? a : b; }
__device__ __forceinline__ float rmax_64(float xf) {
    unsigned x = __float_as_uint(xf);
    x = umax2(x, dpp_u<0xB1>(x)); x = umax2(x, dpp_u<0x4E>(x)); x = umax2(x, dpp_u<0x141>(x)); x = umax2(x, dpp_u<0x140>(x));
    const auto a = __builtin_amdgcn_permlane16_swap(x, x, false, false); x = umax2(a[0], a[1]);
    const auto b = __builtin_amdgcn_permlane32_swap(x, x, false, false); x = umax2(b[0], b[1]);
    return __uint_as_float(x);
}
__device__ __forceinline__ float ln_sd(float var) { return rbf(__builtin_amdgcn_sqrtf(rbf(rbf(var) + LN_EPS))); }
__device__ __forceinline__ int fsw(int row) { return ((row >> 1) & 1) | (((row >> 3) & 1) << 1); }
#define MFMA16(a, b, c) __builtin_amdgcn_mfma_f32_16x16x32_bf16((a), (b), (c), 0, 0, 0)
#define MFMA32(a, b, c) __builtin_amdgcn_mfma_f32_32x32x16_bf16((a), (b), (c), 0, 0, 0)
#define VM0() asm volatile("s_waitcnt vmcnt(0)" ::: "memory")
#define LGKM0() asm volatile("s_waitcnt lgkmcnt(0)" ::: "memory")
#define BAR() do { asm volatile("" ::: "memory"); __builtin_amdgcn_s_barrier(); asm volatile("" ::: "memory"); } while (0)

typedef __amdgpu_buffer_rsrc_t rsrc_t;
__device__ __forceinline__ rsrc_t mk_rsrc(const void* p, unsigned bytes) { return __builtin_amdgcn_make_buffer_rsrc((void*)p, 0, bytes, 0x00020000); }
template <int AUX> __device__ __forceinline__ bf16x8 bld16(rsrc_t r, int voff, int soff) {
    return __builtin_bit_cast(bf16x8, __builtin_amdgcn_raw_buffer_load_b128(r, voff, soff, AUX));
}
template <int AUX> __device__ __forceinline__ f32x4 bldf4(rsrc_t r, int voff, int soff) {
    return __builtin_bit_cast(f32x4, __builtin_amdgcn_raw_buffer_load_b128(r, voff, soff, AUX));
}
template <int AUX> __device__ __forceinline__ u32x4 bldu4(rsrc_t r, int voff, int soff) {
    return __builtin_amdgcn_raw_buffer_load_b128(r, voff, soff, AUX);
}
template <int AUX> __device__ __forceinline__ u32x2 bldu2(rsrc_t r, int voff, int soff) {
    return __builtin_amdgcn_raw_buffer_load_b64(r, voff, soff, AUX);
}
template <int AUX> __device__ __forceinline__ void blds16(rsrc_t r, LAS char* l, int voff, int soff) {
    __builtin_amdgcn_raw_ptr_buffer_load_lds(r, (LAS void*)l, 16, voff, soff, 0, AUX);
}
__device__ __forceinline__ void bst16(u32x4 v, rsrc_t r, int voff, int soff) { __builtin_amdgcn_raw_buffer_store_b128(v, r, voff, soff, SC1); }
__device__ __forceinline__ void bst8(u32x2 v, rsrc_t r, int voff, int soff) { __builtin_amdgcn_raw_buffer_store_b64(v, r, voff, soff, SC1); }
__device__ __forceinline__ void bst4(unsigned v, rsrc_t r, int voff, int soff) { __builtin_amdgcn_raw_buffer_store_b32(v, r, voff, soff, SC1); }

struct Args {
    const u16* x; const u16 *are, *aim, *bre, *bim, *cre, *cim, *logdt, *dskip;
    const u16 *w1, *w2, *w3, *w4;
    char* ws; u16* out;
};

typedef GAS unsigned gu32;
typedef GAS unsigned long long gu64;
#define RLX_AGENT __ATOMIC_RELAXED, __HIP_MEMORY_SCOPE_AGENT
#define WAUX 16
#define OAUX 0
#define PSLEEP 2
#define LATE_STATS 0
#define CODE_PF 41984
#define POLL_MODE 0
#define POLL_GAP 6
#define PREP_DELAY 100ull
constexpr unsigned long long WAIT_TICKS = 400000ull;
__device__ __forceinline__ void publish(char* ws, size_t flag_off, int idx, unsigned tag) {
    if (threadIdx.x == 0) __hip_atomic_store((gu32*)(ws + flag_off) + idx, tag, RLX_AGENT);
}
__device__ __forceinline__ void publish_rep(char* ws, size_t flag_off, int idx, unsigned tag, int lane) {
    if (lane < REP) __hip_atomic_store((gu32*)(ws + flag_off) + lane * 256 + idx, tag, RLX_AGENT);
}
__device__ __forceinline__ void wait_flags(char* ws, size_t offa, int a0, int na, size_t offb, int b0, int nb, unsigned tag) {
    const int lane = threadIdx.x & 63;
    const gu32* p[4]; bool on[4];
#pragma unroll
    for (int k = 0; k < 4; ++k) {
        const int i = lane + 64 * k;
        on[k] = i < na + nb;
        p[k] = i < na ? (const gu32*)(ws + offa) + a0 + i : (const gu32*)(ws + offb) + b0 + (on[k] ? i - na : 0);
    }
    const unsigned long long t0 = __builtin_amdgcn_s_memrealtime();
    for (;;) {
        bool ok = true;
#pragma unroll
        for (int k = 0; k < 4; ++k)
            if (64 * k < na + nb) { const unsigned v = __hip_atomic_load(p[k], RLX_AGENT); ok = ok && (!on[k] || v == tag); }
        if (__all(ok)) break;
        if (__builtin_amdgcn_s_memrealtime() - t0 > WAIT_TICKS) break;
        __builtin_amdgcn_s_sleep(PSLEEP);
    }
}
__device__ __forceinline__ void wait_panels(char* ws, size_t o0, int p0, size_t o1, int p1, size_t o2, int p2, unsigned tag) {
    const int lane = threadIdx.x & 63, k = (lane >> 3) % 3;
    const gu32* p = (const gu32*)(ws + (k == 0 ? o0 : k == 1 ? o1 : o2)) + (k == 0 ? p0 : k == 1 ? p1 : p2) * 32 + (lane & 7);
    const unsigned long long t0 = __builtin_amdgcn_s_memrealtime();
    for (;;) {
        if (__all(__hip_atomic_load(p, RLX_AGENT) == tag)) break;
        if (__builtin_amdgcn_s_memrealtime() - t0 > WAIT_TICKS) break;
        __builtin_amdgcn_s_sleep(PSLEEP);
    }
}
struct StatRow { u32x4 v0, v1; };
__device__ __forceinline__ StatRow row_stats_load(const Args& a, int w, int lane) {
    const int row = blockIdx.x * 8 + (w & 7);
    const rsrc_t rx = mk_rsrc(a.x + (size_t)row * D, w < 8 ? D * 2 : 0);
    StatRow r;
    r.v0 = bldu4<0>(rx, lane * 16, 0); r.v1 = bldu4<0>(rx, 1024 + lane * 16, 0);
    return r;
}
__device__ __forceinline__ void row_stats(const Args& a, const StatRow& r, unsigned tag, int w, int lane) {
    if (w < 8) {
        float s1 = 0.f, s2 = 0.f;
#pragma unroll
        for (int k = 0; k < 4; ++k) {
            s1 = dot2bf(r.v0[k], 0x3f803f80u, s1); s2 = dot2bf(r.v0[k], r.v0[k], s2);
            s1 = dot2bf(r.v1[k], 0x3f803f80u, s1); s2 = dot2bf(r.v1[k], r.v1[k], s2);
        }
        s1 = rsum_64(s1); s2 = rsum_64(s2);
        const float mean = s1 * (1.0f / D), var = fmaxf(s2 * (1.0f / D) - mean * mean, 0.f);
        const unsigned val = (pk_bf16(mean, ln_sd(var)));
        if (lane == 0) __hip_atomic_store((gu64*)(a.ws + WS_GR) + blockIdx.x * 8 + w, ((unsigned long long)tag << 32) | val, RLX_AGENT);
    }
}

struct PrepRegs { u32x4 R[36]; };
__device__ __forceinline__ void prep_issue(const Args& a, int job, int lane, PrepRegs& P) {
    const int sub = job / 18, rem = job % 18, kh = rem / 9, ns = rem % 9;
    const u16* src; int ld;
    if (sub == 0) { src = a.w1; ld = D; }
    else if (sub == 1) { src = a.w2; ld = D; }
    else if ((sub & 1) == 0) { src = a.w3 + ((sub - 2) >> 1) * D; ld = F; }
    else { src = a.w4 + (size_t)((sub - 2) >> 1) * D * D; ld = D; }
    src += (size_t)(kh * 288) * ld + ns * 64;
    const rsrc_t rs = mk_rsrc(src, 288 * ld * 2);
    const int ldb = ld * 2;
    const int vo = (lane >> 3) * ldb + (lane & 7) * 16;
#pragma unroll
    for (int i = 0; i < 36; ++i) P.R[i] = bldu4<0>(rs, vo, i * 8 * ldb);
    __builtin_amdgcn_sched_barrier(0);
}
template <bool PUBLISH, bool COLSUM>
__device__ __forceinline__ void prep_finish(const Args& a, int job, LAS char* lds, int lane, unsigned tag, const PrepRegs& P STAMP_ARG) {
    const int sub = job / 18, rem = job % 18, kh = rem / 9, ns = rem % 9;
    const rsrc_t rd = mk_rsrc(a.ws + WS_WF + (size_t)sub * WSUB * 2, WSUB * 2);
    const int fl = (lane >> 4) & 1;
    const int w0 = (lane >> 3) * 128 + (((lane & 7) ^ (fl << 1)) * 16), w1 = (lane >> 3) * 128 + (((lane & 7) ^ ((fl | 2) << 1)) * 16);
    const int g = lane >> 4, i16 = lane & 15, q = i16 >> 2, p = lane & 3;
    const int f = ((q >> 1) & 1) | ((g & 1) << 1);
    int rb[4];
#pragma unroll
    for (int n = 0; n < 4; ++n) rb[n] = (8 * g + q) * 128 + (((2 * n + (p >> 1)) ^ (f << 1)) * 16) + 8 * (p & 1);
    float cs[4] = {0.f, 0.f, 0.f, 0.f};
#pragma unroll
    for (int u = 0; u < 9; ++u) {
        LAS char* sl = lds + (u & 1) * 4096;
#pragma unroll
        for (int i = 0; i < 4; ++i) *(LAS u32x4*)(sl + i * 1024 + ((i & 1) ? w1 : w0)) = P.R[4 * u + i];
        if (u == 0) STAMP(a.ws, 30);
        if (u == 8) STAMP(a.ws, 31);
#pragma unroll
        for (int n = 0; n < 4; ++n) {
            const u32x4 x = __builtin_bit_cast(u32x4, cat8(tr_read(sl + rb[n]), tr_read(sl + rb[n] + 512)));
            bst16(x, rd, lane * 16, ((kh * 9 + u) * 36 + ns * 4 + n) * 1024);
            if (COLSUM) {
#pragma unroll
                for (int k = 0; k < 4; ++k) cs[n] = dot2bf(x[k], 0x3f803f80u, cs[n]);
            }
        }
    }
    if (COLSUM && (sub & 1) == 0) {
        const rsrc_t rc = mk_rsrc(a.ws + WS_CS, 2 * (D + F) * 4);
#pragma unroll
        for (int n = 0; n < 4; ++n) {
            const float c = rsum_g(cs[n]);
            if (lane < 16) bst4(__float_as_uint(c), rc, lane * 4, (kh * (D + F) + (sub == 0 ? 0 : D + ((sub - 2) >> 1) * D) + ns * 64 + n * 16) * 4);
        }
    }
    STAMP(a.ws, 32);
    if (PUBLISH) {
        VM0();
        STAMP(a.ws, 33);
        if (lane == 0) __hip_atomic_store((gu32*)(a.ws + WS_FW) + job, tag, RLX_AGENT);
    }
}

constexpr int CV_RC = 4160;
constexpr int CV_U = 4 * CV_RC;
constexpr int CV_X = CV_U + 2288 * 2;
constexpr int CV_RED = CV_X + 4096;
constexpr int CV_CH = CV_RED + 24576;
constexpr int KG_ROW = 544;
constexpr int KG_XI = 32 * KG_ROW, KG_T = CV_RED + 4096;

struct cpx { float re, im; };
__device__ __forceinline__ cpx cmul(cpx a, cpx b) { return {a.re * b.re - a.im * b.im, a.re * b.im + a.im * b.re}; }
__device__ __forceinline__ cpx cexp_rev(float a2, float rev, float m) {
    const float e = __builtin_amdgcn_exp2f(a2 * m), p = __builtin_amdgcn_fractf(rev * m);
    return {e * __builtin_amdgcn_cosf(p), e * __builtin_amdgcn_sinf(p)};
}
__device__ __forceinline__ unsigned pk_f16(cpx c) { f32x2 v = {c.re, c.im}; return __builtin_bit_cast(unsigned, __builtin_convertvector(v, f16x2)); }

__device__ __forceinline__ void conv_put(LAS char* lds, int m, u32x4 A, u32x2 B) {
    u32x4 c1 = {__builtin_amdgcn_alignbit(A[1], A[0], 16), __builtin_amdgcn_alignbit(A[2], A[1], 16), __builtin_amdgcn_alignbit(A[3], A[2], 16), __builtin_amdgcn_alignbit(B[0], A[3], 16)};
    u32x4 c2 = {A[1], A[2], A[3], B[0]};
    u32x4 c3 = {c1[1], c1[2], c1[3], __builtin_amdgcn_alignbit(B[1], B[0], 16)};
    *(LAS u32x4*)(lds + 0 * CV_RC + m * 16) = A;
    *(LAS u32x4*)(lds + 1 * CV_RC + m * 16) = c1;
    *(LAS u32x4*)(lds + 2 * CV_RC + m * 16) = c2;
    *(LAS u32x4*)(lds + 3 * CV_RC + m * 16) = c3;
}

constexpr int LDS_FWOK = 3 * CV_CH;
__device__ __forceinline__ void phase_conv(const Args& a, LAS char* lds0, unsigned tagv, const StatRow& sr, unsigned& tag_out STAMP_ARG) {
    const int tid = threadIdx.x, w = __builtin_amdgcn_readfirstlane(tid >> 6), lane = tid & 63;
    const int b = blockIdx.x, chl = w >> 2, wq = w & 3, t8 = tid & 255, ch = 3 * b + chl;
    LAS char* lds = lds0 + chl * CV_CH;
    const int part = wq >> 1, n = (wq & 1) * 64 + lane;
    const int o = ch * NS + n, o2 = ch * NS + (n ^ 64);
    const u16 r_ld = a.logdt[ch], r_ar = a.are[o], r_ai = a.aim[o], r_ar2 = a.are[o2];
    u16 r_b[4] = {}, r_c[4] = {};
    if (part) { r_b[0] = a.bre[o]; r_b[1] = a.bim[o]; r_c[0] = a.cre[o]; r_c[1] = a.cim[o]; r_b[2] = a.bre[o2]; r_b[3] = a.bim[o2]; r_c[2] = a.cre[o2]; r_c[3] = a.cim[o2]; }
    const u16 r_ds = a.dskip[ch];
    __builtin_amdgcn_sched_barrier(0);
    const unsigned tag = __builtin_amdgcn_readfirstlane(tagv) + 1u;
    tag_out = tag;
    if (tid == 0) __hip_atomic_store((gu32*)(a.ws + WS_FA) + b, tag, RLX_AGENT);
    row_stats(a, sr, tag, w, lane);
    STAMP(a.ws, 1);
    __builtin_amdgcn_sched_barrier(0);
    u32x2 xr[4];
    const rsrc_t rx = mk_rsrc(a.x, L * D * 2);
    const int xvo = tid * (2 * D * 2) + ((6 * b) & ~3);
#define GATHER(i) do { __builtin_amdgcn_sched_barrier(0); if ((i) < 2 || w < 4) xr[i] = bldu2<0>(rx, xvo + ((i) & 1) * D * 2, ((i) >> 1) * 1536 * D * 2); __builtin_amdgcn_sched_barrier(0); } while (0)
    GATHER(0);
    const float dt = rbf(expf(bf2f(r_ld)));
    const float are = dt * bf2f(r_ar);
    const float ap = rmax_64(fmaxf(fmaxf(are, dt * bf2f(r_ar2)), 0.f));
    const float a2 = (are - ap) * 1.4426950408889634f, rev = dt * bf2f(r_ai) * 0.15915494309189535f;
    float kscale = 1.f;
    if (part == 0) {
        cpx e[4];
        const cpx g8 = cexp_rev(a2, rev, 8.f), g32 = cexp_rev(a2, rev, 32.f);
        e[0] = {1.f, 0.f}; e[1] = g8; e[2] = cmul(g8, g8); e[3] = cmul(e[2], g8);
        LAS char* er = lds + n * 4;
#pragma unroll
        for (int t = 0; t < 8; ++t) {
            if (t == 0) GATHER(1);
            if (t == 3) GATHER(2);
            if (t == 6) GATHER(3);
#pragma unroll
            for (int c = 0; c < 4; ++c) {
                *(LAS unsigned*)(er + (4 * t + c) * KG_ROW) = pk_f16(e[c]);
                if (t < 7) e[c] = (t == 0 && c == 0) ? g32 : cmul(e[c], g32);
            }
        }
    } else {
        const float b0 = dt * bf2f(r_b[0]), b1 = dt * bf2f(r_b[1]), c0 = bf2f(r_c[0]), c1 = bf2f(r_c[1]);
        const float b2 = dt * bf2f(r_b[2]), b3 = dt * bf2f(r_b[3]), c2 = bf2f(r_c[2]), c3 = bf2f(r_c[3]);
        const float wr = c0 * b0 - c1 * b1, wi = c0 * b1 + c1 * b0, vr = c2 * b2 - c3 * b3, vi = c2 * b3 + c3 * b2;
        const float wm = rmax_64(fmaxf(fmaxf(fabsf(wr), fabsf(wi)), fmaxf(fabsf(vr), fabsf(vi))));
        const unsigned ex = (__float_as_uint(wm) >> 23) & 0xffu;
        const unsigned exc = ex < 2u ? 2u : (ex > 252u ? 252u : ex);
        const float sc = __uint_as_float((254u - exc) << 23);
        kscale = __uint_as_float(exc << 23);
        const cpx W = {wr * sc, -wi * sc};
        const cpx g1 = cexp_rev(a2, -rev, 1.f), p256 = cexp_rev(a2, -rev, 256.f);
        const cpx g2 = cmul(g1, g1), g4 = cmul(g2, g2), p512 = cmul(p256, p256);
        LAS char* xr_ = lds + KG_XI + n * 4;
#define XST(i, v) *(LAS unsigned*)(xr_ + (i) * KG_ROW) = pk_f16(v)
        GATHER(1);
        cpx x = W; XST(0, x);
#pragma unroll
        for (int i = 1; i < 8; ++i) { x = cmul(x, g1); XST(i, x); }
        GATHER(2);
        x = cmul(W, p256); XST(8, x);
#pragma unroll
        for (int i = 9; i < 12; ++i) { x = cmul(x, g2); XST(i, x); }
        cpx y = cmul(W, p512); XST(12, y); XST(13, cmul(y, g4));
        x = cmul(y, p256); XST(14, x); XST(15, cmul(x, g4));
        GATHER(3);
        y = cmul(y, p512); XST(16, y);
#pragma unroll
        for (int i = 17; i < 21; ++i) { y = cmul(y, p256); XST(i, y); }
#undef XST
#undef GATHER
        if (lane == 0 && wq == 2) *(LAS float*)(lds + KG_T - 16) = kscale;
    }
    const int p = lane & 15, g = lane >> 4;
    LAS float* T = (LAS float*)(lds + KG_T);
    int base, idx0, istr;
    if (wq & 1) {
        base = p < 4 ? 1024 + 256 * p : (p == 4 ? 2048 : 0);
        idx0 = p < 4 ? 512 + 32 * p : (p == 4 ? 640 : 680);
        istr = 1;
    } else {
        const int q4 = p & 3;
        base = p < 8 ? p : (p < 12 ? 256 + 2 * q4 : 512 + 4 * (p & 1) + 128 * (p & 2));
        idx0 = p < 8 ? p : (p < 12 ? 256 + q4 : 384 + (p & 1) + 32 * (p & 2));
        istr = p < 8 ? 8 : (p < 12 ? 4 : 2);
    }
    const int j0 = (wq >> 1) * 16 + 4 * g;
    LAS float* tw = T + idx0 + j0 * istr;
    const LAS char* ea = lds + ((wq >> 1) * 16 + p) * KG_ROW + g * 16;
    const LAS char* xa = lds + KG_XI + ((wq & 1) * 16 + p) * KG_ROW + g * 16;
    STAMP(a.ws, 29);
    LGKM0(); BAR();
    STAMP(a.ws, 2);
    {
        f32x4 acc = {};
        f16x8 A[8], B[8];
#pragma unroll
        for (int t = 0; t < 8; ++t) { A[t] = *(const LAS f16x8*)(ea + t * 64); B[t] = *(const LAS f16x8*)(xa + t * 64); }
        float ks = *(const LAS float*)(lds + KG_T - 16);
        __builtin_amdgcn_sched_barrier(0);
#pragma unroll
        for (int t = 0; t < 8; ++t) acc = __builtin_amdgcn_mfma_f32_16x16x32_f16(A[t], B[t], acc, 0, 0, 0);
        if (ap > 0.f) {
#pragma unroll
            for (int q = 0; q < 4; ++q) acc[q] *= __builtin_amdgcn_exp2f(ap * 1.4426950408889634f * (float)(base + 8 * (j0 + q)));
        }
#pragma unroll
        for (int q = 0; q < 4; ++q) tw[q * istr] = acc[q] * ks;
    }
    LGKM0(); BAR();
    STAMP(a.ws, 3);
    {
        float v[12];
        float lf = (float)(2047 - t8 * 8);
        asm volatile("" : "+v"(lf));
        const unsigned tb = (unsigned)(size_t)T - 17024u * 4u;
        if (wq < 3) {
#pragma unroll
            for (int e = 0; e < 12; e += 2) {
                const unsigned bits = pk_bf16(lf - (float)e, lf - (float)(e + 1));
                v[e] = *(const LAS float*)(size_t)(((bits & 0xffffu) << 2) + tb);
                v[e + 1] = *(const LAS float*)(size_t)(((bits >> 16) << 2) + tb);
            }
        } else {
#pragma unroll
            for (int e = 0; e < 12; ++e) {
                const int l = 2047 - (t8 * 8 + e);
                const int idx = l < 128 ? (l < 0 ? 0 : l) : (int)(pk_bf16((float)l, 0.f) & 0xffffu) - 17024;
                const float t = T[idx];
                v[e] = l < 0 ? 0.f : t;
            }
        }
        conv_put(lds, t8, (u32x4){pk_bf16(v[0], v[1]), pk_bf16(v[2], v[3]), pk_bf16(v[4], v[5]), pk_bf16(v[6], v[7])}, (u32x2){pk_bf16(v[8], v[9]), pk_bf16(v[10], v[11])});
        if (t8 < 4) conv_put(lds, 256 + t8, (u32x4){0u, 0u, 0u, 0u}, (u32x2){0u, 0u});
        if (t8 >= 64 && t8 < 94) *(LAS u32x4*)(lds + CV_U + (t8 - 64) * 16) = (u32x4){0u, 0u, 0u, 0u};
    }
    STAMP(a.ws, 4);
    {
        u32x4 gv[2];
        const rsrc_t rgn = mk_rsrc(a.ws + WS_GR, L * 8);
        const unsigned long long t0 = __builtin_amdgcn_s_memrealtime();
        for (;;) {
            gv[0] = bldu4<SC1>(rgn, tid * 16, 0);
            gv[1] = w < 4 ? bldu4<SC1>(rgn, tid * 16, 1536 * 8) : gv[0];
            const bool ok = gv[0][1] == tag && gv[0][3] == tag && gv[1][1] == tag && gv[1][3] == tag;
            if (__all(ok)) break;
            if (__builtin_amdgcn_s_memrealtime() - t0 > WAIT_TICKS) break;
            __builtin_amdgcn_s_sleep(PSLEEP);
            asm volatile("" ::: "memory");
        }
#pragma unroll
        for (int i = 0; i < 2; ++i) {
            if (i == 0 || w < 4) {
                const int row = 2 * tid + 1536 * i;
                u32x2 x0 = xr[2 * i], x1 = xr[2 * i + 1];
                if (b & 1) {
                    x0 = (u32x2){__builtin_amdgcn_alignbit(x0[1], x0[0], 16), x0[1] >> 16};
                    x1 = (u32x2){__builtin_amdgcn_alignbit(x1[1], x1[0], 16), x1[1] >> 16};
                }
                const float m0 = bf_lo(gv[i][0]), m1 = bf_lo(gv[i][2]);
                const float s0 = __builtin_amdgcn_rcpf(bf_hi(gv[i][0])), s1 = __builtin_amdgcn_rcpf(bf_hi(gv[i][2]));
#pragma unroll
                for (int c = 0; c < 3; ++c) {
                    const unsigned xp = c == 0 ? __builtin_amdgcn_perm(x1[0], x0[0], 0x05040100u) : c == 1 ? __builtin_amdgcn_perm(x1[0], x0[0], 0x07060302u)
                                                                                                            : __builtin_amdgcn_perm(x1[1], x0[1], 0x05040100u);
                    const unsigned dd = pk_bf16(bf_lo(xp) - m0, bf_hi(xp) - m1);
                    *(LAS unsigned*)(lds0 + c * CV_CH + CV_U + 480 + row * 2) = pk_bf16(bf_lo(dd) * s0, bf_hi(dd) * s1);
                    *(LAS unsigned*)(lds0 + c * CV_CH + CV_X + row * 2) = xp;
                }
            }
        }
    }
    LGKM0(); BAR();
    STAMP(a.ws, 5);
    unsigned fwv = tag;
    if (w == 11 && lane < 36) fwv = __hip_atomic_load((gu32*)(a.ws + WS_FW) + lane, RLX_AGENT);
    f32x4 acc[8];
#pragma unroll
    for (int c = 0; c < 8; ++c) acc[c] = (f32x4){0.f, 0.f, 0.f, 0.f};
    const int cpy = (3 - p) & 3;
#pragma unroll
    for (int rr = 0; rr < 2; ++rr) {
        const int r = 2 * wq + rr;
        const LAS char* ab = lds + cpy * CV_RC + 2 * (2031 - 32 * r + 8 * g - p - cpy) - 512 * 7;
        const LAS char* fb = lds + CV_U + 2 * (240 - 32 * r + 16 * p - 16 + 8 * g);
        bf16x8 A[8], Fm[8];
#pragma unroll
        for (int s = 0; s < 8; ++s) {
            const s16x4 lo = *(const volatile LAS s16x4*)(ab + 512 * (7 - s));
            const s16x4 hi = *(const volatile LAS s16x4*)(ab + 512 * (7 - s) + 8);
            A[s] = cat8(lo, hi);
        }
#pragma unroll
        for (int e = 0; e < 8; ++e) Fm[e] = *(const LAS bf16x8*)(fb + 512 * e);
#pragma unroll
        for (int c = 0; c < 8; ++c)
#pragma unroll
            for (int s = 0; s <= c; ++s) acc[c] = MFMA16(A[s], Fm[c - s], acc[c]);
    }
    STAMP(a.ws, 6);
    LAS f32x4* red = (LAS f32x4*)(lds + CV_RED);
#pragma unroll
    for (int c = 0; c < 8; ++c) {
        const int ow = c >> 1;
        if (ow != wq) red[(ow * 6 + (c & 1) * 3 + (wq > ow ? wq - 1 : wq)) * 64 + lane] = acc[c];
    }
    if (w == 11) { const bool okw = __all(fwv == tag); if (lane == 0) *(LAS unsigned*)(lds0 + LDS_FWOK) = okw ? 1u : 0u; }
    LGKM0(); BAR();
    const rsrc_t rh = mk_rsrc(a.ws + WS_HT + (size_t)ch * L * 2, L * 2);
    const float dsk = bf2f(r_ds);
#pragma unroll
    for (int i = 0; i < 2; ++i) {
        f32x4 y;
#pragma unroll
        for (int k = 0; k < 4; ++k) {
            const float y01 = (wq & 1) ? acc[2 + i][k] : acc[i][k], y23 = (wq & 1) ? acc[6 + i][k] : acc[4 + i][k];
            y[k] = (wq & 2) ? y23 : y01;
        }
#pragma unroll
        for (int k = 0; k < 3; ++k) y += red[(wq * 6 + i * 3 + k) * 64 + lane];
        const int l0 = 256 * (2 * wq + i) + 16 * p + 4 * g;
        const u32x2 uu = *(const LAS u32x2*)(lds + CV_U + 480 + 2 * l0), xv = *(const LAS u32x2*)(lds + CV_X + 2 * l0);
        float o[4];
#pragma unroll
        for (int k = 0; k < 4; ++k) {
            const float u = (k & 1) ? bf_hi(uu[k >> 1]) : bf_lo(uu[k >> 1]);
            const float x = (k & 1) ? bf_hi(xv[k >> 1]) : bf_lo(xv[k >> 1]);
            o[k] = x + __builtin_fmaf(u, dsk, y[k]);
        }
        bst8((u32x2){pk_bf16(o[0], o[1]), pk_bf16(o[2], o[3])}, rh, 2 * l0, 0);
    }
    STAMP(a.ws, 28);
    VM0(); BAR();
    if (w == 0) publish_rep(a.ws, WS_FH, b, tag, lane);
}

__device__ __forceinline__ int nb0_of(int j) { return (9 * j + 1) >> 1; }

constexpr int OUT_ROW = 176;
constexpr int KC_XB = 73728, KC_ST = KC_XB + 20 * 1024, KC_OUT = KC_ST + 512;
__device__ __forceinline__ void phase_c(char* __restrict__ ws, LAS char* lds, int panel, int j, unsigned tag STAMP_ARG) {
    const int tid = threadIdx.x, w = __builtin_amdgcn_readfirstlane(tid >> 6), lane = tid & 63;
    const int tok0 = panel * 64, nbs = nb0_of(j), nblk = nb0_of(j + 1) - nbs;
    const int g = lane >> 4, i16 = lane & 15, q = i16 >> 2, p = lane & 3;
    const int b = w >> 1, kh = w & 1;
    const bool act = w < 10 && b < nblk;
    const int nb = nbs + (act ? b : 0);
    if (w == 0 && *(const LAS unsigned*)(lds + LDS_FWOK) == 0u) wait_flags(ws, WS_FW, 0, 36, WS_FW, 0, 0, tag);
    BAR();
    bf16x8 W[9];
    f32x4 cs0, cs1;
    if (w < 10) {
        const rsrc_t rw = mk_rsrc(ws + WS_WF, WSUB * 2);
#pragma unroll
        for (int i = 0; i < 9; ++i) W[i] = bld16<SC1>(rw, lane * 16, ((kh * 9 + i) * 36 + nb) * 1024);
        const rsrc_t rc = mk_rsrc(ws + WS_CS, 2 * (D + F) * 4);
        cs0 = bldf4<SC1>(rc, g * 16, nb * 64); cs1 = bldf4<SC1>(rc, g * 16, nb * 64 + (D + F) * 4);
    }
    if (w == 11) wait_flags(ws, WS_FH, (blockIdx.x & (REP - 1)) * 256, 192, WS_FH, 0, 0, tag);
    BAR();
    STAMP(ws, 8);
    {
        const rsrc_t rh = mk_rsrc(ws + WS_HT, D * L * 2);
        const int fl = (lane >> 4) & 1;
        const int v0 = (lane >> 3) * 4096 + (((lane & 7) ^ (fl << 1)) * 16), v1 = (lane >> 3) * 4096 + (((lane & 7) ^ ((fl | 2) << 1)) * 16);
#pragma unroll
        for (int k = 0; k < 6; ++k) { const int idx = 6 * w + k; blds16<SC1>(rh, lds + idx * 1024, (k & 1) ? v1 : v0, idx * 32768 + tok0 * 2); }
    }
    VM0(); BAR();
    STAMP(ws, 9);
    const int f = ((q >> 1) & 1) | ((g & 1) << 1);
    const int lo_ = (8 * g + q) * 128 + 8 * (p & 1);
    f32x4 acc[4] = {};
    LAS f32x2* st = (LAS f32x2*)(lds + KC_ST);
    LAS f32x4* xb = (LAS f32x4*)(lds + KC_XB);
    if (w < 10) {
        const LAS char* hb[4];
#pragma unroll
        for (int mt = 0; mt < 4; ++mt) hb[mt] = lds + kh * 9 * 4096 + lo_ + (((2 * mt + (p >> 1)) ^ (f << 1)) * 16);
        bf16x8 H[2][2][4];
#pragma unroll
        for (int c = 0; c < 6; ++c) {
            if (c < 5) {
#pragma unroll
                for (int i = 0; i < 2; ++i)
                    if (2 * c + i < 9) {
#pragma unroll
                        for (int mt = 0; mt < 4; ++mt) H[c & 1][i][mt] = cat8(tr_read(hb[mt] + (2 * c + i) * 4096), tr_read(hb[mt] + (2 * c + i) * 4096 + 512));
                    }
            }
            __builtin_amdgcn_sched_barrier(0);
            if (c > 0) {
#pragma unroll
                for (int i = 0; i < 2; ++i)
                    if (2 * (c - 1) + i < 9) {
#pragma unroll
                        for (int mt = 0; mt < 4; ++mt) acc[mt] = MFMA16(W[2 * (c - 1) + i], H[(c - 1) & 1][i][mt], acc[mt]);
                    }
            }
            __builtin_amdgcn_sched_barrier(0);
        }
        xb[((w * 2 + 0) * 64) + lane] = sel4(kh, acc[0], acc[2]);
        xb[((w * 2 + 1) * 64) + lane] = sel4(kh, acc[1], acc[3]);
    } else {
        __builtin_amdgcn_s_setprio(3);
        const bf16x8 ones = {0x3f80, 0x3f80, 0x3f80, 0x3f80, 0x3f80, 0x3f80, 0x3f80, 0x3f80};
        const int m0 = 2 * (w - 10);
        const LAS char* h0 = lds + lo_ + (((2 * m0 + (p >> 1)) ^ (f << 1)) * 16);
        const LAS char* h1 = lds + lo_ + (((2 * m0 + 2 + (p >> 1)) ^ (f << 1)) * 16);
        bf16x8 H[2][3][2];
#pragma unroll
        for (int c = 0; c < 7; ++c) {
            if (c < 6) {
#pragma unroll
                for (int i = 0; i < 3; ++i) {
                    H[c & 1][i][0] = cat8(tr_read(h0 + (3 * c + i) * 4096), tr_read(h0 + (3 * c + i) * 4096 + 512));
                    H[c & 1][i][1] = cat8(tr_read(h1 + (3 * c + i) * 4096), tr_read(h1 + (3 * c + i) * 4096 + 512));
                }
            }
            __builtin_amdgcn_sched_barrier(0);
            if (c > 0) {
#pragma unroll
                for (int i = 0; i < 3; ++i) {
                    const bf16x8 H0 = H[(c - 1) & 1][i][0], H1 = H[(c - 1) & 1][i][1];
                    acc[0] = MFMA16(ones, H0, acc[0]); acc[1] = MFMA16(H0, H0, acc[1]);
                    acc[2] = MFMA16(ones, H1, acc[2]); acc[3] = MFMA16(H1, H1, acc[3]);
                }
            }
            __builtin_amdgcn_sched_barrier(0);
        }
        const float sm[2] = {acc[0][0], acc[2][0]};
        if (g == q) {
            const rsrc_t r2 = mk_rsrc(ws + WS_ST2, L * 8);
#pragma unroll
            for (int t = 0; t < 2; ++t) {
                const f32x4 d = acc[2 * t + 1];
                const float s2 = p == 0 ? d[0] : p == 1 ? d[1] : p == 2 ? d[2] : d[3];
                const float mean = sm[t] * (1.0f / D), var = fmaxf(s2 * (1.0f / D) - mean * mean, 0.f);
                const float mb = rbf(mean), rs = __builtin_amdgcn_rcpf(ln_sd(var));
                const float m2 = rbf((mean - mb) * rs), rs2 = __builtin_amdgcn_rcpf(ln_sd(var * rs * rs));
                st[(m0 + t) * 16 + i16] = (f32x2){rs * rs2, (mb * rs + m2) * rs2};
                if (j == 0) bst8((u32x2){__float_as_uint(mb), __float_as_uint(rs)}, r2, (tok0 + (m0 + t) * 16 + i16) * 8, 0);
            }
        }
    }
    __builtin_amdgcn_s_setprio(0);
    STAMP(ws, 24);
    LGKM0(); BAR();
    STAMP(ws, 10);
    const rsrc_t rg = mk_rsrc(ws + WS_G, (L + 4) * D * 2);
    LAS char* og = lds + KC_OUT;
    if (act) {
        const f32x4 cs = cs0 + cs1;
#pragma unroll
        for (int t = 0; t < 2; ++t) {
            const f32x4 a = sel4(kh, acc[2 + t], acc[t]) + xb[((w ^ 1) * 2 + t) * 64 + lane];
            const f32x2 ab = st[(2 * kh + t) * 16 + i16];
            float o[4];
#pragma unroll
            for (int k = 0; k < 4; ++k) o[k] = gelu_tanh(ab[0] * a[k] - ab[1] * cs[k]);
            *(LAS u32x2*)(og + (kh * 32 + t * 16 + i16) * OUT_ROW + b * 32 + g * 8) = (u32x2){pk_bf16(o[0], o[1]), pk_bf16(o[2], o[3])};
        }
    }
    LGKM0(); BAR();
    {
        const int nc = 2 * nblk, row = nblk == 5 ? tid / 10 : tid >> 3, c = tid - row * nc;
        if (row < 64) bst16(*(const LAS u32x4*)(og + row * OUT_ROW + c * 16), rg, row * (D * 2) + c * 16, ((2 + tok0) * D + nbs * 16) * 2);
        if ((panel == 0 || panel == 31) && tid >= 704 && tid < 704 + 2 * nc) {
            const int z = tid - 704, zr = z >= nc ? 1 : 0;
            bst16((u32x4){0u, 0u, 0u, 0u}, rg, zr * (D * 2) + (z - zr * nc) * 16, ((panel == 0 ? 0 : L + 2) * D + nbs * 16) * 2);
        }
    }
    STAMP(ws, 27);
    VM0(); BAR();
    publish(ws, WS_FG, panel * 32 + j, tag);
}

constexpr int KD_HT = 73728, KD_XB = KD_HT + 10240, KD_OUT = KD_XB + 20 * 1024;
__device__ __forceinline__ void phase_d(char* __restrict__ ws, LAS char* lds, int panel, int j, int je, unsigned tag STAMP_ARG) {
    const int tid = threadIdx.x, w = __builtin_amdgcn_readfirstlane(tid >> 6), lane = tid & 63;
    const int tok0 = panel * 64, nbs = nb0_of(j), nblk = nb0_of(j + 1) - nbs;
    const int g = lane >> 4, i16 = lane & 15, q = i16 >> 2, p = lane & 3;
    const int b = w >> 1, kh = w & 1;
    const bool act = w < 10 && b < nblk;
    const int nb = nbs + (act ? b : 0);
    bf16x8 W[9];
    if (w < 10) {
        const rsrc_t rw = mk_rsrc(ws + WS_WF + WSUB * 2, WSUB * 2);
#pragma unroll
        for (int i = 0; i < 9; ++i) W[i] = bld16<SC1>(rw, lane * 16, ((kh * 9 + i) * 36 + nb) * 1024);
    }
    if (w < 2 * nblk) {
        const rsrc_t rh = mk_rsrc(ws + WS_HT, D * L * 2);
        const int fl = ((lane >> 4) & 1) | ((w & 1) << 1);
        blds16<SC1>(rh, lds + KD_HT + w * 1024, (lane >> 3) * 4096 + (((lane & 7) ^ (fl << 1)) * 16), (nbs * 16 + w * 8) * 4096 + tok0 * 2);
    }
    if (w == 10) wait_flags(ws, WS_FW, (2 + 2 * je) * 18, 18, WS_FW, 0, 0, tag);
    if (w == 11) {
        const int p0 = panel > 0 ? panel - 1 : 0, p1 = panel < 31 ? panel + 1 : 31;
        wait_panels(ws, WS_FG, p0, WS_FG, panel, WS_FG, p1, tag);
    }
    BAR();
    STAMP(ws, 12);
    f32x2 st[2];
    {
        const rsrc_t rg = mk_rsrc(ws + WS_G, (L + 4) * D * 2);
        const int c7 = (lane & 7) ^ ((lane >> 3) & 6), vb = (lane >> 3) * 1152 + c7 * 16;
#pragma unroll
        for (int k = 0; k < 6; ++k) {
            const int idx = 6 * w + k, cg = idx >> 3, c = cg * 8 + c7;
            const int o2 = (0x4031 >> (((c * 57) >> 10) * 4)) & 15;
            blds16<SC1>(rg, lds + idx * 1024, vb + o2 * 1152, (tok0 + (idx & 7) * 8) * 1152 + cg * 128);
        }
        const rsrc_t r2 = mk_rsrc(ws + WS_ST2, L * 8);
#pragma unroll
        for (int t = 0; t < 2; ++t) st[t] = __builtin_bit_cast(f32x2, bldu2<SC1>(r2, i16 * 8, (tok0 + kh * 32 + t * 16) * 8));
    }
    VM0();
    asm volatile("" : "+v"(st[0]), "+v"(st[1]));
    BAR();
    STAMP(ws, 13);
    f32x4 acc[4] = {};
    LAS f32x4* xb = (LAS f32x4*)(lds + KD_XB);
    if (w < 10) {
        const int z = i16 & 6;
        const LAS char* a0 = lds + i16 * 128 + ((g ^ z) * 16), * a1 = lds + i16 * 128 + (((4 + g) ^ z) * 16);
        const LAS char* e0 = kh ? a1 + 4 * 8192 : a0;
        const LAS char* e1 = kh ? a0 + 5 * 8192 : a1;
        bf16x8 A[2][2][4];
#pragma unroll
        for (int c = 0; c < 6; ++c) {
            if (c < 5) {
#pragma unroll
                for (int ii = 0; ii < 2; ++ii) {
                    const int i = 2 * c + ii;
                    if (i < 9) {
#pragma unroll
                        for (int mt = 0; mt < 4; ++mt) A[c & 1][ii][mt] = *(const LAS bf16x8*)(((i & 1) ? e1 : e0) + (i >> 1) * 8192 + mt * 2048);
                    }
                }
            }
            __builtin_amdgcn_sched_barrier(0);
            if (c > 0) {
#pragma unroll
                for (int ii = 0; ii < 2; ++ii)
                    if (2 * (c - 1) + ii < 9) {
#pragma unroll
                        for (int mt = 0; mt < 4; ++mt) acc[mt] = MFMA16(W[2 * (c - 1) + ii], A[(c - 1) & 1][ii][mt], acc[mt]);
                    }
            }
            __builtin_amdgcn_sched_barrier(0);
        }
        xb[((w * 2 + 0) * 64) + lane] = sel4(kh, acc[0], acc[2]);
        xb[((w * 2 + 1) * 64) + lane] = sel4(kh, acc[1], acc[3]);
    }
    STAMP(ws, 25);
    LGKM0(); BAR();
    STAMP(ws, 14);
    LAS char* ox = lds + KD_OUT;
    if (w < 10) {
        const int bl = act ? b : 0;
        const int hrow = 16 * bl + 4 * g + q;
        const LAS char* hb = lds + KD_HT + hrow * 128 + 8 * (p & 1);
        const rsrc_t rp = mk_rsrc(ws + WS_PST, 32 * 36 * 64 * 8);
#pragma unroll
        for (int t = 0; t < 2; ++t) {
            const int mt = 2 * kh + t;
            const f32x4 a = sel4(kh, acc[2 + t], acc[t]) + xb[((w ^ 1) * 2 + t) * 64 + lane];
            const u32x2 hu = __builtin_bit_cast(u32x2, tr_read(hb + (((2 * mt + (p >> 1)) ^ (fsw(hrow) << 1)) * 16)));
            const float rs = st[t][1], nm = -st[t][0] * rs;
            u32x2 ov;
#pragma unroll
            for (int k = 0; k < 2; ++k)
                ov[k] = pk_bf16(__builtin_fmaf(bf_lo(hu[k]), rs, nm) + a[2 * k], __builtin_fmaf(bf_hi(hu[k]), rs, nm) + a[2 * k + 1]);
            float s1 = dot2bf(ov[1], 0x3f803f80u, dot2bf(ov[0], 0x3f803f80u, 0.f)), s2 = dot2bf(ov[1], ov[1], dot2bf(ov[0], ov[0], 0.f));
            s1 = rsum_g(s1); s2 = rsum_g(s2);
            if (act) {
                *(LAS u32x2*)(ox + (mt * 16 + i16) * OUT_ROW + b * 32 + g * 8) = ov;
                *(LAS u32x2*)(ox + 64 * OUT_ROW + (mt * 16 + i16) * OUT_ROW + b * 32 + g * 8) = hu;
                if (lane < 16) bst8((u32x2){__float_as_uint(s1), __float_as_uint(s2)}, rp, lane * 8, ((panel * 36 + nb) * 64 + mt * 16) * 8);
            }
        }
    }
    LGKM0(); BAR();
    {
        const int nc = 2 * nblk, row = nblk == 5 ? tid / 10 : tid >> 3, c = tid - row * nc;
        if (row < 64) {
            const int so = (tok0 * D + nbs * 16) * 2, vo = row * (D * 2) + c * 16;
            bst16(*(const LAS u32x4*)(ox + row * OUT_ROW + c * 16), mk_rsrc(ws + WS_X1, L * D * 2), vo, so);
            bst16(*(const LAS u32x4*)(ox + 64 * OUT_ROW + row * OUT_ROW + c * 16), mk_rsrc(ws + WS_HR, L * D * 2), vo, so);
        }
    }
    STAMP(ws, 26);
    VM0(); BAR();
    publish(ws, WS_FX, panel * 32 + j, tag);
}

constexpr int BG_LDS = 131072;
__device__ __forceinline__ void phase_cd_background(const Args& a, LAS char* lds, int panel, int job, unsigned tag STAMP_ARG) {
    char* ws = a.ws;
    const int tid = threadIdx.x, w = __builtin_amdgcn_readfirstlane(tid >> 6), lane = tid & 63, tok0 = panel * 64;
    BAR(); BAR();
    {
        const rsrc_t rh = mk_rsrc(ws + WS_HT, D * L * 2);
        const int fl = (lane >> 4) & 1;
        const int v0 = (lane >> 3) * 4096 + (((lane & 7) ^ (fl << 1)) * 16), v1 = (lane >> 3) * 4096 + (((lane & 7) ^ ((fl | 2) << 1)) * 16);
#pragma unroll
        for (int k = 0; k < 6; ++k) { const int idx = 6 * w + k; blds16<SC1>(rh, lds + idx * 1024, (k & 1) ? v1 : v0, idx * 32768 + tok0 * 2); }
    }
    VM0(); BAR();
    PrepRegs P;
    prep_issue(a, job, lane, P);
    BAR(); BAR(); BAR();
    prep_finish<false, false>(a, job, lds + BG_LDS + (w - 8) * 8192, lane, tag, P STAMP_PASS);
    BAR();
    {
        const rsrc_t rg = mk_rsrc(ws + WS_G, (L + 4) * D * 2);
        const int c7 = (lane & 7) ^ ((lane >> 3) & 6), vb = (lane >> 3) * 1152 + c7 * 16;
#pragma unroll
        for (int k = 0; k < 6; ++k) {
            const int idx = 6 * w + k, cg = idx >> 3, c = cg * 8 + c7;
            const int o2 = (0x4031 >> (((c * 57) >> 10) * 4)) & 15;
            blds16<SC1>(rg, lds + idx * 1024, vb + o2 * 1152, (tok0 + (idx & 7) * 8) * 1152 + cg * 128);
        }
    }
    VM0();
    if (lane == 0) __hip_atomic_store((gu32*)(ws + WS_FW) + job, tag, RLX_AGENT);
    BAR(); BAR(); BAR(); BAR();
}

constexpr int KE_HID = 73728, KE_ST = 2 * 73728, KE_FLG = KE_ST + 1024;
constexpr int NB = 3, RING = 4;
__device__ __forceinline__ void phase_e(char* __restrict__ ws, LAS char* lds, int panel, int j, unsigned tag STAMP_ARG) {
    const int w = __builtin_amdgcn_readfirstlane(threadIdx.x >> 6), lane = threadIdx.x & 63;
    constexpr int S1 = 6, S2 = 12;
    const int tok0 = panel * 64, g = lane >> 4, i16 = lane & 15, nbs = NB * w;
    const rsrc_t rw = mk_rsrc(ws + WS_WF + (size_t)(2 + 2 * j) * WSUB * 2, 2 * WSUB * 2);
    const int wv = lane * 16, wsb = nbs * 1024;
    bf16x8 W[RING][NB];
#pragma unroll
    for (int s = 0; s < RING; ++s)
#pragma unroll
        for (int b = 0; b < NB; ++b) W[s][b] = bld16<SC1>(rw, wv + b * 1024, wsb + s * 36864);
    f32x4 acc[NB][4];
    {
        const rsrc_t rc = mk_rsrc(ws + WS_CS, 2 * (D + F) * 4);
#pragma unroll
        for (int b = 0; b < NB; ++b) {
            acc[b][0] = bldf4<SC1>(rc, g * 16 + b * 64, (D + j * D + nbs * 16) * 4);
            acc[b][1] = bldf4<SC1>(rc, g * 16 + b * 64, (D + j * D + nbs * 16 + D + F) * 4);
        }
    }
    __builtin_amdgcn_sched_barrier(0);
    unsigned fw4 = tag;
    if (w == 10 && lane < 18) fw4 = __hip_atomic_load((gu32*)(ws + WS_FW) + (3 + 2 * j) * 18 + lane, RLX_AGENT);
    if (w == 11) wait_panels(ws, WS_FX, panel, WS_FX, panel, WS_FX, panel, tag);
    BAR();
    STAMP(ws, 16);
    u32x2 pv[36];
    if (w == 11) {
        const rsrc_t rp = mk_rsrc(ws + WS_PST + (size_t)panel * 36 * 512, 36 * 512);
#pragma unroll
        for (int k = 0; k < 36; ++k) pv[k] = bldu2<SC1>(rp, lane * 8, k * 512);
    }
    {
        const rsrc_t rx = mk_rsrc(ws + WS_X1 + (size_t)tok0 * D * 2, 64 * D * 2);
        const int vo = (lane >> 3) * 1152 + (((lane & 7) ^ ((lane >> 3) & 6)) * 16);
#pragma unroll
        for (int k = 0; k < 6; ++k) {
            const int idx = 6 * w + k;
            blds16<SC1>(rx, lds + idx * 1024, vo, (idx & 7) * 9216 + (idx >> 3) * 128);
        }
    }
    __builtin_amdgcn_sched_barrier(0);
    LAS f32x4* st = (LAS f32x4*)(lds + KE_ST);
    LAS unsigned* flg = (LAS unsigned*)(lds + KE_FLG);
    if (w == 11) {
        float a1[4] = {}, a2[4] = {};
#pragma unroll
        for (int k = 0; k < 36; ++k) { a1[k & 3] += __uint_as_float(pv[k][0]); a2[k & 3] += __uint_as_float(pv[k][1]); }
        const float s1 = (a1[0] + a1[1]) + (a1[2] + a1[3]), s2 = (a2[0] + a2[1]) + (a2[2] + a2[3]);
        const float mean = s1 * (1.0f / D), var = fmaxf(s2 * (1.0f / D) - mean * mean, 0.f);
        const float mb = rbf(mean), sb = ln_sd(var), al = __builtin_amdgcn_rcpf(sb);
        const float k0 = -2.0f * 0.7978845608028654f * 1.4426950408889634f;
        st[lane] = (f32x4){k0 * al, k0 * 0.044715f * al * al * al, sb, -mb};
        if (lane < 3) flg[lane] = 0u;
    }
    if (w == 10) { const bool ok4 = __all(fw4 == tag); if (lane == 0) flg[3] = ok4 ? 1u : 0u; }
    VM0(); LGKM0(); BAR();
    STAMP(ws, 17);
    const int z = i16 & 6;
    const int o0 = i16 * 128 + (g ^ z) * 16, o1 = i16 * 128 + ((4 + g) ^ z) * 16;
    {
        float nm[4];
#pragma unroll
        for (int mt = 0; mt < 4; ++mt) nm[mt] = st[mt * 16 + i16][3];
#pragma unroll
        for (int b = 0; b < NB; ++b) {
            const f32x4 cs = acc[b][0] + acc[b][1];
#pragma unroll
            for (int mt = 0; mt < 4; ++mt) acc[b][mt] = cs * nm[mt];
        }
    }
#define E_SPIN(cond) do { while (cond) __builtin_amdgcn_s_sleep(1); __builtin_amdgcn_sched_barrier(0); asm volatile("" ::: "memory"); } while (0)
#define E_FLG(i) __hip_atomic_load(flg + (i), __ATOMIC_RELAXED, __HIP_MEMORY_SCOPE_WORKGROUP)
    bf16x8 A[2][4];
#pragma unroll
    for (int ph = 0; ph < 2; ++ph) {
        const LAS char* ab = lds + ph * KE_HID;
        if (ph == 1) E_SPIN(E_FLG(0) < 4u);
#pragma unroll
        for (int mt = 0; mt < 4; ++mt) A[0][mt] = *(const LAS bf16x8*)(ab + o0 + mt * 2048);
#pragma unroll
        for (int s = 0; s < 18; ++s) {
            if (ph == 1 && s + 1 == S1) E_SPIN(E_FLG(1) < 4u);
            if (ph == 1 && s + 1 == S2) E_SPIN(E_FLG(2) < 4u);
            if (ph == 0 && s == 2 && w == 8 && E_FLG(3) == 0u) {
                wait_flags(ws, WS_FW, (3 + 2 * j) * 18, 18, WS_FW, 0, 0, tag);
                if (lane == 0) __hip_atomic_store(flg + 3, 1u, __ATOMIC_RELAXED, __HIP_MEMORY_SCOPE_WORKGROUP);
            }
            if (ph == 0 && s == 18 - RING) E_SPIN(E_FLG(3) == 0u);
            if (s + 1 < 18) {
#pragma unroll
                for (int mt = 0; mt < 4; ++mt) A[(s + 1) & 1][mt] = *(const LAS bf16x8*)(ab + (((s + 1) & 1) ? o1 : o0) + mt * 2048 + ((s + 1) >> 1) * 8192);
            }
            const int t = ph * 18 + s;
#pragma unroll
            for (int b = 0; b < NB; ++b) {
#pragma unroll
                for (int mt = 0; mt < 4; ++mt) acc[b][mt] = MFMA16(W[t % RING][b], A[s & 1][mt], acc[b][mt]);
                if (t + RING < 36) W[t % RING][b] = bld16<WAUX>(rw, wv + b * 1024, wsb + (t + RING) * 36864);
            }
            __builtin_amdgcn_sched_barrier(0);
        }
        if (ph == 0) {
            STAMP(ws, 18);
#pragma unroll
            for (int mt = 0; mt < 4; ++mt) {
                const f32x4 k = st[mt * 16 + i16];
#pragma unroll
                for (int b = 0; b < NB; ++b) {
                    float o[4];
#pragma unroll
                    for (int e = 0; e < 4; ++e) {
                        const float a = acc[b][mt][e];
                        const float ex = __builtin_amdgcn_exp2f(a * (k[0] + k[1] * (a * a)));
                        o[e] = a * __builtin_amdgcn_rcpf(k[2] + k[2] * ex);
                    }
                    const int nb = nbs + b;
                    *(LAS u32x2*)(lds + KE_HID + (nb >> 2) * 8192 + (mt * 16 + i16) * 128 + (((2 * (nb & 3) + (g >> 1)) ^ z) * 16) + 8 * (g & 1)) =
                        (u32x2){pk_bf16(o[0], o[1]), pk_bf16(o[2], o[3])};
                    acc[b][mt] = (f32x4){0.f, 0.f, 0.f, 0.f};
                }
            }
            LGKM0();
            if (lane == 0) __hip_atomic_fetch_add(flg + (w >> 2), 1u, __ATOMIC_RELAXED, __HIP_MEMORY_SCOPE_WORKGROUP);
            STAMP(ws, 19);
            if (w >= 8) __builtin_amdgcn_s_setprio(2); else if (w >= 4) __builtin_amdgcn_s_setprio(1);
        }
    }
#undef E_SPIN
#undef E_FLG
    __builtin_amdgcn_s_setprio(0);
    STAMP(ws, 20);
    const rsrc_t rp = mk_rsrc(ws + WS_PART + (size_t)(panel * 8 + j) * 72 * 1024, 72 * 1024);
#pragma unroll
    for (int b = 0; b < NB; ++b)
#pragma unroll
        for (int m2 = 0; m2 < 2; ++m2) {
            const f32x4 a0 = acc[b][2 * m2], a1 = acc[b][2 * m2 + 1];
            const u32x4 v = {pk_bf16(a0[0], a0[1]), pk_bf16(a0[2], a0[3]), pk_bf16(a1[0], a1[1]), pk_bf16(a1[2], a1[3])};
            const int pr = (nbs + b) * 2 + m2;
            if (pr >= 9 * j && pr < 9 * j + 9) *(LAS u32x4*)(lds + (pr - 9 * j) * 1024 + lane * 16) = v;
            else bst16(v, rp, lane * 16, pr * 1024);
        }
    LGKM0();
    VM0(); BAR();
    publish(ws, WS_FP, panel * 32 + j, tag);
}

__device__ __forceinline__ void phase_f(char* __restrict__ ws, LAS char* lds, u16* __restrict__ out, int panel, int jj, unsigned tag STAMP_ARG) {
    const int tid = threadIdx.x, w = __builtin_amdgcn_readfirstlane(tid >> 6), lane = tid & 63;
    const int g = lane >> 4, i16 = lane & 15;
    const int pr = jj * 9 + (w < 9 ? w : 0), nb = pr >> 1, m2 = pr & 1;
    u32x2 xa[2], ha[2];
    const int so = ((panel * 64 + m2 * 32) * D + nb * 16) * 2, vo = (i16 * D + 4 * g) * 2;
    if (w < 9) {
        const rsrc_t r1 = mk_rsrc(ws + WS_X1, L * D * 2), rr = mk_rsrc(ws + WS_HR, L * D * 2);
#pragma unroll
        for (int t = 0; t < 2; ++t) { xa[t] = bldu2<SC1>(r1, vo, so + t * 16 * D * 2); ha[t] = bldu2<SC1>(rr, vo, so + t * 16 * D * 2); }
    }
    if (w == 11) wait_panels(ws, WS_FP, panel, WS_FP, panel, WS_FP, panel, tag);
    BAR();
    STAMP(ws, 22);
    if (w < 9) {
        const rsrc_t rp = mk_rsrc(ws + WS_PART + (size_t)(panel * 8) * 72 * 1024, 8 * 72 * 1024);
        u32x4 v[8];
#pragma unroll
        for (int k = 0; k < 7; ++k) v[k] = bldu4<SC1>(rp, lane * 16, ((k < jj ? k : k + 1) * 72 + pr) * 1024);
        v[7] = *(const LAS u32x4*)(lds + w * 1024 + lane * 16);
        float s[8] = {};
#pragma unroll
        for (int k = 0; k < 8; ++k)
#pragma unroll
            for (int e = 0; e < 4; ++e) { s[2 * e] += bf_lo(v[k][e]); s[2 * e + 1] += bf_hi(v[k][e]); }
#pragma unroll
        for (int t = 0; t < 2; ++t) {
            float o[4];
#pragma unroll
            for (int k = 0; k < 4; ++k) {
                const float x1 = (k & 1) ? bf_hi(xa[t][k >> 1]) : bf_lo(xa[t][k >> 1]);
                const float h = (k & 1) ? bf_hi(ha[t][k >> 1]) : bf_lo(ha[t][k >> 1]);
                o[k] = h + (x1 + s[4 * t + k]);
            }
            __builtin_amdgcn_raw_buffer_store_b64((u32x2){pk_bf16(o[0], o[1]), pk_bf16(o[2], o[3])}, mk_rsrc(out, L * D * 2), vo, so + t * 16 * D * 2, OAUX);
        }
    }
}

constexpr int LDS_CTRL = 3 * CV_CH > KE_FLG + 32 ? 3 * CV_CH : KE_FLG + 32;
constexpr int LDS_BYTES = LDS_CTRL + 64;

NOPK __global__ __launch_bounds__(NTHR) void mega_kernel(Args a) {
    extern __shared__ __attribute__((aligned(16))) char smem[];
    LAS char* lds = (LAS char*)smem;
    const int tid = threadIdx.x, w = __builtin_amdgcn_readfirstlane(tid >> 6), lane = tid & 63, b = blockIdx.x;
    STAMP_DECL
    STAMP(a.ws, 0);
    unsigned cpf = 0;
    if (w == 8) {
        unsigned long long pc;
        asm volatile("s_getpc_b64 %0" : "=s"(pc));
        const int line = (b >> 3) * 13 + lane;
        if (lane < 13 && line * 128 < CODE_PF) cpf = *(const GAS unsigned*)((pc & ~255ull) + (unsigned long long)line * 128);
    }
    const unsigned tagv = __hip_atomic_load((gu32*)(a.ws + WS_EPOCH), RLX_AGENT);
    const StatRow sr = row_stats_load(a, w, lane);
    __builtin_amdgcn_sched_barrier(0);
    unsigned tag;
    if (b < 192) phase_conv(a, lds, tagv, sr, tag STAMP_PASS);
    else {
        tag = __builtin_amdgcn_readfirstlane(tagv) + 1u;
        if (tid == 0) __hip_atomic_store((gu32*)(a.ws + WS_FA) + b, tag, RLX_AGENT);
        if (tid == 0) *(LAS unsigned*)(lds + LDS_FWOK) = 0u;
        row_stats(a, sr, tag, w, lane);
        LGKM0(); BAR();
        STAMP(a.ws, 1);
        const int i = w * 64 + (b - 192);
        if (i >= 36) {
            const unsigned long long t0 = __builtin_amdgcn_s_memrealtime();
            while (__builtin_amdgcn_s_memrealtime() - t0 < PREP_DELAY) __builtin_amdgcn_s_sleep(8);
        }
        if (i < 180) {
            const int job = i < 36 ? i : (2 + 2 * ((i - 36) / 18)) * 18 + (i - 36) % 18;
            PrepRegs P;
            prep_issue(a, job, lane, P);
            prep_finish<true, true>(a, job, lds + w * 8192, lane, tag, P STAMP_PASS);
        }
        BAR();
    }
    asm volatile("" :: "v"(cpf));
    STAMP(a.ws, 7);
    const int je = 2 * (b & 3) + ((b >> 3) & 1);
    {
        const int panel = (b & 7) * 4 + (b >> 6), j = (b >> 3) & 7;
        const int m = (w - 8) * 128 + ((b & 7) | ((b >> 4) << 3));
        if ((j & 1) && (w == 8 || w == 9) && m < 144) phase_cd_background(a, lds, panel, (3 + 2 * (m / 18)) * 18 + m % 18, tag STAMP_PASS);
        else {
            phase_c(a.ws, lds, panel, j, tag STAMP_PASS);
            STAMP(a.ws, 11);
            phase_d(a.ws, lds, panel, j, je, tag STAMP_PASS);
        }
    }
    STAMP(a.ws, 15);
    phase_e(a.ws, lds, ((b >> 2) & 1) * 16 + (b >> 4), je, tag STAMP_PASS);
    STAMP(a.ws, 21);
    phase_f(a.ws, lds, a.out, ((b >> 2) & 1) * 16 + (b >> 4), je, tag STAMP_PASS);
    if (b == 0 && w == 10) {
        wait_flags(a.ws, WS_FA, 0, 256, WS_FA, 0, 0, tag);
        if (lane == 0) __hip_atomic_store((gu32*)(a.ws + WS_EPOCH), tag, RLX_AGENT);
    }
    STAMP(a.ws, 23);
    STAMP_FLUSH(a.ws);
}

extern "C" void kernel_launch(void* const* d_in, const int* in_sizes, int n_in,
                              void* d_out, int out_size, void* d_ws, size_t ws_size,
                              hipStream_t stream) {
    Args a;
    a.x = (const u16*)d_in[0];
    a.are = (const u16*)d_in[1]; a.aim = (const u16*)d_in[2];
    a.bre = (const u16*)d_in[3]; a.bim = (const u16*)d_in[4];
    a.cre = (const u16*)d_in[5]; a.cim = (const u16*)d_in[6];
    a.logdt = (const u16*)d_in[7]; a.dskip = (const u16*)d_in[8];
    a.w1 = (const u16*)d_in[10]; a.w2 = (const u16*)d_in[11];
    a.w3 = (const u16*)d_in[12]; a.w4 = (const u16*)d_in[13];
    a.ws = (char*)d_ws; a.out = (u16*)d_out;
    (void)hipFuncSetAttribute((const void*)mega_kernel, hipFuncAttributeMaxDynamicSharedMemorySize, LDS_BYTES);
    mega_kernel<<<NWG, NTHR, LDS_BYTES, stream>>>(a);
}
```

```cpp
#include <hip/hip_runtime.h>
#include <hip/hip_bf16.h>
#include <stdint.h>

typedef unsigned short u16;
typedef short bf16x8 __attribute__((ext_vector_type(8)));
typedef short s16x4 __attribute__((ext_vector_type(4)));
typedef float f32x2 __attribute__((ext_vector_type(2)));
typedef float f32x4 __attribute__((ext_vector_type(4)));
typedef float f32x16 __attribute__((ext_vector_type(16)));
typedef unsigned u32x2 __attribute__((ext_vector_type(2)));
typedef unsigned u32x4 __attribute__((ext_vector_type(4)));
typedef __bf16 bf16x2_t __attribute__((ext_vector_type(2)));
typedef _Float16 f16x2 __attribute__((ext_vector_type(2)));
typedef _Float16 f16x8 __attribute__((ext_vector_type(8)));
#define LAS __attribute__((address_space(3)))
#define GAS __attribute__((address_space(1)))
#if defined(__HIP_DEVICE_COMPILE__)
#define NOPK __attribute__((target("no-packed-fp32-ops")))
#else
#define NOPK
#endif

constexpr int L = 2048, D = 576, NS = 128, F = 4608;
constexpr int WSUB = D * D;
constexpr float LN_EPS = 1e-5f;
constexpr int NWG = 256, NTHR = 768;
constexpr int SC1 = 16;

constexpr size_t WS_EPOCH = 0;
constexpr int REP = 8;
constexpr size_t WS_FA = 256;
constexpr size_t WS_FW = WS_FA + 1024;
constexpr size_t WS_FG = WS_FW + 2048;
constexpr size_t WS_FX = WS_FG + 4096;
constexpr size_t WS_FP = WS_FX + 4096;
constexpr size_t WS_FH = WS_FP + 4096;
constexpr size_t WS_GR = 32768;
constexpr size_t WS_HT = WS_GR + (size_t)L * 8;
constexpr size_t WS_WF = WS_HT + (size_t)D * L * 2;
constexpr size_t WS_CS = WS_WF + (size_t)18 * WSUB * 2;
constexpr size_t WS_ST2 = WS_CS + (size_t)2 * (D + F) * 4;
constexpr size_t WS_G = WS_ST2 + (size_t)L * 8;
constexpr size_t WS_X1 = WS_G + (size_t)(L + 4) * D * 2;
constexpr size_t WS_HR = WS_X1 + (size_t)L * D * 2;
constexpr size_t WS_PST = WS_HR + (size_t)L * D * 2;
constexpr size_t WS_PART = WS_PST + (size_t)32 * 36 * 64 * 8;
constexpr size_t WS_END = WS_PART + (size_t)32 * 8 * 72 * 512 * 2;

__device__ __forceinline__ unsigned pk_bf16(float lo, float hi) {
    f32x2 v = {lo, hi};
    return __builtin_bit_cast(unsigned, __builtin_convertvector(v, bf16x2_t));
}
__device__ __forceinline__ float bf_lo(unsigned p) { return __uint_as_float(p << 16); }
__device__ __forceinline__ float bf_hi(unsigned p) { return __uint_as_float(p & 0xffff0000u); }
__device__ __forceinline__ float bf2f(u16 x) { return __uint_as_float((unsigned)x << 16); }
__device__ __forceinline__ float rbf(float x) { return bf_lo(pk_bf16(x, x)); }
__device__ __forceinline__ float dot2bf(unsigned a, unsigned b, float c) {
    return __builtin_amdgcn_fdot2_f32_bf16(__builtin_bit_cast(bf16x2_t, a), __builtin_bit_cast(bf16x2_t, b), c, false);
}
__device__ __forceinline__ float gelu_tanh(float x) {
    const float c0 = -2.0f * 0.7978845608028654f * 1.4426950408889634f;
    const float c1 = c0 * 0.044715f;
    float e = __builtin_amdgcn_exp2f(x * (c0 + c1 * x * x));
    return x * __builtin_amdgcn_rcpf(1.0f + e);
}
__device__ __forceinline__ s16x4 tr_read(const LAS char* p) {
    return __builtin_amdgcn_ds_read_tr16_b64_v4i16((LAS s16x4*)p);
}
__device__ __forceinline__ bf16x8 cat8(s16x4 a, s16x4 b) {
    return __builtin_shufflevector(a, b, 0, 1, 2, 3, 4, 5, 6, 7);
}
__device__ __forceinline__ f32x4 sel4(bool c, f32x4 a, f32x4 b) {
    f32x4 r;
#pragma unroll
    for (int k = 0; k < 4; ++k) { float x = a[k], y = b[k]; asm volatile("" : "+v"(x), "+v"(y)); r[k] = c ? x : y; }
    return r;
}
__device__ __forceinline__ float rsum_g(float x) {
    const unsigned u = __float_as_uint(x);
    const auto a = __builtin_amdgcn_permlane16_swap(u, u, false, false);
    const float y = __uint_as_float(a[0]) + __uint_as_float(a[1]);
    const unsigned v = __float_as_uint(y);
    const auto b = __builtin_amdgcn_permlane32_swap(v, v, false, false);
    return __uint_as_float(b[0]) + __uint_as_float(b[1]);
}
template <int CTRL> __device__ __forceinline__ float dpp_f(float x) {
    return __uint_as_float(__builtin_amdgcn_update_dpp(0u, __float_as_uint(x), CTRL, 0xf, 0xf, false));
}
__device__ __forceinline__ float rsum_64(float x) {
    x += dpp_f<0xB1>(x);
    x += dpp_f<0x4E>(x);
    x += dpp_f<0x141>(x);
    x += dpp_f<0x140>(x);
    return rsum_g(x);
}
template <int CTRL> __device__ __forceinline__ unsigned dpp_u(unsigned x) { return __builtin_amdgcn_update_dpp(0u, x, CTRL, 0xf, 0xf, false); }
__device__ __forceinline__ unsigned umax2(unsigned a, unsigned b) { return a > b ? a : b; }
__device__ __forceinline__ float rmax_64(float xf) {
    unsigned x = __float_as_uint(xf);
    x = umax2(x, dpp_u<0xB1>(x)); x = umax2(x, dpp_u<0x4E>(x)); x = umax2(x, dpp_u<0x141>(x)); x = umax2(x, dpp_u<0x140>(x));
    const auto a = __builtin_amdgcn_permlane16_swap(x, x, false, false); x = umax2(a[0], a[1]);
    const auto b = __builtin_amdgcn_permlane32_swap(x, x, false, false); x = umax2(b[0], b[1]);
    return __uint_as_float(x);
}
__device__ __forceinline__ float ln_sd(float var) { return rbf(__builtin_amdgcn_sqrtf(rbf(rbf(var) + LN_EPS))); }
__device__ __forceinline__ int fsw(int row) { return ((row >> 1) & 1) | (((row >> 3) & 1) << 1); }
#define MFMA16(a, b, c) __builtin_amdgcn_mfma_f32_16x16x32_bf16((a), (b), (c), 0, 0, 0)
#define MFMA32(a, b, c) __builtin_amdgcn_mfma_f32_32x32x16_bf16((a), (b), (c), 0, 0, 0)
#define VM0() asm volatile("s_waitcnt vmcnt(0)" ::: "memory")
#define LGKM0() asm volatile("s_waitcnt lgkmcnt(0)" ::: "memory")
#define BAR() do { asm volatile("" ::: "memory"); __builtin_amdgcn_s_barrier(); asm volatile("" ::: "memory"); } while (0)

typedef __amdgpu_buffer_rsrc_t rsrc_t;
__device__ __forceinline__ rsrc_t mk_rsrc(const void* p, unsigned bytes) { return __builtin_amdgcn_make_buffer_rsrc((void*)p, 0, bytes, 0x00020000); }
template <int AUX> __device__ __forceinline__ bf16x8 bld16(rsrc_t r, int voff, int soff) {
    return __builtin_bit_cast(bf16x8, __builtin_amdgcn_raw_buffer_load_b128(r, voff, soff, AUX));
}
template <int AUX> __device__ __forceinline__ f32x4 bldf4(rsrc_t r, int voff, int soff) {
    return __builtin_bit_cast(f32x4, __builtin_amdgcn_raw_buffer_load_b128(r, voff, soff, AUX));
}
template <int AUX> __device__ __forceinline__ u32x4 bldu4(rsrc_t r, int voff, int soff) {
    return __builtin_amdgcn_raw_buffer_load_b128(r, voff, soff, AUX);
}
template <int AUX> __device__ __forceinline__ u32x2 bldu2(rsrc_t r, int voff, int soff) {
    return __builtin_amdgcn_raw_buffer_load_b64(r, voff, soff, AUX);
}
template <int AUX> __device__ __forceinline__ void blds16(rsrc_t r, LAS char* l, int voff, int soff) {
    __builtin_amdgcn_raw_ptr_buffer_load_lds(r, (LAS void*)l, 16, voff, soff, 0, AUX);
}
__device__ __forceinline__ void bst16(u32x4 v, rsrc_t r, int voff, int soff) { __builtin_amdgcn_raw_buffer_store_b128(v, r, voff, soff, SC1); }
__device__ __forceinline__ void bst8(u32x2 v, rsrc_t r, int voff, int soff) { __builtin_amdgcn_raw_buffer_store_b64(v, r, voff, soff, SC1); }
__device__ __forceinline__ void bst4(unsigned v, rsrc_t r, int voff, int soff) { __builtin_amdgcn_raw_buffer_store_b32(v, r, voff, soff, SC1); }

struct Args {
    const u16* x; const u16 *are, *aim, *bre, *bim, *cre, *cim, *logdt, *dskip;
    const u16 *w1, *w2, *w3, *w4;
    char* ws; u16* out;
};

typedef GAS unsigned gu32;
typedef GAS unsigned long long gu64;
#define RLX_AGENT __ATOMIC_RELAXED, __HIP_MEMORY_SCOPE_AGENT
constexpr unsigned long long PREP_DELAY = 100ull;
constexpr int CODE_PF = 41984;
constexpr unsigned long long WAIT_TICKS = 400000ull;
__device__ __forceinline__ void publish(char* ws, size_t flag_off, int idx, unsigned tag) {
    if (threadIdx.x == 0) __hip_atomic_store((gu32*)(ws + flag_off) + idx, tag, RLX_AGENT);
}
__device__ __forceinline__ void publish_rep(char* ws, size_t flag_off, int idx, unsigned tag, int lane) {
    if (lane < REP) __hip_atomic_store((gu32*)(ws + flag_off) + lane * 256 + idx, tag, RLX_AGENT);
}
__device__ __forceinline__ void wait_flags(char* ws, size_t offa, int a0, int na, size_t offb, int b0, int nb, unsigned tag) {
    const int lane = threadIdx.x & 63;
    const gu32* p[4]; bool on[4];
#pragma unroll
    for (int k = 0; k < 4; ++k) {
        const int i = lane + 64 * k;
        on[k] = i < na + nb;
        p[k] = i < na ? (const gu32*)(ws + offa) + a0 + i : (const gu32*)(ws + offb) + b0 + (on[k] ? i - na : 0);
    }
    const unsigned long long t0 = __builtin_amdgcn_s_memrealtime();
    for (;;) {
        bool ok = true;
#pragma unroll
        for (int k = 0; k < 4; ++k)
            if (64 * k < na + nb) { const unsigned v = __hip_atomic_load(p[k], RLX_AGENT); ok = ok && (!on[k] || v == tag); }
        if (__all(ok)) break;
        if (__builtin_amdgcn_s_memrealtime() - t0 > WAIT_TICKS) break;
        __builtin_amdgcn_s_sleep(2);
    }
}
__device__ __forceinline__ void wait_panels(char* ws, size_t o0, int p0, size_t o1, int p1, size_t o2, int p2, unsigned tag) {
    const int lane = threadIdx.x & 63, k = (lane >> 3) % 3;
    const gu32* p = (const gu32*)(ws + (k == 0 ? o0 : k == 1 ? o1 : o2)) + (k == 0 ? p0 : k == 1 ? p1 : p2) * 32 + (lane & 7);
    const unsigned long long t0 = __builtin_amdgcn_s_memrealtime();
    for (;;) {
        if (__all(__hip_atomic_load(p, RLX_AGENT) == tag)) break;
        if (__builtin_amdgcn_s_memrealtime() - t0 > WAIT_TICKS) break;
        __builtin_amdgcn_s_sleep(2);
    }
}
struct StatRow { u32x4 v0, v1; };
__device__ __forceinline__ StatRow row_stats_load(const Args& a, int w, int lane) {
    const int row = blockIdx.x * 8 + (w & 7);
    const rsrc_t rx = mk_rsrc(a.x + (size_t)row * D, w < 8 ? D * 2 : 0);
    StatRow r;
    r.v0 = bldu4<0>(rx, lane * 16, 0); r.v1 = bldu4<0>(rx, 1024 + lane * 16, 0);
    return r;
}
__device__ __forceinline__ void row_stats(const Args& a, const StatRow& r, unsigned tag, int w, int lane) {
    if (w < 8) {
        float s1 = 0.f, s2 = 0.f;
#pragma unroll
        for (int k = 0; k < 4; ++k) {
            s1 = dot2bf(r.v0[k], 0x3f803f80u, s1); s2 = dot2bf(r.v0[k], r.v0[k], s2);
            s1 = dot2bf(r.v1[k], 0x3f803f80u, s1); s2 = dot2bf(r.v1[k], r.v1[k], s2);
        }
        s1 = rsum_64(s1); s2 = rsum_64(s2);
        const float mean = s1 * (1.0f / D), var = fmaxf(s2 * (1.0f / D) - mean * mean, 0.f);
        const unsigned val = (pk_bf16(mean, ln_sd(var)));
        if (lane == 0) __hip_atomic_store((gu64*)(a.ws + WS_GR) + blockIdx.x * 8 + w, ((unsigned long long)tag << 32) | val, RLX_AGENT);
    }
}

struct PrepRegs { u32x4 R[36]; };
__device__ __forceinline__ void prep_issue(const Args& a, int job, int lane, PrepRegs& P) {
    const int sub = job / 18, rem = job % 18, kh = rem / 9, ns = rem % 9;
    const u16* src; int ld;
    if (sub == 0) { src = a.w1; ld = D; }
    else if (sub == 1) { src = a.w2; ld = D; }
    else if ((sub & 1) == 0) { src = a.w3 + ((sub - 2) >> 1) * D; ld = F; }
    else { src = a.w4 + (size_t)((sub - 2) >> 1) * D * D; ld = D; }
    src += (size_t)(kh * 288) * ld + ns * 64;
    const rsrc_t rs = mk_rsrc(src, 288 * ld * 2);
    const int ldb = ld * 2;
    const int vo = (lane >> 3) * ldb + (lane & 7) * 16;
#pragma unroll
    for (int i = 0; i < 36; ++i) P.R[i] = bldu4<0>(rs, vo, i * 8 * ldb);
    __builtin_amdgcn_sched_barrier(0);
}
template <bool PUBLISH, bool COLSUM>
__device__ __forceinline__ void prep_finish(const Args& a, int job, LAS char* lds, int lane, unsigned tag, const PrepRegs& P) {
    const int sub = job / 18, rem = job % 18, kh = rem / 9, ns = rem % 9;
    const rsrc_t rd = mk_rsrc(a.ws + WS_WF + (size_t)sub * WSUB * 2, WSUB * 2);
    const int fl = (lane >> 4) & 1;
    const int w0 = (lane >> 3) * 128 + (((lane & 7) ^ (fl << 1)) * 16), w1 = (lane >> 3) * 128 + (((lane & 7) ^ ((fl | 2) << 1)) * 16);
    const int g = lane >> 4, i16 = lane & 15, q = i16 >> 2, p = lane & 3;
    const int f = ((q >> 1) & 1) | ((g & 1) << 1);
    int rb[4];
#pragma unroll
    for (int n = 0; n < 4; ++n) rb[n] = (8 * g + q) * 128 + (((2 * n + (p >> 1)) ^ (f << 1)) * 16) + 8 * (p & 1);
    float cs[4] = {0.f, 0.f, 0.f, 0.f};
#pragma unroll
    for (int u = 0; u < 9; ++u) {
        LAS char* sl = lds + (u & 1) * 4096;
#pragma unroll
        for (int i = 0; i < 4; ++i) *(LAS u32x4*)(sl + i * 1024 + ((i & 1) ? w1 : w0)) = P.R[4 * u + i];
#pragma unroll
        for (int n = 0; n < 4; ++n) {
            const u32x4 x = __builtin_bit_cast(u32x4, cat8(tr_read(sl + rb[n]), tr_read(sl + rb[n] + 512)));
            bst16(x, rd, lane * 16, ((kh * 9 + u) * 36 + ns * 4 + n) * 1024);
            if (COLSUM) {
#pragma unroll
                for (int k = 0; k < 4; ++k) cs[n] = dot2bf(x[k], 0x3f803f80u, cs[n]);
            }
        }
    }
    if (COLSUM && (sub & 1) == 0) {
        const rsrc_t rc = mk_rsrc(a.ws + WS_CS, 2 * (D + F) * 4);
#pragma unroll
        for (int n = 0; n < 4; ++n) {
            const float c = rsum_g(cs[n]);
            if (lane < 16) bst4(__float_as_uint(c), rc, lane * 4, (kh * (D + F) + (sub == 0 ? 0 : D + ((sub - 2) >> 1) * D) + ns * 64 + n * 16) * 4);
        }
    }
    if (PUBLISH) {
        VM0();
        if (lane == 0) __hip_atomic_store((gu32*)(a.ws + WS_FW) + job, tag, RLX_AGENT);
    }
}

constexpr int CV_RC = 4160;
constexpr int CV_U = 4 * CV_RC;
constexpr int CV_X = CV_U + 2288 * 2;
constexpr int CV_RED = CV_X + 4096;
constexpr int CV_CH = CV_RED + 24576;
constexpr int KG_ROW = 544;
constexpr int KG_XI = 32 * KG_ROW, KG_T = CV_RED + 4096;

struct cpx { float re, im; };
__device__ __forceinline__ cpx cmul(cpx a, cpx b) { return {a.re * b.re - a.im * b.im, a.re * b.im + a.im * b.re}; }
__device__ __forceinline__ cpx cexp_rev(float a2, float rev, float m) {
    const float e = __builtin_amdgcn_exp2f(a2 * m), p = __builtin_amdgcn_fractf(rev * m);
    return {e * __builtin_amdgcn_cosf(p), e * __builtin_amdgcn_sinf(p)};
}
__device__ __forceinline__ unsigned pk_f16(cpx c) { f32x2 v = {c.re, c.im}; return __builtin_bit_cast(unsigned, __builtin_convertvector(v, f16x2)); }

__device__ __forceinline__ void conv_put(LAS char* lds, int m, u32x4 A, u32x2 B) {
    u32x4 c1 = {__builtin_amdgcn_alignbit(A[1], A[0], 16), __builtin_amdgcn_alignbit(A[2], A[1], 16), __builtin_amdgcn_alignbit(A[3], A[2], 16), __builtin_amdgcn_alignbit(B[0], A[3], 16)};
    u32x4 c2 = {A[1], A[2], A[3], B[0]};
    u32x4 c3 = {c1[1], c1[2], c1[3], __builtin_amdgcn_alignbit(B[1], B[0], 16)};
    *(LAS u32x4*)(lds + 0 * CV_RC + m * 16) = A;
    *(LAS u32x4*)(lds + 1 * CV_RC + m * 16) = c1;
    *(LAS u32x4*)(lds + 2 * CV_RC + m * 16) = c2;
    *(LAS u32x4*)(lds + 3 * CV_RC + m * 16) = c3;
}

constexpr int LDS_FWOK = 3 * CV_CH;
__device__ __forceinline__ void phase_conv(const Args& a, LAS char* lds0, unsigned tagv, const StatRow& sr, unsigned& tag_out) {
    const int tid = threadIdx.x, w = __builtin_amdgcn_readfirstlane(tid >> 6), lane = tid & 63;
    const int b = blockIdx.x, chl = w >> 2, wq = w & 3, t8 = tid & 255, ch = 3 * b + chl;
    LAS char* lds = lds0 + chl * CV_CH;
    const int part = wq >> 1, n = (wq & 1) * 64 + lane;
    const int o = ch * NS + n, o2 = ch * NS + (n ^ 64);
    const u16 r_ld = a.logdt[ch], r_ar = a.are[o], r_ai = a.aim[o], r_ar2 = a.are[o2];
    u16 r_b[4] = {}, r_c[4] = {};
    if (part) { r_b[0] = a.bre[o]; r_b[1] = a.bim[o]; r_c[0] = a.cre[o]; r_c[1] = a.cim[o]; r_b[2] = a.bre[o2]; r_b[3] = a.bim[o2]; r_c[2] = a.cre[o2]; r_c[3] = a.cim[o2]; }
    const u16 r_ds = a.dskip[ch];
    __builtin_amdgcn_sched_barrier(0);
    const unsigned tag = __builtin_amdgcn_readfirstlane(tagv) + 1u;
    tag_out = tag;
    if (tid == 0) __hip_atomic_store((gu32*)(a.ws + WS_FA) + b, tag, RLX_AGENT);
    row_stats(a, sr, tag, w, lane);
    __builtin_amdgcn_sched_barrier(0);
    u32x2 xr[4];
    const rsrc_t rx = mk_rsrc(a.x, L * D * 2);
    const int xvo = tid * (2 * D * 2) + ((6 * b) & ~3);
#define GATHER(i) do { __builtin_amdgcn_sched_barrier(0); if ((i) < 2 || w < 4) xr[i] = bldu2<0>(rx, xvo + ((i) & 1) * D * 2, ((i) >> 1) * 1536 * D * 2); __builtin_amdgcn_sched_barrier(0); } while (0)
    GATHER(0);
    const float dt = rbf(expf(bf2f(r_ld)));
    const float are = dt * bf2f(r_ar);
    const float ap = rmax_64(fmaxf(fmaxf(are, dt * bf2f(r_ar2)), 0.f));
    const float a2 = (are - ap) * 1.4426950408889634f, rev = dt * bf2f(r_ai) * 0.15915494309189535f;
    float kscale = 1.f;
    if (part == 0) {
        cpx e[4];
        const cpx g8 = cexp_rev(a2, rev, 8.f), g32 = cexp_rev(a2, rev, 32.f);
        e[0] = {1.f, 0.f}; e[1] = g8; e[2] = cmul(g8, g8); e[3] = cmul(e[2], g8);
        LAS char* er = lds + n * 4;
#pragma unroll
        for (int t = 0; t < 8; ++t) {
            if (t == 0) GATHER(1);
            if (t == 3) GATHER(2);
            if (t == 6) GATHER(3);
#pragma unroll
            for (int c = 0; c < 4; ++c) {
                *(LAS unsigned*)(er + (4 * t + c) * KG_ROW) = pk_f16(e[c]);
                if (t < 7) e[c] = (t == 0 && c == 0) ? g32 : cmul(e[c], g32);
            }
        }
    } else {
        const float b0 = dt * bf2f(r_b[0]), b1 = dt * bf2f(r_b[1]), c0 = bf2f(r_c[0]), c1 = bf2f(r_c[1]);
        const float b2 = dt * bf2f(r_b[2]), b3 = dt * bf2f(r_b[3]), c2 = bf2f(r_c[2]), c3 = bf2f(r_c[3]);
        const float wr = c0 * b0 - c1 * b1, wi = c0 * b1 + c1 * b0, vr = c2 * b2 - c3 * b3, vi = c2 * b3 + c3 * b2;
        const float wm = rmax_64(fmaxf(fmaxf(fabsf(wr), fabsf(wi)), fmaxf(fabsf(vr), fabsf(vi))));
        const unsigned ex = (__float_as_uint(wm) >> 23) & 0xffu;
        const unsigned exc = ex < 2u ? 2u : (ex > 252u ? 252u : ex);
        const float sc = __uint_as_float((254u - exc) << 23);
        kscale = __uint_as_float(exc << 23);
        const cpx W = {wr * sc, -wi * sc};
        const cpx g1 = cexp_rev(a2, -rev, 1.f), p256 = cexp_rev(a2, -rev, 256.f);
        const cpx g2 = cmul(g1, g1), g4 = cmul(g2, g2), p512 = cmul(p256, p256);
        LAS char* xr_ = lds + KG_XI + n * 4;
#define XST(i, v) *(LAS unsigned*)(xr_ + (i) * KG_ROW) = pk_f16(v)
        GATHER(1);
        cpx x = W; XST(0, x);
#pragma unroll
        for (int i = 1; i < 8; ++i) { x = cmul(x, g1); XST(i, x); }
        GATHER(2);
        x = cmul(W, p256); XST(8, x);
#pragma unroll
        for (int i = 9; i < 12; ++i) { x = cmul(x, g2); XST(i, x); }
        cpx y = cmul(W, p512); XST(12, y); XST(13, cmul(y, g4));
        x = cmul(y, p256); XST(14, x); XST(15, cmul(x, g4));
        GATHER(3);
        y = cmul(y, p512); XST(16, y);
#pragma unroll
        for (int i = 17; i < 21; ++i) { y = cmul(y, p256); XST(i, y); }
#undef XST
#undef GATHER
        if (lane == 0 && wq == 2) *(LAS float*)(lds + KG_T - 16) = kscale;
    }
    const int p = lane & 15, g = lane >> 4;
    LAS float* T = (LAS float*)(lds + KG_T);
    int base, idx0, istr;
    if (wq & 1) {
        base = p < 4 ? 1024 + 256 * p : (p == 4 ? 2048 : 0);
        idx0 = p < 4 ? 512 + 32 * p : (p == 4 ? 640 : 680);
        istr = 1;
    } else {
        const int q4 = p & 3;
        base = p < 8 ? p : (p < 12 ? 256 + 2 * q4 : 512 + 4 * (p & 1) + 128 * (p & 2));
        idx0 = p < 8 ? p : (p < 12 ? 256 + q4 : 384 + (p & 1) + 32 * (p & 2));
        istr = p < 8 ? 8 : (p < 12 ? 4 : 2);
    }
    const int j0 = (wq >> 1) * 16 + 4 * g;
    LAS float* tw = T + idx0 + j0 * istr;
    const LAS char* ea = lds + ((wq >> 1) * 16 + p) * KG_ROW + g * 16;
    const LAS char* xa = lds + KG_XI + ((wq & 1) * 16 + p) * KG_ROW + g * 16;
    LGKM0(); BAR();
    {
        f32x4 acc = {};
        f16x8 A[8], B[8];
#pragma unroll
        for (int t = 0; t < 8; ++t) { A[t] = *(const LAS f16x8*)(ea + t * 64); B[t] = *(const LAS f16x8*)(xa + t * 64); }
        float ks = *(const LAS float*)(lds + KG_T - 16);
        __builtin_amdgcn_sched_barrier(0);
#pragma unroll
        for (int t = 0; t < 8; ++t) acc = __builtin_amdgcn_mfma_f32_16x16x32_f16(A[t], B[t], acc, 0, 0, 0);
        if (ap > 0.f) {
#pragma unroll
            for (int q = 0; q < 4; ++q) acc[q] *= __builtin_amdgcn_exp2f(ap * 1.4426950408889634f * (float)(base + 8 * (j0 + q)));
        }
#pragma unroll
        for (int q = 0; q < 4; ++q) tw[q * istr] = acc[q] * ks;
    }
    LGKM0(); BAR();
    {
        float v[12];
        float lf = (float)(2047 - t8 * 8);
        asm volatile("" : "+v"(lf));
        const unsigned tb = (unsigned)(size_t)T - 17024u * 4u;
        if (wq < 3) {
#pragma unroll
            for (int e = 0; e < 12; e += 2) {
                const unsigned bits = pk_bf16(lf - (float)e, lf - (float)(e + 1));
                v[e] = *(const LAS float*)(size_t)(((bits & 0xffffu) << 2) + tb);
                v[e + 1] = *(const LAS float*)(size_t)(((bits >> 16) << 2) + tb);
            }
        } else {
#pragma unroll
            for (int e = 0; e < 12; ++e) {
                const int l = 2047 - (t8 * 8 + e);
                const int idx = l < 128 ? (l < 0 ? 0 : l) : (int)(pk_bf16((float)l, 0.f) & 0xffffu) - 17024;
                const float t = T[idx];
                v[e] = l < 0 ? 0.f : t;
            }
        }
        conv_put(lds, t8, (u32x4){pk_bf16(v[0], v[1]), pk_bf16(v[2], v[3]), pk_bf16(v[4], v[5]), pk_bf16(v[6], v[7])}, (u32x2){pk_bf16(v[8], v[9]), pk_bf16(v[10], v[11])});
        if (t8 < 4) conv_put(lds, 256 + t8, (u32x4){0u, 0u, 0u, 0u}, (u32x2){0u, 0u});
        if (t8 >= 64 && t8 < 94) *(LAS u32x4*)(lds + CV_U + (t8 - 64) * 16) = (u32x4){0u, 0u, 0u, 0u};
    }
    {
        u32x4 gv[2];
        const rsrc_t rgn = mk_rsrc(a.ws + WS_GR, L * 8);
        const unsigned long long t0 = __builtin_amdgcn_s_memrealtime();
        for (;;) {
            gv[0] = bldu4<SC1>(rgn, tid * 16, 0);
            gv[1] = w < 4 ? bldu4<SC1>(rgn, tid * 16, 1536 * 8) : gv[0];
            const bool ok = gv[0][1] == tag && gv[0][3] == tag && gv[1][1] == tag && gv[1][3] == tag;
            if (__all(ok)) break;
            if (__builtin_amdgcn_s_memrealtime() - t0 > WAIT_TICKS) break;
            __builtin_amdgcn_s_sleep(2);
            asm volatile("" ::: "memory");
        }
#pragma unroll
        for (int i = 0; i < 2; ++i) {
            if (i == 0 || w < 4) {
                const int row = 2 * tid + 1536 * i;
                u32x2 x0 = xr[2 * i], x1 = xr[2 * i + 1];
                if (b & 1) {
                    x0 = (u32x2){__builtin_amdgcn_alignbit(x0[1], x0[0], 16), x0[1] >> 16};
                    x1 = (u32x2){__builtin_amdgcn_alignbit(x1[1], x1[0], 16), x1[1] >> 16};
                }
                const float m0 = bf_lo(gv[i][0]), m1 = bf_lo(gv[i][2]);
                const float s0 = __builtin_amdgcn_rcpf(bf_hi(gv[i][0])), s1 = __builtin_amdgcn_rcpf(bf_hi(gv[i][2]));
#pragma unroll
                for (int c = 0; c < 3; ++c) {
                    const unsigned xp = c == 0 ? __builtin_amdgcn_perm(x1[0], x0[0], 0x05040100u) : c == 1 ? __builtin_amdgcn_perm(x1[0], x0[0], 0x07060302u)
                                                                                                            : __builtin_amdgcn_perm(x1[1], x0[1], 0x05040100u);
                    const unsigned dd = pk_bf16(bf_lo(xp) - m0, bf_hi(xp) - m1);
                    *(LAS unsigned*)(lds0 + c * CV_CH + CV_U + 480 + row * 2) = pk_bf16(bf_lo(dd) * s0, bf_hi(dd) * s1);
                    *(LAS unsigned*)(lds0 + c * CV_CH + CV_X + row * 2) = xp;
                }
            }
        }
    }
    LGKM0(); BAR();
    unsigned fwv = tag;
    if (w == 11 && lane < 36) fwv = __hip_atomic_load((gu32*)(a.ws + WS_FW) + lane, RLX_AGENT);
    f32x4 acc[8];
#pragma unroll
    for (int c = 0; c < 8; ++c) acc[c] = (f32x4){0.f, 0.f, 0.f, 0.f};
    const int cpy = (3 - p) & 3;
#pragma unroll
    for (int rr = 0; rr < 2; ++rr) {
        const int r = 2 * wq + rr;
        const LAS char* ab = lds + cpy * CV_RC + 2 * (2031 - 32 * r + 8 * g - p - cpy) - 512 * 7;
        const LAS char* fb = lds + CV_U + 2 * (240 - 32 * r + 16 * p - 16 + 8 * g);
        bf16x8 A[8], Fm[8];
#pragma unroll
        for (int s = 0; s < 8; ++s) {
            const s16x4 lo = *(const volatile LAS s16x4*)(ab + 512 * (7 - s));
            const s16x4 hi = *(const volatile LAS s16x4*)(ab + 512 * (7 - s) + 8);
            A[s] = cat8(lo, hi);
        }
#pragma unroll
        for (int e = 0; e < 8; ++e) Fm[e] = *(const LAS bf16x8*)(fb + 512 * e);
#pragma unroll
        for (int c = 0; c < 8; ++c)
#pragma unroll
            for (int s = 0; s <= c; ++s) acc[c] = MFMA16(A[s], Fm[c - s], acc[c]);
    }
    LAS f32x4* red = (LAS f32x4*)(lds + CV_RED);
#pragma unroll
    for (int c = 0; c < 8; ++c) {
        const int ow = c >> 1;
        if (ow != wq) red[(ow * 6 + (c & 1) * 3 + (wq > ow ? wq - 1 : wq)) * 64 + lane] = acc[c];
    }
    if (w == 11) { const bool okw = __all(fwv == tag); if (lane == 0) *(LAS unsigned*)(lds0 + LDS_FWOK) = okw ? 1u : 0u; }
    LGKM0(); BAR();
    const rsrc_t rh = mk_rsrc(a.ws + WS_HT + (size_t)ch * L * 2, L * 2);
    const float dsk = bf2f(r_ds);
#pragma unroll
    for (int i = 0; i < 2; ++i) {
        f32x4 y;
#pragma unroll
        for (int k = 0; k < 4; ++k) {
            const float y01 = (wq & 1) ? acc[2 + i][k] : acc[i][k], y23 = (wq & 1) ? acc[6 + i][k] : acc[4 + i][k];
            y[k] = (wq & 2) ? y23 : y01;
        }
#pragma unroll
        for (int k = 0; k < 3; ++k) y += red[(wq * 6 + i * 3 + k) * 64 + lane];
        const int l0 = 256 * (2 * wq + i) + 16 * p + 4 * g;
        const u32x2 uu = *(const LAS u32x2*)(lds + CV_U + 480 + 2 * l0), xv = *(const LAS u32x2*)(lds + CV_X + 2 * l0);
        float o[4];
#pragma unroll
        for (int k = 0; k < 4; ++k) {
            const float u = (k & 1) ? bf_hi(uu[k >> 1]) : bf_lo(uu[k >> 1]);
            const float x = (k & 1) ? bf_hi(xv[k >> 1]) : bf_lo(xv[k >> 1]);
            o[k] = x + __builtin_fmaf(u, dsk, y[k]);
        }
        bst8((u32x2){pk_bf16(o[0], o[1]), pk_bf16(o[2], o[3])}, rh, 2 * l0, 0);
    }
    VM0(); BAR();
    if (w == 0) publish_rep(a.ws, WS_FH, b, tag, lane);
}

__device__ __forceinline__ int nb0_of(int j) { return (9 * j + 1) >> 1; }

constexpr int OUT_ROW = 176;
constexpr int KC_XB = 73728, KC_ST = KC_XB + 20 * 1024, KC_OUT = KC_ST + 512;
__device__ __forceinline__ void phase_c(char* __restrict__ ws, LAS char* lds, int panel, int j, unsigned tag) {
    const int tid = threadIdx.x, w = __builtin_amdgcn_readfirstlane(tid >> 6), lane = tid & 63;
    const int tok0 = panel * 64, nbs = nb0_of(j), nblk = nb0_of(j + 1) - nbs;
    const int g = lane >> 4, i16 = lane & 15, q = i16 >> 2, p = lane & 3;
    const int b = w >> 1, kh = w & 1;
    const bool act = w < 10 && b < nblk;
    const int nb = nbs + (act ? b : 0);
    if (w == 0 && *(const LAS unsigned*)(lds + LDS_FWOK) == 0u) wait_flags(ws, WS_FW, 0, 36, WS_FW, 0, 0, tag);
    BAR();
    bf16x8 W[9];
    f32x4 cs0, cs1;
    if (w < 10) {
        const rsrc_t rw = mk_rsrc(ws + WS_WF, WSUB * 2);
#pragma unroll
        for (int i = 0; i < 9; ++i) W[i] = bld16<SC1>(rw, lane * 16, ((kh * 9 + i) * 36 + nb) * 1024);
        const rsrc_t rc = mk_rsrc(ws + WS_CS, 2 * (D + F) * 4);
        cs0 = bldf4<SC1>(rc, g * 16, nb * 64); cs1 = bldf4<SC1>(rc, g * 16, nb * 64 + (D + F) * 4);
    }
    if (w == 11) wait_flags(ws, WS_FH, (blockIdx.x & (REP - 1)) * 256, 192, WS_FH, 0, 0, tag);
    BAR();
    {
        const rsrc_t rh = mk_rsrc(ws + WS_HT, D * L * 2);
        const int fl = (lane >> 4) & 1;
        const int v0 = (lane >> 3) * 4096 + (((lane & 7) ^ (fl << 1)) * 16), v1 = (lane >> 3) * 4096 + (((lane & 7) ^ ((fl | 2) << 1)) * 16);
#pragma unroll
        for (int k = 0; k < 6; ++k) { const int idx = 6 * w + k; blds16<SC1>(rh, lds + idx * 1024, (k & 1) ? v1 : v0, idx * 32768 + tok0 * 2); }
    }
    VM0(); BAR();
    const int f = ((q >> 1) & 1) | ((g & 1) << 1);
    const int lo_ = (8 * g + q) * 128 + 8 * (p & 1);
    f32x4 acc[4] = {};
    LAS f32x2* st = (LAS f32x2*)(lds + KC_ST);
    LAS f32x4* xb = (LAS f32x4*)(lds + KC_XB);
    if (w < 10) {
        const LAS char* hb[4];
#pragma unroll
        for (int mt = 0; mt < 4; ++mt) hb[mt] = lds + kh * 9 * 4096 + lo_ + (((2 * mt + (p >> 1)) ^ (f << 1)) * 16);
        bf16x8 H[2][2][4];
#pragma unroll
        for (int c = 0; c < 6; ++c) {
            if (c < 5) {
#pragma unroll
                for (int i = 0; i < 2; ++i)
                    if (2 * c + i < 9) {
#pragma unroll
                        for (int mt = 0; mt < 4; ++mt) H[c & 1][i][mt] = cat8(tr_read(hb[mt] + (2 * c + i) * 4096), tr_read(hb[mt] + (2 * c + i) * 4096 + 512));
                    }
            }
            __builtin_amdgcn_sched_barrier(0);
            if (c > 0) {
#pragma unroll
                for (int i = 0; i < 2; ++i)
                    if (2 * (c - 1) + i < 9) {
#pragma unroll
                        for (int mt = 0; mt < 4; ++mt) acc[mt] = MFMA16(W[2 * (c - 1) + i], H[(c - 1) & 1][i][mt], acc[mt]);
                    }
            }
            __builtin_amdgcn_sched_barrier(0);
        }
        xb[((w * 2 + 0) * 64) + lane] = sel4(kh, acc[0], acc[2]);
        xb[((w * 2 + 1) * 64) + lane] = sel4(kh, acc[1], acc[3]);
    } else {
        __builtin_amdgcn_s_setprio(3);
        const bf16x8 ones = {0x3f80, 0x3f80, 0x3f80, 0x3f80, 0x3f80, 0x3f80, 0x3f80, 0x3f80};
        const int m0 = 2 * (w - 10);
        const LAS char* h0 = lds + lo_ + (((2 * m0 + (p >> 1)) ^ (f << 1)) * 16);
        const LAS char* h1 = lds + lo_ + (((2 * m0 + 2 + (p >> 1)) ^ (f << 1)) * 16);
        bf16x8 H[2][3][2];
#pragma unroll
        for (int c = 0; c < 7; ++c) {
            if (c < 6) {
#pragma unroll
                for (int i = 0; i < 3; ++i) {
                    H[c & 1][i][0] = cat8(tr_read(h0 + (3 * c + i) * 4096), tr_read(h0 + (3 * c + i) * 4096 + 512));
                    H[c & 1][i][1] = cat8(tr_read(h1 + (3 * c + i) * 4096), tr_read(h1 + (3 * c + i) * 4096 + 512));
                }
            }
            __builtin_amdgcn_sched_barrier(0);
            if (c > 0) {
#pragma unroll
                for (int i = 0; i < 3; ++i) {
                    const bf16x8 H0 = H[(c - 1) & 1][i][0], H1 = H[(c - 1) & 1][i][1];
                    acc[0] = MFMA16(ones, H0, acc[0]); acc[1] = MFMA16(H0, H0, acc[1]);
                    acc[2] = MFMA16(ones, H1, acc[2]); acc[3] = MFMA16(H1, H1, acc[3]);
                }
            }
            __builtin_amdgcn_sched_barrier(0);
        }
        const float sm[2] = {acc[0][0], acc[2][0]};
        if (g == q) {
            const rsrc_t r2 = mk_rsrc(ws + WS_ST2, L * 8);
#pragma unroll
            for (int t = 0; t < 2; ++t) {
                const f32x4 d = acc[2 * t + 1];
                const float s2 = p == 0 ? d[0] : p == 1 ? d[1] : p == 2 ? d[2] : d[3];
                const float mean = sm[t] * (1.0f / D), var = fmaxf(s2 * (1.0f / D) - mean * mean, 0.f);
                const float mb = rbf(mean), rs = __builtin_amdgcn_rcpf(ln_sd(var));
                const float m2 = rbf((mean - mb) * rs), rs2 = __builtin_amdgcn_rcpf(ln_sd(var * rs * rs));
                st[(m0 + t) * 16 + i16] = (f32x2){rs * rs2, (mb * rs + m2) * rs2};
                if (j == 0) bst8((u32x2){__float_as_uint(mb), __float_as_uint(rs)}, r2, (tok0 + (m0 + t) * 16 + i16) * 8, 0);
            }
        }
    }
    __builtin_amdgcn_s_setprio(0);
    LGKM0(); BAR();
    const rsrc_t rg = mk_rsrc(ws + WS_G, (L + 4) * D * 2);
    LAS char* og = lds + KC_OUT;
    if (act) {
        const f32x4 cs = cs0 + cs1;
#pragma unroll
        for (int t = 0; t < 2; ++t) {
            const f32x4 a = sel4(kh, acc[2 + t], acc[t]) + xb[((w ^ 1) * 2 + t) * 64 + lane];
            const f32x2 ab = st[(2 * kh + t) * 16 + i16];
            float o[4];
#pragma unroll
            for (int k = 0; k < 4; ++k) o[k] = gelu_tanh(ab[0] * a[k] - ab[1] * cs[k]);
            *(LAS u32x2*)(og + (kh * 32 + t * 16 + i16) * OUT_ROW + b * 32 + g * 8) = (u32x2){pk_bf16(o[0], o[1]), pk_bf16(o[2], o[3])};
        }
    }
    LGKM0(); BAR();
    {
        const int nc = 2 * nblk, row = nblk == 5 ? tid / 10 : tid >> 3, c = tid - row * nc;
        if (row < 64) bst16(*(const LAS u32x4*)(og + row * OUT_ROW + c * 16), rg, row * (D * 2) + c * 16, ((2 + tok0) * D + nbs * 16) * 2);
        if ((panel == 0 || panel == 31) && tid >= 704 && tid < 704 + 2 * nc) {
            const int z = tid - 704, zr = z >= nc ? 1 : 0;
            bst16((u32x4){0u, 0u, 0u, 0u}, rg, zr * (D * 2) + (z - zr * nc) * 16, ((panel == 0 ? 0 : L + 2) * D + nbs * 16) * 2);
        }
    }
    VM0(); BAR();
    publish(ws, WS_FG, panel * 32 + j, tag);
}

constexpr int KD_HT = 73728, KD_XB = KD_HT + 10240, KD_OUT = KD_XB + 20 * 1024;
__device__ __forceinline__ void phase_d(char* __restrict__ ws, LAS char* lds, int panel, int j, int je, unsigned tag) {
    const int tid = threadIdx.x, w = __builtin_amdgcn_readfirstlane(tid >> 6), lane = tid & 63;
    const int tok0 = panel * 64, nbs = nb0_of(j), nblk = nb0_of(j + 1) - nbs;
    const int g = lane >> 4, i16 = lane & 15, q = i16 >> 2, p = lane & 3;
    const int b = w >> 1, kh = w & 1;
    const bool act = w < 10 && b < nblk;
    const int nb = nbs + (act ? b : 0);
    bf16x8 W[9];
    if (w < 10) {
        const rsrc_t rw = mk_rsrc(ws + WS_WF + WSUB * 2, WSUB * 2);
#pragma unroll
        for (int i = 0; i < 9; ++i) W[i] = bld16<SC1>(rw, lane * 16, ((kh * 9 + i) * 36 + nb) * 1024);
    }
    if (w < 2 * nblk) {
        const rsrc_t rh = mk_rsrc(ws + WS_HT, D * L * 2);
        const int fl = ((lane >> 4) & 1) | ((w & 1) << 1);
        blds16<SC1>(rh, lds + KD_HT + w * 1024, (lane >> 3) * 4096 + (((lane & 7) ^ (fl << 1)) * 16), (nbs * 16 + w * 8) * 4096 + tok0 * 2);
    }
    if (w == 10) wait_flags(ws, WS_FW, (2 + 2 * je) * 18, 18, WS_FW, 0, 0, tag);
    if (w == 11) {
        const int p0 = panel > 0 ? panel - 1 : 0, p1 = panel < 31 ? panel + 1 : 31;
        wait_panels(ws, WS_FG, p0, WS_FG, panel, WS_FG, p1, tag);
    }
    BAR();
    f32x2 st[2];
    {
        const rsrc_t rg = mk_rsrc(ws + WS_G, (L + 4) * D * 2);
        const int c7 = (lane & 7) ^ ((lane >> 3) & 6), vb = (lane >> 3) * 1152 + c7 * 16;
#pragma unroll
        for (int k = 0; k < 6; ++k) {
            const int idx = 6 * w + k, cg = idx >> 3, c = cg * 8 + c7;
            const int o2 = (0x4031 >> (((c * 57) >> 10) * 4)) & 15;
            blds16<SC1>(rg, lds + idx * 1024, vb + o2 * 1152, (tok0 + (idx & 7) * 8) * 1152 + cg * 128);
        }
        const rsrc_t r2 = mk_rsrc(ws + WS_ST2, L * 8);
#pragma unroll
        for (int t = 0; t < 2; ++t) st[t] = __builtin_bit_cast(f32x2, bldu2<SC1>(r2, i16 * 8, (tok0 + kh * 32 + t * 16) * 8));
    }
    VM0();
    asm volatile("" : "+v"(st[0]), "+v"(st[1]));
    BAR();
    f32x4 acc[4] = {};
    LAS f32x4* xb = (LAS f32x4*)(lds + KD_XB);
    if (w < 10) {
        const int z = i16 & 6;
        const LAS char* a0 = lds + i16 * 128 + ((g ^ z) * 16), * a1 = lds + i16 * 128 + (((4 + g) ^ z) * 16);
        const LAS char* e0 = kh ? a1 + 4 * 8192 : a0;
        const LAS char* e1 = kh ? a0 + 5 * 8192 : a1;
        bf16x8 A[2][2][4];
#pragma unroll
        for (int c = 0; c < 6; ++c) {
            if (c < 5) {
#pragma unroll
                for (int ii = 0; ii < 2; ++ii) {
                    const int i = 2 * c + ii;
                    if (i < 9) {
#pragma unroll
                        for (int mt = 0; mt < 4; ++mt) A[c & 1][ii][mt] = *(const LAS bf16x8*)(((i & 1) ? e1 : e0) + (i >> 1) * 8192 + mt * 2048);
                    }
                }
            }
            __builtin_amdgcn_sched_barrier(0);
            if (c > 0) {
#pragma unroll
                for (int ii = 0; ii < 2; ++ii)
                    if (2 * (c - 1) + ii < 9) {
#pragma unroll
                        for (int mt = 0; mt < 4; ++mt) acc[mt] = MFMA16(W[2 * (c - 1) + ii], A[(c - 1) & 1][ii][mt], acc[mt]);
                    }
            }
            __builtin_amdgcn_sched_barrier(0);
        }
        xb[((w * 2 + 0) * 64) + lane] = sel4(kh, acc[0], acc[2]);
        xb[((w * 2 + 1) * 64) + lane] = sel4(kh, acc[1], acc[3]);
    }
    LGKM0(); BAR();
    LAS char* ox = lds + KD_OUT;
    if (w < 10) {
        const int bl = act ? b : 0;
        const int hrow = 16 * bl + 4 * g + q;
        const LAS char* hb = lds + KD_HT + hrow * 128 + 8 * (p & 1);
        const rsrc_t rp = mk_rsrc(ws + WS_PST, 32 * 36 * 64 * 8);
#pragma unroll
        for (int t = 0; t < 2; ++t) {
            const int mt = 2 * kh + t;
            const f32x4 a = sel4(kh, acc[2 + t], acc[t]) + xb[((w ^ 1) * 2 + t) * 64 + lane];
            const u32x2 hu = __builtin_bit_cast(u32x2, tr_read(hb + (((2 * mt + (p >> 1)) ^ (fsw(hrow) << 1)) * 16)));
            const float rs = st[t][1], nm = -st[t][0] * rs;
            u32x2 ov;
#pragma unroll
            for (int k = 0; k < 2; ++k)
                ov[k] = pk_bf16(__builtin_fmaf(bf_lo(hu[k]), rs, nm) + a[2 * k], __builtin_fmaf(bf_hi(hu[k]), rs, nm) + a[2 * k + 1]);
            float s1 = dot2bf(ov[1], 0x3f803f80u, dot2bf(ov[0], 0x3f803f80u, 0.f)), s2 = dot2bf(ov[1], ov[1], dot2bf(ov[0], ov[0], 0.f));
            s1 = rsum_g(s1); s2 = rsum_g(s2);
            if (act) {
                *(LAS u32x2*)(ox + (mt * 16 + i16) * OUT_ROW + b * 32 + g * 8) = ov;
                *(LAS u32x2*)(ox + 64 * OUT_ROW + (mt * 16 + i16) * OUT_ROW + b * 32 + g * 8) = hu;
                if (lane < 16) bst8((u32x2){__float_as_uint(s1), __float_as_uint(s2)}, rp, lane * 8, ((panel * 36 + nb) * 64 + mt * 16) * 8);
            }
        }
    }
    LGKM0(); BAR();
    {
        const int nc = 2 * nblk, row = nblk == 5 ? tid / 10 : tid >> 3, c = tid - row * nc;
        if (row < 64) {
            const int so = (tok0 * D + nbs * 16) * 2, vo = row * (D * 2) + c * 16;
            bst16(*(const LAS u32x4*)(ox + row * OUT_ROW + c * 16), mk_rsrc(ws + WS_X1, L * D * 2), vo, so);
            bst16(*(const LAS u32x4*)(ox + 64 * OUT_ROW + row * OUT_ROW + c * 16), mk_rsrc(ws + WS_HR, L * D * 2), vo, so);
        }
    }
    VM0(); BAR();
    publish(ws, WS_FX, panel * 32 + j, tag);
}

constexpr int BG_LDS = 131072;
__device__ __forceinline__ void phase_cd_background(const Args& a, LAS char* lds, int panel, int job, unsigned tag) {
    char* ws = a.ws;
    const int tid = threadIdx.x, w = __builtin_amdgcn_readfirstlane(tid >> 6), lane = tid & 63, tok0 = panel * 64;
    BAR(); BAR();
    {
        const rsrc_t rh = mk_rsrc(ws + WS_HT, D * L * 2);
        const int fl = (lane >> 4) & 1;
        const int v0 = (lane >> 3) * 4096 + (((lane & 7) ^ (fl << 1)) * 16), v1 = (lane >> 3) * 4096 + (((lane & 7) ^ ((fl | 2) << 1)) * 16);
#pragma unroll
        for (int k = 0; k < 6; ++k) { const int idx = 6 * w + k; blds16<SC1>(rh, lds + idx * 1024, (k & 1) ? v1 : v0, idx * 32768 + tok0 * 2); }
    }
    VM0(); BAR();
    PrepRegs P;
    prep_issue(a, job, lane, P);
    BAR(); BAR(); BAR();
    prep_finish<false, false>(a, job, lds + BG_LDS + (w - 8) * 8192, lane, tag, P);
    BAR();
    {
        const rsrc_t rg = mk_rsrc(ws + WS_G, (L + 4) * D * 2);
        const int c7 = (lane & 7) ^ ((lane >> 3) & 6), vb = (lane >> 3) * 1152 + c7 * 16;
#pragma unroll
        for (int k = 0; k < 6; ++k) {
            const int idx = 6 * w + k, cg = idx >> 3, c = cg * 8 + c7;
            const int o2 = (0x4031 >> (((c * 57) >> 10) * 4)) & 15;
            blds16<SC1>(rg, lds + idx * 1024, vb + o2 * 1152, (tok0 + (idx & 7) * 8) * 1152 + cg * 128);
        }
    }
    VM0();
    if (lane == 0) __hip_atomic_store((gu32*)(ws + WS_FW) + job, tag, RLX_AGENT);
    BAR(); BAR(); BAR(); BAR();
}

constexpr int KE_HID = 73728, KE_ST = 2 * 73728, KE_FLG = KE_ST + 1024;
constexpr int NB = 3, RING = 4;
__device__ __forceinline__ void phase_e(char* __restrict__ ws, LAS char* lds, int panel, int j, unsigned tag) {
    const int w = __builtin_amdgcn_readfirstlane(threadIdx.x >> 6), lane = threadIdx.x & 63;
    constexpr int S1 = 6, S2 = 12;
    const int tok0 = panel * 64, g = lane >> 4, i16 = lane & 15, nbs = NB * w;
    const rsrc_t rw = mk_rsrc(ws + WS_WF + (size_t)(2 + 2 * j) * WSUB * 2, 2 * WSUB * 2);
    const int wv = lane * 16, wsb = nbs * 1024;
    bf16x8 W[RING][NB];
#pragma unroll
    for (int s = 0; s < RING; ++s)
#pragma unroll
        for (int b = 0; b < NB; ++b) W[s][b] = bld16<SC1>(rw, wv + b * 1024, wsb + s * 36864);
    f32x4 acc[NB][4];
    {
        const rsrc_t rc = mk_rsrc(ws + WS_CS, 2 * (D + F) * 4);
#pragma unroll
        for (int b = 0; b < NB; ++b) {
            acc[b][0] = bldf4<SC1>(rc, g * 16 + b * 64, (D + j * D + nbs * 16) * 4);
            acc[b][1] = bldf4<SC1>(rc, g * 16 + b * 64, (D + j * D + nbs * 16 + D + F) * 4);
        }
    }
    __builtin_amdgcn_sched_barrier(0);
    unsigned fw4 = tag;
    if (w == 10 && lane < 18) fw4 = __hip_atomic_load((gu32*)(ws + WS_FW) + (3 + 2 * j) * 18 + lane, RLX_AGENT);
    if (w == 11) wait_panels(ws, WS_FX, panel, WS_FX, panel, WS_FX, panel, tag);
    BAR();
    u32x2 pv[36];
    if (w == 11) {
        const rsrc_t rp = mk_rsrc(ws + WS_PST + (size_t)panel * 36 * 512, 36 * 512);
#pragma unroll
        for (int k = 0; k < 36; ++k) pv[k] = bldu2<SC1>(rp, lane * 8, k * 512);
    }
    {
        const rsrc_t rx = mk_rsrc(ws + WS_X1 + (size_t)tok0 * D * 2, 64 * D * 2);
        const int vo = (lane >> 3) * 1152 + (((lane & 7) ^ ((lane >> 3) & 6)) * 16);
#pragma unroll
        for (int k = 0; k < 6; ++k) {
            const int idx = 6 * w + k;
            blds16<SC1>(rx, lds + idx * 1024, vo, (idx & 7) * 9216 + (idx >> 3) * 128);
        }
    }
    __builtin_amdgcn_sched_barrier(0);
    LAS f32x4* st = (LAS f32x4*)(lds + KE_ST);
    LAS unsigned* flg = (LAS unsigned*)(lds + KE_FLG);
    if (w == 11) {
        float a1[4] = {}, a2[4] = {};
#pragma unroll
        for (int k = 0; k < 36; ++k) { a1[k & 3] += __uint_as_float(pv[k][0]); a2[k & 3] += __uint_as_float(pv[k][1]); }
        const float s1 = (a1[0] + a1[1]) + (a1[2] + a1[3]), s2 = (a2[0] + a2[1]) + (a2[2] + a2[3]);
        const float mean = s1 * (1.0f / D), var = fmaxf(s2 * (1.0f / D) - mean * mean, 0.f);
        const float mb = rbf(mean), sb = ln_sd(var), al = __builtin_amdgcn_rcpf(sb);
        const float k0 = -2.0f * 0.7978845608028654f * 1.4426950408889634f;
        st[lane] = (f32x4){k0 * al, k0 * 0.044715f * al * al * al, sb, -mb};
        if (lane < 3) flg[lane] = 0u;
    }
    if (w == 10) { const bool ok4 = __all(fw4 == tag); if (lane == 0) flg[3] = ok4 ? 1u : 0u; }
    VM0(); LGKM0(); BAR();
    const int z = i16 & 6;
    const int o0 = i16 * 128 + (g ^ z) * 16, o1 = i16 * 128 + ((4 + g) ^ z) * 16;
    {
        float nm[4];
#pragma unroll
        for (int mt = 0; mt < 4; ++mt) nm[mt] = st[mt * 16 + i16][3];
#pragma unroll
        for (int b = 0; b < NB; ++b) {
            const f32x4 cs = acc[b][0] + acc[b][1];
#pragma unroll
            for (int mt = 0; mt < 4; ++mt) acc[b][mt] = cs * nm[mt];
        }
    }
#define E_SPIN(cond) do { while (cond) __builtin_amdgcn_s_sleep(1); __builtin_amdgcn_sched_barrier(0); asm volatile("" ::: "memory"); } while (0)
#define E_FLG(i) __hip_atomic_load(flg + (i), __ATOMIC_RELAXED, __HIP_MEMORY_SCOPE_WORKGROUP)
    bf16x8 A[2][4];
#pragma unroll
    for (int ph = 0; ph < 2; ++ph) {
        const LAS char* ab = lds + ph * KE_HID;
        if (ph == 1) E_SPIN(E_FLG(0) < 4u);
#pragma unroll
        for (int mt = 0; mt < 4; ++mt) A[0][mt] = *(const LAS bf16x8*)(ab + o0 + mt * 2048);
#pragma unroll
        for (int s = 0; s < 18; ++s) {
            if (ph == 1 && s + 1 == S1) E_SPIN(E_FLG(1) < 4u);
            if (ph == 1 && s + 1 == S2) E_SPIN(E_FLG(2) < 4u);
            if (ph == 0 && s == 2 && w == 8 && E_FLG(3) == 0u) {
                wait_flags(ws, WS_FW, (3 + 2 * j) * 18, 18, WS_FW, 0, 0, tag);
                if (lane == 0) __hip_atomic_store(flg + 3, 1u, __ATOMIC_RELAXED, __HIP_MEMORY_SCOPE_WORKGROUP);
            }
            if (ph == 0 && s == 18 - RING) E_SPIN(E_FLG(3) == 0u);
            if (s + 1 < 18) {
#pragma unroll
                for (int mt = 0; mt < 4; ++mt) A[(s + 1) & 1][mt] = *(const LAS bf16x8*)(ab + (((s + 1) & 1) ? o1 : o0) + mt * 2048 + ((s + 1) >> 1) * 8192);
            }
            const int t = ph * 18 + s;
#pragma unroll
            for (int b = 0; b < NB; ++b) {
#pragma unroll
                for (int mt = 0; mt < 4; ++mt) acc[b][mt] = MFMA16(W[t % RING][b], A[s & 1][mt], acc[b][mt]);
                if (t + RING < 36) W[t % RING][b] = bld16<SC1>(rw, wv + b * 1024, wsb + (t + RING) * 36864);
            }
            __builtin_amdgcn_sched_barrier(0);
        }
        if (ph == 0) {
#pragma unroll
            for (int mt = 0; mt < 4; ++mt) {
                const f32x4 k = st[mt * 16 + i16];
#pragma unroll
                for (int b = 0; b < NB; ++b) {
                    float o[4];
#pragma unroll
                    for (int e = 0; e < 4; ++e) {
                        const float a = acc[b][mt][e];
                        const float ex = __builtin_amdgcn_exp2f(a * (k[0] + k[1] * (a * a)));
                        o[e] = a * __builtin_amdgcn_rcpf(k[2] + k[2] * ex);
                    }
                    const int nb = nbs + b;
                    *(LAS u32x2*)(lds + KE_HID + (nb >> 2) * 8192 + (mt * 16 + i16) * 128 + (((2 * (nb & 3) + (g >> 1)) ^ z) * 16) + 8 * (g & 1)) =
                        (u32x2){pk_bf16(o[0], o[1]), pk_bf16(o[2], o[3])};
                    acc[b][mt] = (f32x4){0.f, 0.f, 0.f, 0.f};
                }
            }
            LGKM0();
            if (lane == 0) __hip_atomic_fetch_add(flg + (w >> 2), 1u, __ATOMIC_RELAXED, __HIP_MEMORY_SCOPE_WORKGROUP);
            if (w >= 8) __builtin_amdgcn_s_setprio(2); else if (w >= 4) __builtin_amdgcn_s_setprio(1);
        }
    }
#undef E_SPIN
#undef E_FLG
    __builtin_amdgcn_s_setprio(0);
    const rsrc_t rp = mk_rsrc(ws + WS_PART + (size_t)(panel * 8 + j) * 72 * 1024, 72 * 1024);
#pragma unroll
    for (int b = 0; b < NB; ++b)
#pragma unroll
        for (int m2 = 0; m2 < 2; ++m2) {
            const f32x4 a0 = acc[b][2 * m2], a1 = acc[b][2 * m2 + 1];
            const u32x4 v = {pk_bf16(a0[0], a0[1]), pk_bf16(a0[2], a0[3]), pk_bf16(a1[0], a1[1]), pk_bf16(a1[2], a1[3])};
            const int pr = (nbs + b) * 2 + m2;
            if (pr >= 9 * j && pr < 9 * j + 9) *(LAS u32x4*)(lds + (pr - 9 * j) * 1024 + lane * 16) = v;
            else bst16(v, rp, lane * 16, pr * 1024);
        }
    LGKM0();
    VM0(); BAR();
    publish(ws, WS_FP, panel * 32 + j, tag);
}

__device__ __forceinline__ void phase_f(char* __restrict__ ws, LAS char* lds, u16* __restrict__ out, int panel, int jj, unsigned tag) {
    const int tid = threadIdx.x, w = __builtin_amdgcn_readfirstlane(tid >> 6), lane = tid & 63;
    const int g = lane >> 4, i16 = lane & 15;
    const int pr = jj * 9 + (w < 9 ? w : 0), nb = pr >> 1, m2 = pr & 1;
    u32x2 xa[2], ha[2];
    const int so = ((panel * 64 + m2 * 32) * D + nb * 16) * 2, vo = (i16 * D + 4 * g) * 2;
    if (w < 9) {
        const rsrc_t r1 = mk_rsrc(ws + WS_X1, L * D * 2), rr = mk_rsrc(ws + WS_HR, L * D * 2);
#pragma unroll
        for (int t = 0; t < 2; ++t) { xa[t] = bldu2<SC1>(r1, vo, so + t * 16 * D * 2); ha[t] = bldu2<SC1>(rr, vo, so + t * 16 * D * 2); }
    }
    if (w == 11) wait_panels(ws, WS_FP, panel, WS_FP, panel, WS_FP, panel, tag);
    BAR();
    if (w < 9) {
        const rsrc_t rp = mk_rsrc(ws + WS_PART + (size_t)(panel * 8) * 72 * 1024, 8 * 72 * 1024);
        u32x4 v[8];
#pragma unroll
        for (int k = 0; k < 7; ++k) v[k] = bldu4<SC1>(rp, lane * 16, ((k < jj ? k : k + 1) * 72 + pr) * 1024);
        v[7] = *(const LAS u32x4*)(lds + w * 1024 + lane * 16);
        float s[8] = {};
#pragma unroll
        for (int k = 0; k < 8; ++k)
#pragma unroll
            for (int e = 0; e < 4; ++e) { s[2 * e] += bf_lo(v[k][e]); s[2 * e + 1] += bf_hi(v[k][e]); }
#pragma unroll
        for (int t = 0; t < 2; ++t) {
            float o[4];
#pragma unroll
            for (int k = 0; k < 4; ++k) {
                const float x1 = (k & 1) ? bf_hi(xa[t][k >> 1]) : bf_lo(xa[t][k >> 1]);
                const float h = (k & 1) ? bf_hi(ha[t][k >> 1]) : bf_lo(ha[t][k >> 1]);
                o[k] = h + (x1 + s[4 * t + k]);
            }
            __builtin_amdgcn_raw_buffer_store_b64((u32x2){pk_bf16(o[0], o[1]), pk_bf16(o[2], o[3])}, mk_rsrc(out, L * D * 2), vo, so + t * 16 * D * 2, 0);
        }
    }
}

constexpr int LDS_CTRL = 3 * CV_CH > KE_FLG + 32 ? 3 * CV_CH : KE_FLG + 32;
constexpr int LDS_BYTES = LDS_CTRL + 64;

NOPK __global__ __launch_bounds__(NTHR) void mega_kernel(Args a) {
    extern __shared__ __attribute__((aligned(16))) char smem[];
    LAS char* lds = (LAS char*)smem;
    const int tid = threadIdx.x, w = __builtin_amdgcn_readfirstlane(tid >> 6), lane = tid & 63, b = blockIdx.x;
    unsigned cpf = 0;
    if (w == 8) {
        unsigned long long pc;
        asm volatile("s_getpc_b64 %0" : "=s"(pc));
        const int line = (b >> 3) * 13 + lane;
        if (lane < 13 && line * 128 < CODE_PF) cpf = *(const GAS unsigned*)((pc & ~255ull) + (unsigned long long)line * 128);
    }
    const unsigned tagv = __hip_atomic_load((gu32*)(a.ws + WS_EPOCH), RLX_AGENT);
    const StatRow sr = row_stats_load(a, w, lane);
    __builtin_amdgcn_sched_barrier(0);
    unsigned tag;
    if (b < 192) phase_conv(a, lds, tagv, sr, tag);
    else {
        tag = __builtin_amdgcn_readfirstlane(tagv) + 1u;
        if (tid == 0) __hip_atomic_store((gu32*)(a.ws + WS_FA) + b, tag, RLX_AGENT);
        if (tid == 0) *(LAS unsigned*)(lds + LDS_FWOK) = 0u;
        row_stats(a, sr, tag, w, lane);
        LGKM0(); BAR();
        const int i = w * 64 + (b - 192);
        if (i >= 36) {
            const unsigned long long t0 = __builtin_amdgcn_s_memrealtime();
            while (__builtin_amdgcn_s_memrealtime() - t0 < PREP_DELAY) __builtin_amdgcn_s_sleep(8);
        }
        if (i < 180) {
            const int job = i < 36 ? i : (2 + 2 * ((i - 36) / 18)) * 18 + (i - 36) % 18;
            PrepRegs P;
            prep_issue(a, job, lane, P);
            prep_finish<true, true>(a, job, lds + w * 8192, lane, tag, P);
        }
        BAR();
    }
    asm volatile("" :: "v"(cpf));
    const int je = 2 * (b & 3) + ((b >> 3) & 1);
    {
        const int panel = (b & 7) * 4 + (b >> 6), j = (b >> 3) & 7;
        const int m = (w - 8) * 128 + ((b & 7) | ((b >> 4) << 3));
        if ((j & 1) && (w == 8 || w == 9) && m < 144) phase_cd_background(a, lds, panel, (3 + 2 * (m / 18)) * 18 + m % 18, tag);
        else {
            phase_c(a.ws, lds, panel, j, tag);
            phase_d(a.ws, lds, panel, j, je, tag);
        }
    }
    phase_e(a.ws, lds, ((b >> 2) & 1) * 16 + (b >> 4), je, tag);
    phase_f(a.ws, lds, a.out, ((b >> 2) & 1) * 16 + (b >> 4), je, tag);
    if (b == 0 && w == 10) {
        wait_flags(a.ws, WS_FA, 0, 256, WS_FA, 0, 0, tag);
        if (lane == 0) __hip_atomic_store((gu32*)(a.ws + WS_EPOCH), tag, RLX_AGENT);
    }
}

extern "C" void kernel_launch(void* const* d_in, const int* in_sizes, int n_in,
                              void* d_out, int out_size, void* d_ws, size_t ws_size,
                              hipStream_t stream) {
    Args a;
    a.x = (const u16*)d_in[0];
    a.are = (const u16*)d_in[1]; a.aim = (const u16*)d_in[2];
    a.bre = (const u16*)d_in[3]; a.bim = (const u16*)d_in[4];
    a.cre = (const u16*)d_in[5]; a.cim = (const u16*)d_in[6];
    a.logdt = (const u16*)d_in[7]; a.dskip = (const u16*)d_in[8];
    a.w1 = (const u16*)d_in[10]; a.w2 = (const u16*)d_in[11];
    a.w3 = (const u16*)d_in[12]; a.w4 = (const u16*)d_in[13];
    a.ws = (char*)d_ws; a.out = (u16*)d_out;
    (void)hipFuncSetAttribute((const void*)mega_kernel, hipFuncAttributeMaxDynamicSharedMemorySize, LDS_BYTES);
    mega_kernel<<<NWG, NTHR, LDS_BYTES, stream>>>(a);
}
```

```cpp
#include <hip/hip_runtime.h>
#include <hip/hip_bf16.h>
#include <stdint.h>

typedef unsigned short u16;
typedef short bf16x8 __attribute__((ext_vector_type(8)));
typedef short s16x4 __attribute__((ext_vector_type(4)));
typedef float f32x2 __attribute__((ext_vector_type(2)));
typedef float f32x4 __attribute__((ext_vector_type(4)));
typedef float f32x16 __attribute__((ext_vector_type(16)));
typedef unsigned u32x2 __attribute__((ext_vector_type(2)));
typedef unsigned u32x4 __attribute__((ext_vector_type(4)));
typedef __bf16 bf16x2_t __attribute__((ext_vector_type(2)));
typedef _Float16 f16x2 __attribute__((ext_vector_type(2)));
typedef _Float16 f16x8 __attribute__((ext_vector_type(8)));
#define LAS __attribute__((address_space(3)))
#define GAS __attribute__((address_space(1)))
#if defined(__HIP_DEVICE_COMPILE__)
#define NOPK __attribute__((target("no-packed-fp32-ops")))
#else
#define NOPK
#endif

constexpr int L = 2048, D = 576, NS = 128, F = 4608;
constexpr int WSUB = D * D;
constexpr float LN_EPS = 1e-5f;
constexpr int NWG = 256, NTHR = 768;
constexpr int SC1 = 16;

constexpr size_t WS_EPOCH = 0;
constexpr int REP = 8;
constexpr size_t WS_FA = 256;
constexpr size_t WS_FW = WS_FA + 1024;
constexpr size_t WS_FG = WS_FW + 2048;
constexpr size_t WS_FX = WS_FG + 4096;
constexpr size_t WS_FP = WS_FX + 4096;
constexpr size_t WS_FH = WS_FP + 4096;
constexpr size_t WS_GR = 32768;
constexpr size_t WS_HT = WS_GR + (size_t)L * 8;
constexpr size_t WS_WF = WS_HT + (size_t)D * L * 2;
constexpr size_t WS_CS = WS_WF + (size_t)18 * WSUB * 2;
constexpr size_t WS_ST2 = WS_CS + (size_t)2 * (D + F) * 4;
constexpr size_t WS_G = WS_ST2 + (size_t)L * 8;
constexpr size_t WS_X1 = WS_G + (size_t)(L + 4) * D * 2;
constexpr size_t WS_PST = WS_X1 + (size_t)L * D * 2;
constexpr size_t WS_PART = WS_PST + (size_t)32 * 36 * 64 * 8;
constexpr size_t WS_END = WS_PART + (size_t)32 * 8 * 72 * 512 * 2;

__device__ __forceinline__ unsigned pk_bf16(float lo, float hi) {
    f32x2 v = {lo, hi};
    return __builtin_bit_cast(unsigned, __builtin_convertvector(v, bf16x2_t));
}
__device__ __forceinline__ float bf_lo(unsigned p) { return __uint_as_float(p << 16); }
__device__ __forceinline__ float bf_hi(unsigned p) { return __uint_as_float(p & 0xffff0000u); }
__device__ __forceinline__ float bf2f(u16 x) { return __uint_as_float((unsigned)x << 16); }
__device__ __forceinline__ float rbf(float x) { return bf_lo(pk_bf16(x, x)); }
__device__ __forceinline__ float dot2bf(unsigned a, unsigned b, float c) {
    return __builtin_amdgcn_fdot2_f32_bf16(__builtin_bit_cast(bf16x2_t, a), __builtin_bit_cast(bf16x2_t, b), c, false);
}
__device__ __forceinline__ float gelu_tanh(float x) {
    const float c0 = -2.0f * 0.7978845608028654f * 1.4426950408889634f;
    const float c1 = c0 * 0.044715f;
    float e = __builtin_amdgcn_exp2f(x * (c0 + c1 * x * x));
    return x * __builtin_amdgcn_rcpf(1.0f + e);
}
__device__ __forceinline__ s16x4 tr_read(const LAS char* p) {
    return __builtin_amdgcn_ds_read_tr16_b64_v4i16((LAS s16x4*)p);
}
__device__ __forceinline__ bf16x8 cat8(s16x4 a, s16x4 b) {
    return __builtin_shufflevector(a, b, 0, 1, 2, 3, 4, 5, 6, 7);
}
__device__ __forceinline__ f32x4 sel4(bool c, f32x4 a, f32x4 b) {
    f32x4 r;
#pragma unroll
    for (int k = 0; k < 4; ++k) { float x = a[k], y = b[k]; asm volatile("" : "+v"(x), "+v"(y)); r[k] = c ? x : y; }
    return r;
}
__device__ __forceinline__ float rsum_g(float x) {
    const unsigned u = __float_as_uint(x);
    const auto a = __builtin_amdgcn_permlane16_swap(u, u, false, false);
    const float y = __uint_as_float(a[0]) + __uint_as_float(a[1]);
    const unsigned v = __float_as_uint(y);
    const auto b = __builtin_amdgcn_permlane32_swap(v, v, false, false);
    return __uint_as_float(b[0]) + __uint_as_float(b[1]);
}
template <int CTRL> __device__ __forceinline__ float dpp_f(float x) {
    return __uint_as_float(__builtin_amdgcn_update_dpp(0u, __float_as_uint(x), CTRL, 0xf, 0xf, false));
}
__device__ __forceinline__ float rsum_64(float x) {
    x += dpp_f<0xB1>(x);
    x += dpp_f<0x4E>(x);
    x += dpp_f<0x141>(x);
    x += dpp_f<0x140>(x);
    return rsum_g(x);
}
template <int CTRL> __device__ __forceinline__ unsigned dpp_u(unsigned x) { return __builtin_amdgcn_update_dpp(0u, x, CTRL, 0xf, 0xf, false); }
__device__ __forceinline__ unsigned umax2(unsigned a, unsigned b) { return a > b ? a : b; }
__device__ __forceinline__ float rmax_64(float xf) {
    unsigned x = __float_as_uint(xf);
    x = umax2(x, dpp_u<0xB1>(x)); x = umax2(x, dpp_u<0x4E>(x)); x = umax2(x, dpp_u<0x141>(x)); x = umax2(x, dpp_u<0x140>(x));
    const auto a = __builtin_amdgcn_permlane16_swap(x, x, false, false); x = umax2(a[0], a[1]);
    const auto b = __builtin_amdgcn_permlane32_swap(x, x, false, false); x = umax2(b[0], b[1]);
    return __uint_as_float(x);
}
__device__ __forceinline__ float ln_sd(float var) { return rbf(__builtin_amdgcn_sqrtf(rbf(rbf(var) + LN_EPS))); }
__device__ __forceinline__ int fsw(int row) { return ((row >> 1) & 1) | (((row >> 3) & 1) << 1); }
#define MFMA16(a, b, c) __builtin_amdgcn_mfma_f32_16x16x32_bf16((a), (b), (c), 0, 0, 0)
#define MFMA32(a, b, c) __builtin_amdgcn_mfma_f32_32x32x16_bf16((a), (b), (c), 0, 0, 0)
#define VM0() asm volatile("s_waitcnt vmcnt(0)" ::: "memory")
#define LGKM0() asm volatile("s_waitcnt lgkmcnt(0)" ::: "memory")
#define BAR() do { asm volatile("" ::: "memory"); __builtin_amdgcn_s_barrier(); asm volatile("" ::: "memory"); } while (0)

typedef __amdgpu_buffer_rsrc_t rsrc_t;
__device__ __forceinline__ rsrc_t mk_rsrc(const void* p, unsigned bytes) { return __builtin_amdgcn_make_buffer_rsrc((void*)p, 0, bytes, 0x00020000); }
template <int AUX> __device__ __forceinline__ bf16x8 bld16(rsrc_t r, int voff, int soff) {
    return __builtin_bit_cast(bf16x8, __builtin_amdgcn_raw_buffer_load_b128(r, voff, soff, AUX));
}
template <int AUX> __device__ __forceinline__ f32x4 bldf4(rsrc_t r, int voff, int soff) {
    return __builtin_bit_cast(f32x4, __builtin_amdgcn_raw_buffer_load_b128(r, voff, soff, AUX));
}
template <int AUX> __device__ __forceinline__ u32x4 bldu4(rsrc_t r, int voff, int soff) {
    return __builtin_amdgcn_raw_buffer_load_b128(r, voff, soff, AUX);
}
template <int AUX> __device__ __forceinline__ u32x2 bldu2(rsrc_t r, int voff, int soff) {
    return __builtin_amdgcn_raw_buffer_load_b64(r, voff, soff, AUX);
}
template <int AUX> __device__ __forceinline__ void blds16(rsrc_t r, LAS char* l, int voff, int soff) {
    __builtin_amdgcn_raw_ptr_buffer_load_lds(r, (LAS void*)l, 16, voff, soff, 0, AUX);
}
__device__ __forceinline__ void bst16(u32x4 v, rsrc_t r, int voff, int soff) { __builtin_amdgcn_raw_buffer_store_b128(v, r, voff, soff, SC1); }
__device__ __forceinline__ void bst8(u32x2 v, rsrc_t r, int voff, int soff) { __builtin_amdgcn_raw_buffer_store_b64(v, r, voff, soff, SC1); }
__device__ __forceinline__ void bst4(unsigned v, rsrc_t r, int voff, int soff) { __builtin_amdgcn_raw_buffer_store_b32(v, r, voff, soff, SC1); }

struct Args {
    const u16* x; const u16 *are, *aim, *bre, *bim, *cre, *cim, *logdt, *dskip;
    const u16 *w1, *w2, *w3, *w4;
    char* ws; u16* out;
};

typedef GAS unsigned gu32;
typedef GAS unsigned long long gu64;
#define RLX_AGENT __ATOMIC_RELAXED, __HIP_MEMORY_SCOPE_AGENT
constexpr unsigned long long PREP_DELAY = 100ull;
constexpr int CODE_PF = 41984;
constexpr unsigned long long WAIT_TICKS = 400000ull;
__device__ __forceinline__ void publish(char* ws, size_t flag_off, int idx, unsigned tag) {
    if (threadIdx.x == 0) __hip_atomic_store((gu32*)(ws + flag_off) + idx, tag, RLX_AGENT);
}
__device__ __forceinline__ void publish_rep(char* ws, size_t flag_off, int idx, unsigned tag, int lane) {
    if (lane < REP) __hip_atomic_store((gu32*)(ws + flag_off) + lane * 256 + idx, tag, RLX_AGENT);
}
__device__ __forceinline__ void wait_flags(char* ws, size_t offa, int a0, int na, size_t offb, int b0, int nb, unsigned tag) {
    const int lane = threadIdx.x & 63;
    const gu32* p[4]; bool on[4];
#pragma unroll
    for (int k = 0; k < 4; ++k) {
        const int i = lane + 64 * k;
        on[k] = i < na + nb;
        p[k] = i < na ? (const gu32*)(ws + offa) + a0 + i : (const gu32*)(ws + offb) + b0 + (on[k] ? i - na : 0);
    }
    const unsigned long long t0 = __builtin_amdgcn_s_memrealtime();
    for (;;) {
        bool ok = true;
#pragma unroll
        for (int k = 0; k < 4; ++k)
            if (64 * k < na + nb) { const unsigned v = __hip_atomic_load(p[k], RLX_AGENT); ok = ok && (!on[k] || v == tag); }
        if (__all(ok)) break;
        if (__builtin_amdgcn_s_memrealtime() - t0 > WAIT_TICKS) break;
        __builtin_amdgcn_s_sleep(2);
    }
}
__device__ __forceinline__ void wait_panels(char* ws, size_t o0, int p0, size_t o1, int p1, size_t o2, int p2, unsigned tag) {
    const int lane = threadIdx.x & 63, k = (lane >> 3) % 3;
    const gu32* p = (const gu32*)(ws + (k == 0 ? o0 : k == 1 ? o1 : o2)) + (k == 0 ? p0 : k == 1 ? p1 : p2) * 32 + (lane & 7);
    const unsigned long long t0 = __builtin_amdgcn_s_memrealtime();
    for (;;) {
        if (__all(__hip_atomic_load(p, RLX_AGENT) == tag)) break;
        if (__builtin_amdgcn_s_memrealtime() - t0 > WAIT_TICKS) break;
        __builtin_amdgcn_s_sleep(2);
    }
}
struct StatRow { u32x4 v0, v1; };
__device__ __forceinline__ StatRow row_stats_load(const Args& a, int w, int lane) {
    const int row = blockIdx.x * 8 + (w & 7);
    const rsrc_t rx = mk_rsrc(a.x + (size_t)row * D, w < 8 ? D * 2 : 0);
    StatRow r;
    r.v0 = bldu4<0>(rx, lane * 16, 0); r.v1 = bldu4<0>(rx, 1024 + lane * 16, 0);
    return r;
}
__device__ __forceinline__ void row_stats(const Args& a, const StatRow& r, unsigned tag, int w, int lane) {
    if (w < 8) {
        float s1 = 0.f, s2 = 0.f;
#pragma unroll
        for (int k = 0; k < 4; ++k) {
            s1 = dot2bf(r.v0[k], 0x3f803f80u, s1); s2 = dot2bf(r.v0[k], r.v0[k], s2);
            s1 = dot2bf(r.v1[k], 0x3f803f80u, s1); s2 = dot2bf(r.v1[k], r.v1[k], s2);
        }
        s1 = rsum_64(s1); s2 = rsum_64(s2);
        const float mean = s1 * (1.0f / D), var = fmaxf(s2 * (1.0f / D) - mean * mean, 0.f);
        const unsigned val = (pk_bf16(mean, ln_sd(var)));
        if (lane == 0) __hip_atomic_store((gu64*)(a.ws + WS_GR) + blockIdx.x * 8 + w, ((unsigned long long)tag << 32) | val, RLX_AGENT);
    }
}

struct PrepRegs { u32x4 R[36]; };
__device__ __forceinline__ void prep_issue(const Args& a, int job, int lane, PrepRegs& P) {
    const int sub = job / 18, rem = job % 18, kh = rem / 9, ns = rem % 9;
    const u16* src; int ld;
    if (sub == 0) { src = a.w1; ld = D; }
    else if (sub == 1) { src = a.w2; ld = D; }
    else if ((sub & 1) == 0) { src = a.w3 + ((sub - 2) >> 1) * D; ld = F; }
    else { src = a.w4 + (size_t)((sub - 2) >> 1) * D * D; ld = D; }
    src += (size_t)(kh * 288) * ld + ns * 64;
    const rsrc_t rs = mk_rsrc(src, 288 * ld * 2);
    const int ldb = ld * 2;
    const int vo = (lane >> 3) * ldb + (lane & 7) * 16;
#pragma unroll
    for (int i = 0; i < 36; ++i) P.R[i] = bldu4<0>(rs, vo, i * 8 * ldb);
    __builtin_amdgcn_sched_barrier(0);
}
template <bool PUBLISH, bool COLSUM>
__device__ __forceinline__ void prep_finish(const Args& a, int job, LAS char* lds, int lane, unsigned tag, const PrepRegs& P) {
    const int sub = job / 18, rem = job % 18, kh = rem / 9, ns = rem % 9;
    const rsrc_t rd = mk_rsrc(a.ws + WS_WF + (size_t)sub * WSUB * 2, WSUB * 2);
    const int fl = (lane >> 4) & 1;
    const int w0 = (lane >> 3) * 128 + (((lane & 7) ^ (fl << 1)) * 16), w1 = (lane >> 3) * 128 + (((lane & 7) ^ ((fl | 2) << 1)) * 16);
    const int g = lane >> 4, i16 = lane & 15, q = i16 >> 2, p = lane & 3;
    const int f = ((q >> 1) & 1) | ((g & 1) << 1);
    int rb[4];
#pragma unroll
    for (int n = 0; n < 4; ++n) rb[n] = (8 * g + q) * 128 + (((2 * n + (p >> 1)) ^ (f << 1)) * 16) + 8 * (p & 1);
    float cs[4] = {0.f, 0.f, 0.f, 0.f};
#pragma unroll
    for (int u = 0; u < 9; ++u) {
        LAS char* sl = lds + (u & 1) * 4096;
#pragma unroll
        for (int i = 0; i < 4; ++i) *(LAS u32x4*)(sl + i * 1024 + ((i & 1) ? w1 : w0)) = P.R[4 * u + i];
#pragma unroll
        for (int n = 0; n < 4; ++n) {
            const u32x4 x = __builtin_bit_cast(u32x4, cat8(tr_read(sl + rb[n]), tr_read(sl + rb[n] + 512)));
            bst16(x, rd, lane * 16, ((kh * 9 + u) * 36 + ns * 4 + n) * 1024);
            if (COLSUM) {
#pragma unroll
                for (int k = 0; k < 4; ++k) cs[n] = dot2bf(x[k], 0x3f803f80u, cs[n]);
            }
        }
    }
    if (COLSUM && (sub & 1) == 0) {
        const rsrc_t rc = mk_rsrc(a.ws + WS_CS, 2 * (D + F) * 4);
#pragma unroll
        for (int n = 0; n < 4; ++n) {
            const float c = rsum_g(cs[n]);
            if (lane < 16) bst4(__float_as_uint(c), rc, lane * 4, (kh * (D + F) + (sub == 0 ? 0 : D + ((sub - 2) >> 1) * D) + ns * 64 + n * 16) * 4);
        }
    }
    if (PUBLISH) {
        VM0();
        if (lane == 0) __hip_atomic_store((gu32*)(a.ws + WS_FW) + job, tag, RLX_AGENT);
    }
}

constexpr int CV_RC = 4160;
constexpr int CV_U = 4 * CV_RC;
constexpr int CV_X = CV_U + 2288 * 2;
constexpr int CV_RED = CV_X + 4096;
constexpr int CV_CH = CV_RED + 24576;
constexpr int KG_ROW = 544;
constexpr int KG_XI = 32 * KG_ROW, KG_T = CV_RED + 4096;

struct cpx { float re, im; };
__device__ __forceinline__ cpx cmul(cpx a, cpx b) { return {a.re * b.re - a.im * b.im, a.re * b.im + a.im * b.re}; }
__device__ __forceinline__ cpx cexp_rev(float a2, float rev, float m) {
    const float e = __builtin_amdgcn_exp2f(a2 * m), p = __builtin_amdgcn_fractf(rev * m);
    return {e * __builtin_amdgcn_cosf(p), e * __builtin_amdgcn_sinf(p)};
}
__device__ __forceinline__ unsigned pk_f16(cpx c) { f32x2 v = {c.re, c.im}; return __builtin_bit_cast(unsigned, __builtin_convertvector(v, f16x2)); }

__device__ __forceinline__ void conv_put(LAS char* lds, int m, u32x4 A, u32x2 B) {
    u32x4 c1 = {__builtin_amdgcn_alignbit(A[1], A[0], 16), __builtin_amdgcn_alignbit(A[2], A[1], 16), __builtin_amdgcn_alignbit(A[3], A[2], 16), __builtin_amdgcn_alignbit(B[0], A[3], 16)};
    u32x4 c2 = {A[1], A[2], A[3], B[0]};
    u32x4 c3 = {c1[1], c1[2], c1[3], __builtin_amdgcn_alignbit(B[1], B[0], 16)};
    *(LAS u32x4*)(lds + 0 * CV_RC + m * 16) = A;
    *(LAS u32x4*)(lds + 1 * CV_RC + m * 16) = c1;
    *(LAS u32x4*)(lds + 2 * CV_RC + m * 16) = c2;
    *(LAS u32x4*)(lds + 3 * CV_RC + m * 16) = c3;
}

constexpr int LDS_FWOK = 3 * CV_CH;
__device__ __forceinline__ void phase_conv(const Args& a, LAS char* lds0, unsigned tagv, const StatRow& sr, unsigned& tag_out) {
    const int tid = threadIdx.x, w = __builtin_amdgcn_readfirstlane(tid >> 6), lane = tid & 63;
    const int b = blockIdx.x, chl = w >> 2, wq = w & 3, t8 = tid & 255, ch = 3 * b + chl;
    LAS char* lds = lds0 + chl * CV_CH;
    const int part = wq >> 1, n = (wq & 1) * 64 + lane;
    const int o = ch * NS + n, o2 = ch * NS + (n ^ 64);
    const u16 r_ld = a.logdt[ch], r_ar = a.are[o], r_ai = a.aim[o], r_ar2 = a.are[o2];
    u16 r_b[4] = {}, r_c[4] = {};
    if (part) { r_b[0] = a.bre[o]; r_b[1] = a.bim[o]; r_c[0] = a.cre[o]; r_c[1] = a.cim[o]; r_b[2] = a.bre[o2]; r_b[3] = a.bim[o2]; r_c[2] = a.cre[o2]; r_c[3] = a.cim[o2]; }
    const u16 r_ds = a.dskip[ch];
    __builtin_amdgcn_sched_barrier(0);
    const unsigned tag = __builtin_amdgcn_readfirstlane(tagv) + 1u;
    tag_out = tag;
    if (tid == 0) __hip_atomic_store((gu32*)(a.ws + WS_FA) + b, tag, RLX_AGENT);
    row_stats(a, sr, tag, w, lane);
    __builtin_amdgcn_sched_barrier(0);
    u32x2 xr[4];
    const rsrc_t rx = mk_rsrc(a.x, L * D * 2);
    const int xvo = tid * (2 * D * 2) + ((6 * b) & ~3);
#define GATHER(i) do { __builtin_amdgcn_sched_barrier(0); if ((i) < 2 || w < 4) xr[i] = bldu2<0>(rx, xvo + ((i) & 1) * D * 2, ((i) >> 1) * 1536 * D * 2); __builtin_amdgcn_sched_barrier(0); } while (0)
    GATHER(0);
    const float dt = rbf(expf(bf2f(r_ld)));
    const float are = dt * bf2f(r_ar);
    const float amax = fmaxf(are, dt * bf2f(r_ar2));
    float ap = 0.f;
    if (__any(amax > 0.f)) ap = rmax_64(fmaxf(amax, 0.f));
    const float a2 = (are - ap) * 1.4426950408889634f, rev = dt * bf2f(r_ai) * 0.15915494309189535f;
    float kscale = 1.f;
    if (part == 0) {
        cpx e[4];
        const cpx g8 = cexp_rev(a2, rev, 8.f), g32 = cexp_rev(a2, rev, 32.f);
        e[0] = {1.f, 0.f}; e[1] = g8; e[2] = cmul(g8, g8); e[3] = cmul(e[2], g8);
        LAS char* er = lds + n * 4;
#pragma unroll
        for (int t = 0; t < 8; ++t) {
            if (t == 0) GATHER(1);
            if (t == 3) GATHER(2);
            if (t == 6) GATHER(3);
#pragma unroll
            for (int c = 0; c < 4; ++c) {
                *(LAS unsigned*)(er + (4 * t + c) * KG_ROW) = pk_f16(e[c]);
                if (t < 7) e[c] = (t == 0 && c == 0) ? g32 : cmul(e[c], g32);
            }
        }
    } else {
        const float b0 = dt * bf2f(r_b[0]), b1 = dt * bf2f(r_b[1]), c0 = bf2f(r_c[0]), c1 = bf2f(r_c[1]);
        const float b2 = dt * bf2f(r_b[2]), b3 = dt * bf2f(r_b[3]), c2 = bf2f(r_c[2]), c3 = bf2f(r_c[3]);
        const float wr = c0 * b0 - c1 * b1, wi = c0 * b1 + c1 * b0, vr = c2 * b2 - c3 * b3, vi = c2 * b3 + c3 * b2;
        const float wm = rmax_64(fmaxf(fmaxf(fabsf(wr), fabsf(wi)), fmaxf(fabsf(vr), fabsf(vi))));
        const unsigned ex = (__float_as_uint(wm) >> 23) & 0xffu;
        const unsigned exc = ex < 2u ? 2u : (ex > 252u ? 252u : ex);
        const float sc = __uint_as_float((254u - exc) << 23);
        kscale = __uint_as_float(exc << 23);
        const cpx W = {wr * sc, -wi * sc};
        const cpx g1 = cexp_rev(a2, -rev, 1.f), p256 = cexp_rev(a2, -rev, 256.f);
        const cpx g2 = cmul(g1, g1), g4 = cmul(g2, g2), p512 = cmul(p256, p256);
        LAS char* xr_ = lds + KG_XI + n * 4;
#define XST(i, v) *(LAS unsigned*)(xr_ + (i) * KG_ROW) = pk_f16(v)
        GATHER(1);
        cpx x = W; XST(0, x);
#pragma unroll
        for (int i = 1; i < 8; ++i) { x = cmul(x, g1); XST(i, x); }
        GATHER(2);
        x = cmul(W, p256); XST(8, x);
#pragma unroll
        for (int i = 9; i < 12; ++i) { x = cmul(x, g2); XST(i, x); }
        cpx y = cmul(W, p512); XST(12, y); XST(13, cmul(y, g4));
        x = cmul(y, p256); XST(14, x); XST(15, cmul(x, g4));
        GATHER(3);
        y = cmul(y, p512); XST(16, y);
#pragma unroll
        for (int i = 17; i < 21; ++i) { y = cmul(y, p256); XST(i, y); }
#undef XST
#undef GATHER
        if (lane == 0 && wq == 2) *(LAS float*)(lds + KG_T - 16) = kscale;
    }
    const int p = lane & 15, g = lane >> 4;
    LAS float* T = (LAS float*)(lds + KG_T);
    int base, idx0, istr;
    if (wq & 1) {
        base = p < 4 ? 1024 + 256 * p : (p == 4 ? 2048 : 0);
        idx0 = p < 4 ? 512 + 32 * p : (p == 4 ? 640 : 680);
        istr = 1;
    } else {
        const int q4 = p & 3;
        base = p < 8 ? p : (p < 12 ? 256 + 2 * q4 : 512 + 4 * (p & 1) + 128 * (p & 2));
        idx0 = p < 8 ? p : (p < 12 ? 256 + q4 : 384 + (p & 1) + 32 * (p & 2));
        istr = p < 8 ? 8 : (p < 12 ? 4 : 2);
    }
    const int j0 = (wq >> 1) * 16 + 4 * g;
    LAS float* tw = T + idx0 + j0 * istr;
    const LAS char* ea = lds + ((wq >> 1) * 16 + p) * KG_ROW + g * 16;
    const LAS char* xa = lds + KG_XI + ((wq & 1) * 16 + p) * KG_ROW + g * 16;
    LGKM0(); BAR();
    {
        f32x4 acc = {};
        f16x8 A[8], B[8];
#pragma unroll
        for (int t = 0; t < 8; ++t) { A[t] = *(const LAS f16x8*)(ea + t * 64); B[t] = *(const LAS f16x8*)(xa + t * 64); }
        float ks = *(const LAS float*)(lds + KG_T - 16);
        __builtin_amdgcn_sched_barrier(0);
#pragma unroll
        for (int t = 0; t < 8; ++t) acc = __builtin_amdgcn_mfma_f32_16x16x32_f16(A[t], B[t], acc, 0, 0, 0);
        if (ap > 0.f) {
#pragma unroll
            for (int q = 0; q < 4; ++q) acc[q] *= __builtin_amdgcn_exp2f(ap * 1.4426950408889634f * (float)(base + 8 * (j0 + q)));
        }
#pragma unroll
        for (int q = 0; q < 4; ++q) tw[q * istr] = acc[q] * ks;
    }
    LGKM0(); BAR();
    {
        float v[12];
        float lf = (float)(2047 - t8 * 8);
        asm volatile("" : "+v"(lf));
        const unsigned tb = (unsigned)(size_t)T - 17024u * 4u;
        if (wq < 3) {
#pragma unroll
            for (int e = 0; e < 12; e += 2) {
                const unsigned bits = pk_bf16(lf - (float)e, lf - (float)(e + 1));
                v[e] = *(const LAS float*)(size_t)(((bits & 0xffffu) << 2) + tb);
                v[e + 1] = *(const LAS float*)(size_t)(((bits >> 16) << 2) + tb);
            }
        } else {
#pragma unroll
            for (int e = 0; e < 12; ++e) {
                const int l = 2047 - (t8 * 8 + e);
                const int idx = l < 128 ? (l < 0 ? 0 : l) : (int)(pk_bf16((float)l, 0.f) & 0xffffu) - 17024;
                const float t = T[idx];
                v[e] = l < 0 ? 0.f : t;
            }
        }
        conv_put(lds, t8, (u32x4){pk_bf16(v[0], v[1]), pk_bf16(v[2], v[3]), pk_bf16(v[4], v[5]), pk_bf16(v[6], v[7])}, (u32x2){pk_bf16(v[8], v[9]), pk_bf16(v[10], v[11])});
        if (t8 < 4) conv_put(lds, 256 + t8, (u32x4){0u, 0u, 0u, 0u}, (u32x2){0u, 0u});
        if (t8 >= 64 && t8 < 94) *(LAS u32x4*)(lds + CV_U + (t8 - 64) * 16) = (u32x4){0u, 0u, 0u, 0u};
    }
    {
        u32x4 gv[2];
        const rsrc_t rgn = mk_rsrc(a.ws + WS_GR, L * 8);
        const unsigned long long t0 = __builtin_amdgcn_s_memrealtime();
        for (;;) {
            gv[0] = bldu4<SC1>(rgn, tid * 16, 0);
            gv[1] = w < 4 ? bldu4<SC1>(rgn, tid * 16, 1536 * 8) : gv[0];
            const bool ok = gv[0][1] == tag && gv[0][3] == tag && gv[1][1] == tag && gv[1][3] == tag;
            if (__all(ok)) break;
            if (__builtin_amdgcn_s_memrealtime() - t0 > WAIT_TICKS) break;
            __builtin_amdgcn_s_sleep(2);
            asm volatile("" ::: "memory");
        }
#pragma unroll
        for (int i = 0; i < 2; ++i) {
            if (i == 0 || w < 4) {
                const int row = 2 * tid + 1536 * i;
                u32x2 x0 = xr[2 * i], x1 = xr[2 * i + 1];
                if (b & 1) {
                    x0 = (u32x2){__builtin_amdgcn_alignbit(x0[1], x0[0], 16), x0[1] >> 16};
                    x1 = (u32x2){__builtin_amdgcn_alignbit(x1[1], x1[0], 16), x1[1] >> 16};
                }
                const float m0 = bf_lo(gv[i][0]), m1 = bf_lo(gv[i][2]);
                const float s0 = __builtin_amdgcn_rcpf(bf_hi(gv[i][0])), s1 = __builtin_amdgcn_rcpf(bf_hi(gv[i][2]));
#pragma unroll
                for (int c = 0; c < 3; ++c) {
                    const unsigned xp = c == 0 ? __builtin_amdgcn_perm(x1[0], x0[0], 0x05040100u) : c == 1 ? __builtin_amdgcn_perm(x1[0], x0[0], 0x07060302u)
                                                                                                            : __builtin_amdgcn_perm(x1[1], x0[1], 0x05040100u);
                    const unsigned dd = pk_bf16(bf_lo(xp) - m0, bf_hi(xp) - m1);
                    *(LAS unsigned*)(lds0 + c * CV_CH + CV_U + 480 + row * 2) = pk_bf16(bf_lo(dd) * s0, bf_hi(dd) * s1);
                    *(LAS unsigned*)(lds0 + c * CV_CH + CV_X + row * 2) = xp;
                }
            }
        }
    }
    LGKM0(); BAR();
    unsigned fwv = tag;
    if (w == 11 && lane < 36) fwv = __hip_atomic_load((gu32*)(a.ws + WS_FW) + lane, RLX_AGENT);
    f32x4 acc[8];
#pragma unroll
    for (int c = 0; c < 8; ++c) acc[c] = (f32x4){0.f, 0.f, 0.f, 0.f};
    const int cpy = (3 - p) & 3;
#pragma unroll
    for (int rr = 0; rr < 2; ++rr) {
        const int r = 2 * wq + rr;
        const LAS char* ab = lds + cpy * CV_RC + 2 * (2031 - 32 * r + 8 * g - p - cpy) - 512 * 7;
        const LAS char* fb = lds + CV_U + 2 * (240 - 32 * r + 16 * p - 16 + 8 * g);
        bf16x8 A[8], Fm[8];
#pragma unroll
        for (int s = 0; s < 8; ++s) {
            const s16x4 lo = *(const volatile LAS s16x4*)(ab + 512 * (7 - s));
            const s16x4 hi = *(const volatile LAS s16x4*)(ab + 512 * (7 - s) + 8);
            A[s] = cat8(lo, hi);
        }
#pragma unroll
        for (int e = 0; e < 8; ++e) Fm[e] = *(const LAS bf16x8*)(fb + 512 * e);
#pragma unroll
        for (int c = 0; c < 8; ++c)
#pragma unroll
            for (int s = 0; s <= c; ++s) acc[c] = MFMA16(A[s], Fm[c - s], acc[c]);
    }
    LAS f32x4* red = (LAS f32x4*)(lds + CV_RED) + lane;
    f32x4 own[2];
#define RED_CASE(WQ) { _Pragma("unroll") for (int c = 0; c < 8; ++c) if ((c >> 1) != WQ) red[((c >> 1) * 6 + (c & 1) * 3 + (WQ > (c >> 1) ? WQ - 1 : WQ)) * 64] = acc[c]; \
                       own[0] = acc[2 * WQ]; own[1] = acc[2 * WQ + 1]; }
    if (wq == 0) RED_CASE(0) else if (wq == 1) RED_CASE(1) else if (wq == 2) RED_CASE(2) else RED_CASE(3)
#undef RED_CASE
    if (w == 11) { const bool okw = __all(fwv == tag); if (lane == 0) *(LAS unsigned*)(lds0 + LDS_FWOK) = okw ? 1u : 0u; }
    LGKM0(); BAR();
    const rsrc_t rh = mk_rsrc(a.ws + WS_HT + (size_t)ch * L * 2, L * 2);
    const float dsk = bf2f(r_ds);
#pragma unroll
    for (int i = 0; i < 2; ++i) {
        f32x4 y = own[i];
#pragma unroll
        for (int k = 0; k < 3; ++k) y += red[(wq * 6 + i * 3 + k) * 64];
        const int l0 = 256 * (2 * wq + i) + 16 * p + 4 * g;
        const u32x2 uu = *(const LAS u32x2*)(lds + CV_U + 480 + 2 * l0), xv = *(const LAS u32x2*)(lds + CV_X + 2 * l0);
        float o[4];
#pragma unroll
        for (int k = 0; k < 4; ++k) {
            const float u = (k & 1) ? bf_hi(uu[k >> 1]) : bf_lo(uu[k >> 1]);
            const float x = (k & 1) ? bf_hi(xv[k >> 1]) : bf_lo(xv[k >> 1]);
            o[k] = x + __builtin_fmaf(u, dsk, y[k]);
        }
        bst8((u32x2){pk_bf16(o[0], o[1]), pk_bf16(o[2], o[3])}, rh, 2 * l0, 0);
    }
    VM0(); BAR();
    if (w == 0) publish_rep(a.ws, WS_FH, b, tag, lane);
}

__device__ __forceinline__ int nb0_of(int j) { return (9 * j + 1) >> 1; }

constexpr int OUT_ROW = 176;
constexpr int KC_XB = 73728, KC_ST = KC_XB + 20 * 1024, KC_OUT = KC_ST + 512;
__device__ __forceinline__ void phase_c(char* __restrict__ ws, LAS char* lds, int panel, int j, unsigned tag) {
    const int tid = threadIdx.x, w = __builtin_amdgcn_readfirstlane(tid >> 6), lane = tid & 63;
    const int tok0 = panel * 64, nbs = nb0_of(j), nblk = nb0_of(j + 1) - nbs;
    const int g = lane >> 4, i16 = lane & 15, q = i16 >> 2, p = lane & 3;
    const int b = w >> 1, kh = w & 1;
    const bool act = w < 10 && b < nblk;
    const int nb = nbs + (act ? b : 0);
    if (w == 0 && *(const LAS unsigned*)(lds + LDS_FWOK) == 0u) wait_flags(ws, WS_FW, 0, 36, WS_FW, 0, 0, tag);
    BAR();
    bf16x8 W[9];
    f32x4 cs0, cs1;
    if (w < 10) {
        const rsrc_t rw = mk_rsrc(ws + WS_WF, WSUB * 2);
#pragma unroll
        for (int i = 0; i < 9; ++i) W[i] = bld16<SC1>(rw, lane * 16, ((kh * 9 + i) * 36 + nb) * 1024);
        const rsrc_t rc = mk_rsrc(ws + WS_CS, 2 * (D + F) * 4);
        cs0 = bldf4<SC1>(rc, g * 16, nb * 64); cs1 = bldf4<SC1>(rc, g * 16, nb * 64 + (D + F) * 4);
    }
    if (w == 11) wait_flags(ws, WS_FH, (blockIdx.x & (REP - 1)) * 256, 192, WS_FH, 0, 0, tag);
    BAR();
    {
        const rsrc_t rh = mk_rsrc(ws + WS_HT, D * L * 2);
        const int fl = (lane >> 4) & 1;
        const int v0 = (lane >> 3) * 4096 + (((lane & 7) ^ (fl << 1)) * 16), v1 = (lane >> 3) * 4096 + (((lane & 7) ^ ((fl | 2) << 1)) * 16);
#pragma unroll
        for (int k = 0; k < 6; ++k) { const int idx = 6 * w + k; blds16<SC1>(rh, lds + idx * 1024, (k & 1) ? v1 : v0, idx * 32768 + tok0 * 2); }
    }
    VM0(); BAR();
    const int f = ((q >> 1) & 1) | ((g & 1) << 1);
    const int lo_ = (8 * g + q) * 128 + 8 * (p & 1);
    f32x4 acc[4] = {};
    LAS f32x2* st = (LAS f32x2*)(lds + KC_ST);
    LAS f32x4* xb = (LAS f32x4*)(lds + KC_XB);
    if (w < 10) {
        const LAS char* hb[4];
#pragma unroll
        for (int mt = 0; mt < 4; ++mt) hb[mt] = lds + kh * 9 * 4096 + lo_ + (((2 * mt + (p >> 1)) ^ (f << 1)) * 16);
        bf16x8 H[2][2][4];
#pragma unroll
        for (int c = 0; c < 6; ++c) {
            if (c < 5) {
#pragma unroll
                for (int i = 0; i < 2; ++i)
                    if (2 * c + i < 9) {
#pragma unroll
                        for (int mt = 0; mt < 4; ++mt) H[c & 1][i][mt] = cat8(tr_read(hb[mt] + (2 * c + i) * 4096), tr_read(hb[mt] + (2 * c + i) * 4096 + 512));
                    }
            }
            __builtin_amdgcn_sched_barrier(0);
            if (c > 0) {
#pragma unroll
                for (int i = 0; i < 2; ++i)
                    if (2 * (c - 1) + i < 9) {
#pragma unroll
                        for (int mt = 0; mt < 4; ++mt) acc[mt] = MFMA16(W[2 * (c - 1) + i], H[(c - 1) & 1][i][mt], acc[mt]);
                    }
            }
            __builtin_amdgcn_sched_barrier(0);
        }
        xb[((w * 2 + 0) * 64) + lane] = sel4(kh, acc[0], acc[2]);
        xb[((w * 2 + 1) * 64) + lane] = sel4(kh, acc[1], acc[3]);
    } else {
        __builtin_amdgcn_s_setprio(3);
        const bf16x8 ones = {0x3f80, 0x3f80, 0x3f80, 0x3f80, 0x3f80, 0x3f80, 0x3f80, 0x3f80};
        const int m0 = 2 * (w - 10);
        const LAS char* h0 = lds + lo_ + (((2 * m0 + (p >> 1)) ^ (f << 1)) * 16);
        const LAS char* h1 = lds + lo_ + (((2 * m0 + 2 + (p >> 1)) ^ (f << 1)) * 16);
        bf16x8 H[2][3][2];
#pragma unroll
        for (int c = 0; c < 7; ++c) {
            if (c < 6) {
#pragma unroll
                for (int i = 0; i < 3; ++i) {
                    H[c & 1][i][0] = cat8(tr_read(h0 + (3 * c + i) * 4096), tr_read(h0 + (3 * c + i) * 4096 + 512));
                    H[c & 1][i][1] = cat8(tr_read(h1 + (3 * c + i) * 4096), tr_read(h1 + (3 * c + i) * 4096 + 512));
                }
            }
            __builtin_amdgcn_sched_barrier(0);
            if (c > 0) {
#pragma unroll
                for (int i = 0; i < 3; ++i) {
                    const bf16x8 H0 = H[(c - 1) & 1][i][0], H1 = H[(c - 1) & 1][i][1];
                    acc[0] = MFMA16(ones, H0, acc[0]); acc[1] = MFMA16(H0, H0, acc[1]);
                    acc[2] = MFMA16(ones, H1, acc[2]); acc[3] = MFMA16(H1, H1, acc[3]);
                }
            }
            __builtin_amdgcn_sched_barrier(0);
        }
        const float sm[2] = {acc[0][0], acc[2][0]};
        if (g == q) {
            const rsrc_t r2 = mk_rsrc(ws + WS_ST2, L * 8);
#pragma unroll
            for (int t = 0; t < 2; ++t) {
                const f32x4 d = acc[2 * t + 1];
                const float s2 = p == 0 ? d[0] : p == 1 ? d[1] : p == 2 ? d[2] : d[3];
                const float mean = sm[t] * (1.0f / D), var = fmaxf(s2 * (1.0f / D) - mean * mean, 0.f);
                const float mb = rbf(mean), rs = __builtin_amdgcn_rcpf(ln_sd(var));
                const float m2 = rbf((mean - mb) * rs), rs2 = __builtin_amdgcn_rcpf(ln_sd(var * rs * rs));
                st[(m0 + t) * 16 + i16] = (f32x2){rs * rs2, (mb * rs + m2) * rs2};
                if (j == 0) bst8((u32x2){__float_as_uint(mb), __float_as_uint(rs)}, r2, (tok0 + (m0 + t) * 16 + i16) * 8, 0);
            }
        }
    }
    __builtin_amdgcn_s_setprio(0);
    LGKM0(); BAR();
    const rsrc_t rg = mk_rsrc(ws + WS_G, (L + 4) * D * 2);
    LAS char* og = lds + KC_OUT;
    if (act) {
        const f32x4 cs = cs0 + cs1;
#pragma unroll
        for (int t = 0; t < 2; ++t) {
            const f32x4 a = sel4(kh, acc[2 + t], acc[t]) + xb[((w ^ 1) * 2 + t) * 64 + lane];
            const f32x2 ab = st[(2 * kh + t) * 16 + i16];
            float o[4];
#pragma unroll
            for (int k = 0; k < 4; ++k) o[k] = gelu_tanh(ab[0] * a[k] - ab[1] * cs[k]);
            *(LAS u32x2*)(og + (kh * 32 + t * 16 + i16) * OUT_ROW + b * 32 + g * 8) = (u32x2){pk_bf16(o[0], o[1]), pk_bf16(o[2], o[3])};
        }
    }
    LGKM0(); BAR();
    {
        const int nc = 2 * nblk, row = nblk == 5 ? tid / 10 : tid >> 3, c = tid - row * nc;
        if (row < 64) bst16(*(const LAS u32x4*)(og + row * OUT_ROW + c * 16), rg, row * (D * 2) + c * 16, ((2 + tok0) * D + nbs * 16) * 2);
        if ((panel == 0 || panel == 31) && tid >= 704 && tid < 704 + 2 * nc) {
            const int z = tid - 704, zr = z >= nc ? 1 : 0;
            bst16((u32x4){0u, 0u, 0u, 0u}, rg, zr * (D * 2) + (z - zr * nc) * 16, ((panel == 0 ? 0 : L + 2) * D + nbs * 16) * 2);
        }
    }
    VM0(); BAR();
    publish(ws, WS_FG, panel * 32 + j, tag);
}

constexpr int KD_HT = 73728, KD_XB = KD_HT + 10240, KD_OUT = KD_XB + 20 * 1024;
__device__ __forceinline__ void phase_d(char* __restrict__ ws, LAS char* lds, int panel, int j, int je, unsigned tag) {
    const int tid = threadIdx.x, w = __builtin_amdgcn_readfirstlane(tid >> 6), lane = tid & 63;
    const int tok0 = panel * 64, nbs = nb0_of(j), nblk = nb0_of(j + 1) - nbs;
    const int g = lane >> 4, i16 = lane & 15, q = i16 >> 2, p = lane & 3;
    const int b = w >> 1, kh = w & 1;
    const bool act = w < 10 && b < nblk;
    const int nb = nbs + (act ? b : 0);
    bf16x8 W[9];
    if (w < 10) {
        const rsrc_t rw = mk_rsrc(ws + WS_WF + WSUB * 2, WSUB * 2);
#pragma unroll
        for (int i = 0; i < 9; ++i) W[i] = bld16<SC1>(rw, lane * 16, ((kh * 9 + i) * 36 + nb) * 1024);
    }
    if (w < 2 * nblk) {
        const rsrc_t rh = mk_rsrc(ws + WS_HT, D * L * 2);
        const int fl = ((lane >> 4) & 1) | ((w & 1) << 1);
        blds16<SC1>(rh, lds + KD_HT + w * 1024, (lane >> 3) * 4096 + (((lane & 7) ^ (fl << 1)) * 16), (nbs * 16 + w * 8) * 4096 + tok0 * 2);
    }
    if (w == 11) {
        const int p0 = panel > 0 ? panel - 1 : 0, p1 = panel < 31 ? panel + 1 : 31;
        wait_panels(ws, WS_FG, p0, WS_FG, panel, WS_FG, p1, tag);
    }
    BAR();
    f32x2 st[2];
    {
        const rsrc_t rg = mk_rsrc(ws + WS_G, (L + 4) * D * 2);
        const int c7 = (lane & 7) ^ ((lane >> 3) & 6), vb = (lane >> 3) * 1152 + c7 * 16;
#pragma unroll
        for (int k = 0; k < 6; ++k) {
            const int idx = 6 * w + k, cg = idx >> 3, c = cg * 8 + c7;
            const int o2 = (0x4031 >> (((c * 57) >> 10) * 4)) & 15;
            blds16<SC1>(rg, lds + idx * 1024, vb + o2 * 1152, (tok0 + (idx & 7) * 8) * 1152 + cg * 128);
        }
        const rsrc_t r2 = mk_rsrc(ws + WS_ST2, L * 8);
#pragma unroll
        for (int t = 0; t < 2; ++t) st[t] = __builtin_bit_cast(f32x2, bldu2<SC1>(r2, i16 * 8, (tok0 + kh * 32 + t * 16) * 8));
    }
    VM0();
    asm volatile("" : "+v"(st[0]), "+v"(st[1]));
    BAR();
    f32x4 acc[4] = {};
    LAS f32x4* xb = (LAS f32x4*)(lds + KD_XB);
    if (w < 10) {
        const int z = i16 & 6;
        const LAS char* a0 = lds + i16 * 128 + ((g ^ z) * 16), * a1 = lds + i16 * 128 + (((4 + g) ^ z) * 16);
        const LAS char* e0 = kh ? a1 + 4 * 8192 : a0;
        const LAS char* e1 = kh ? a0 + 5 * 8192 : a1;
        bf16x8 A[2][2][4];
#pragma unroll
        for (int c = 0; c < 6; ++c) {
            if (c < 5) {
#pragma unroll
                for (int ii = 0; ii < 2; ++ii) {
                    const int i = 2 * c + ii;
                    if (i < 9) {
#pragma unroll
                        for (int mt = 0; mt < 4; ++mt) A[c & 1][ii][mt] = *(const LAS bf16x8*)(((i & 1) ? e1 : e0) + (i >> 1) * 8192 + mt * 2048);
                    }
                }
            }
            __builtin_amdgcn_sched_barrier(0);
            if (c > 0) {
#pragma unroll
                for (int ii = 0; ii < 2; ++ii)
                    if (2 * (c - 1) + ii < 9) {
#pragma unroll
                        for (int mt = 0; mt < 4; ++mt) acc[mt] = MFMA16(W[2 * (c - 1) + ii], A[(c - 1) & 1][ii][mt], acc[mt]);
                    }
            }
            __builtin_amdgcn_sched_barrier(0);
        }
        xb[((w * 2 + 0) * 64) + lane] = sel4(kh, acc[0], acc[2]);
        xb[((w * 2 + 1) * 64) + lane] = sel4(kh, acc[1], acc[3]);
    }
    LGKM0(); BAR();
    LAS char* ox = lds + KD_OUT;
    if (w < 10) {
        const int bl = act ? b : 0;
        const int hrow = 16 * bl + 4 * g + q;
        const LAS char* hb = lds + KD_HT + hrow * 128 + 8 * (p & 1);
        const rsrc_t rp = mk_rsrc(ws + WS_PST, 32 * 36 * 64 * 8);
#pragma unroll
        for (int t = 0; t < 2; ++t) {
            const int mt = 2 * kh + t;
            const f32x4 a = sel4(kh, acc[2 + t], acc[t]) + xb[((w ^ 1) * 2 + t) * 64 + lane];
            const u32x2 hu = __builtin_bit_cast(u32x2, tr_read(hb + (((2 * mt + (p >> 1)) ^ (fsw(hrow) << 1)) * 16)));
            const float rs = st[t][1], nm = -st[t][0] * rs;
            u32x2 ov;
#pragma unroll
            for (int k = 0; k < 2; ++k)
                ov[k] = pk_bf16(__builtin_fmaf(bf_lo(hu[k]), rs, nm) + a[2 * k], __builtin_fmaf(bf_hi(hu[k]), rs, nm) + a[2 * k + 1]);
            float s1 = dot2bf(ov[1], 0x3f803f80u, dot2bf(ov[0], 0x3f803f80u, 0.f)), s2 = dot2bf(ov[1], ov[1], dot2bf(ov[0], ov[0], 0.f));
            s1 = rsum_g(s1); s2 = rsum_g(s2);
            if (act) {
                *(LAS u32x2*)(ox + (mt * 16 + i16) * OUT_ROW + b * 32 + g * 8) = ov;
                if (lane < 16) bst8((u32x2){__float_as_uint(s1), __float_as_uint(s2)}, rp, lane * 8, ((panel * 36 + nb) * 64 + mt * 16) * 8);
            }
        }
    }
    LGKM0(); BAR();
    {
        const int nc = 2 * nblk, row = nblk == 5 ? tid / 10 : tid >> 3, c = tid - row * nc;
        if (row < 64) {
            const int so = (tok0 * D + nbs * 16) * 2, vo = row * (D * 2) + c * 16;
            bst16(*(const LAS u32x4*)(ox + row * OUT_ROW + c * 16), mk_rsrc(ws + WS_X1, L * D * 2), vo, so);
        }
    }
    if (w == 10) wait_flags(ws, WS_FW, (2 + 2 * je) * 18, 18, WS_FW, 0, 0, tag);
    VM0(); BAR();
    publish(ws, WS_FX, panel * 32 + j, tag);
}

constexpr int BG_LDS = 131072;
__device__ __forceinline__ void phase_cd_background(const Args& a, LAS char* lds, int panel, int job, unsigned tag) {
    char* ws = a.ws;
    const int tid = threadIdx.x, w = __builtin_amdgcn_readfirstlane(tid >> 6), lane = tid & 63, tok0 = panel * 64;
    BAR(); BAR();
    {
        const rsrc_t rh = mk_rsrc(ws + WS_HT, D * L * 2);
        const int fl = (lane >> 4) & 1;
        const int v0 = (lane >> 3) * 4096 + (((lane & 7) ^ (fl << 1)) * 16), v1 = (lane >> 3) * 4096 + (((lane & 7) ^ ((fl | 2) << 1)) * 16);
#pragma unroll
        for (int k = 0; k < 6; ++k) { const int idx = 6 * w + k; blds16<SC1>(rh, lds + idx * 1024, (k & 1) ? v1 : v0, idx * 32768 + tok0 * 2); }
    }
    VM0(); BAR();
    PrepRegs P;
    prep_issue(a, job, lane, P);
    BAR(); BAR(); BAR();
    prep_finish<false, false>(a, job, lds + BG_LDS + (w - 8) * 8192, lane, tag, P);
    BAR();
    {
        const rsrc_t rg = mk_rsrc(ws + WS_G, (L + 4) * D * 2);
        const int c7 = (lane & 7) ^ ((lane >> 3) & 6), vb = (lane >> 3) * 1152 + c7 * 16;
#pragma unroll
        for (int k = 0; k < 6; ++k) {
            const int idx = 6 * w + k, cg = idx >> 3, c = cg * 8 + c7;
            const int o2 = (0x4031 >> (((c * 57) >> 10) * 4)) & 15;
            blds16<SC1>(rg, lds + idx * 1024, vb + o2 * 1152, (tok0 + (idx & 7) * 8) * 1152 + cg * 128);
        }
    }
    VM0();
    if (lane == 0) __hip_atomic_store((gu32*)(ws + WS_FW) + job, tag, RLX_AGENT);
    BAR(); BAR(); BAR(); BAR();
}

constexpr int KE_HID = 73728, KE_ST = 2 * 73728, KE_FLG = KE_ST + 1024;
constexpr int NB = 3, RING = 4;
__device__ __forceinline__ void phase_e(char* __restrict__ ws, LAS char* lds, int panel, int j, unsigned tag) {
    const int w = __builtin_amdgcn_readfirstlane(threadIdx.x >> 6), lane = threadIdx.x & 63;
    constexpr int S1 = 6, S2 = 12;
    const int tok0 = panel * 64, g = lane >> 4, i16 = lane & 15, nbs = NB * w;
    const rsrc_t rw = mk_rsrc(ws + WS_WF + (size_t)(2 + 2 * j) * WSUB * 2, 2 * WSUB * 2);
    const int wv = lane * 16, wsb = nbs * 1024;
    bf16x8 W[RING][NB];
#pragma unroll
    for (int s = 0; s < RING; ++s)
#pragma unroll
        for (int b = 0; b < NB; ++b) W[s][b] = bld16<SC1>(rw, wv + b * 1024, wsb + s * 36864);
    f32x4 acc[NB][4];
    {
        const rsrc_t rc = mk_rsrc(ws + WS_CS, 2 * (D + F) * 4);
#pragma unroll
        for (int b = 0; b < NB; ++b) {
            acc[b][0] = bldf4<SC1>(rc, g * 16 + b * 64, (D + j * D + nbs * 16) * 4);
            acc[b][1] = bldf4<SC1>(rc, g * 16 + b * 64, (D + j * D + nbs * 16 + D + F) * 4);
        }
    }
    __builtin_amdgcn_sched_barrier(0);
    unsigned fw4 = tag;
    if (w == 10 && lane < 18) fw4 = __hip_atomic_load((gu32*)(ws + WS_FW) + (3 + 2 * j) * 18 + lane, RLX_AGENT);
    if (w == 11) wait_panels(ws, WS_FX, panel, WS_FX, panel, WS_FX, panel, tag);
    BAR();
    u32x2 pv[36];
    if (w == 11) {
        const rsrc_t rp = mk_rsrc(ws + WS_PST + (size_t)panel * 36 * 512, 36 * 512);
#pragma unroll
        for (int k = 0; k < 36; ++k) pv[k] = bldu2<SC1>(rp, lane * 8, k * 512);
    }
    {
        const rsrc_t rx = mk_rsrc(ws + WS_X1 + (size_t)tok0 * D * 2, 64 * D * 2);
        const int vo = (lane >> 3) * 1152 + (((lane & 7) ^ ((lane >> 3) & 6)) * 16);
#pragma unroll
        for (int k = 0; k < 6; ++k) {
            const int idx = 6 * w + k;
            blds16<SC1>(rx, lds + idx * 1024, vo, (idx & 7) * 9216 + (idx >> 3) * 128);
        }
    }
    __builtin_amdgcn_sched_barrier(0);
    LAS f32x4* st = (LAS f32x4*)(lds + KE_ST);
    LAS unsigned* flg = (LAS unsigned*)(lds + KE_FLG);
    if (w == 11) {
        float a1[4] = {}, a2[4] = {};
#pragma unroll
        for (int k = 0; k < 36; ++k) { a1[k & 3] += __uint_as_float(pv[k][0]); a2[k & 3] += __uint_as_float(pv[k][1]); }
        const float s1 = (a1[0] + a1[1]) + (a1[2] + a1[3]), s2 = (a2[0] + a2[1]) + (a2[2] + a2[3]);
        const float mean = s1 * (1.0f / D), var = fmaxf(s2 * (1.0f / D) - mean * mean, 0.f);
        const float mb = rbf(mean), sb = ln_sd(var), al = __builtin_amdgcn_rcpf(sb);
        const float k0 = -2.0f * 0.7978845608028654f * 1.4426950408889634f;
        st[lane] = (f32x4){k0 * al, k0 * 0.044715f * al * al * al, sb, -mb};
        if (lane < 3) flg[lane] = 0u;
    }
    if (w == 10) { const bool ok4 = __all(fw4 == tag); if (lane == 0) flg[3] = ok4 ? 1u : 0u; }
    VM0(); LGKM0(); BAR();
    const int z = i16 & 6;
    const int o0 = i16 * 128 + (g ^ z) * 16, o1 = i16 * 128 + ((4 + g) ^ z) * 16;
    {
        float nm[4];
#pragma unroll
        for (int mt = 0; mt < 4; ++mt) nm[mt] = st[mt * 16 + i16][3];
#pragma unroll
        for (int b = 0; b < NB; ++b) {
            const f32x4 cs = acc[b][0] + acc[b][1];
#pragma unroll
            for (int mt = 0; mt < 4; ++mt) acc[b][mt] = cs * nm[mt];
        }
    }
#define E_SPIN(cond) do { while (cond) __builtin_amdgcn_s_sleep(1); __builtin_amdgcn_sched_barrier(0); asm volatile("" ::: "memory"); } while (0)
#define E_FLG(i) __hip_atomic_load(flg + (i), __ATOMIC_RELAXED, __HIP_MEMORY_SCOPE_WORKGROUP)
    bf16x8 A[2][4];
#pragma unroll
    for (int ph = 0; ph < 2; ++ph) {
        const LAS char* ab = lds + ph * KE_HID;
        if (ph == 1) E_SPIN(E_FLG(0) < 4u);
#pragma unroll
        for (int mt = 0; mt < 4; ++mt) A[0][mt] = *(const LAS bf16x8*)(ab + o0 + mt * 2048);
#pragma unroll
        for (int s = 0; s < 18; ++s) {
            if (ph == 1 && s + 1 == S1) E_SPIN(E_FLG(1) < 4u);
            if (ph == 1 && s + 1 == S2) E_SPIN(E_FLG(2) < 4u);
            if (ph == 0 && s == 2 && w == 8 && E_FLG(3) == 0u) {
                wait_flags(ws, WS_FW, (3 + 2 * j) * 18, 18, WS_FW, 0, 0, tag);
                if (lane == 0) __hip_atomic_store(flg + 3, 1u, __ATOMIC_RELAXED, __HIP_MEMORY_SCOPE_WORKGROUP);
            }
            if (ph == 0 && s == 18 - RING) E_SPIN(E_FLG(3) == 0u);
            if (s + 1 < 18) {
#pragma unroll
                for (int mt = 0; mt < 4; ++mt) A[(s + 1) & 1][mt] = *(const LAS bf16x8*)(ab + (((s + 1) & 1) ? o1 : o0) + mt * 2048 + ((s + 1) >> 1) * 8192);
            }
            const int t = ph * 18 + s;
#pragma unroll
            for (int b = 0; b < NB; ++b) {
#pragma unroll
                for (int mt = 0; mt < 4; ++mt) acc[b][mt] = MFMA16(W[t % RING][b], A[s & 1][mt], acc[b][mt]);
                if (t + RING < 36) W[t % RING][b] = bld16<SC1>(rw, wv + b * 1024, wsb + (t + RING) * 36864);
            }
            __builtin_amdgcn_sched_barrier(0);
        }
        if (ph == 0) {
#pragma unroll
            for (int mt = 0; mt < 4; ++mt) {
                const f32x4 k = st[mt * 16 + i16];
#pragma unroll
                for (int b = 0; b < NB; ++b) {
                    float o[4];
#pragma unroll
                    for (int e = 0; e < 4; ++e) {
                        const float a = acc[b][mt][e];
                        const float ex = __builtin_amdgcn_exp2f(a * (k[0] + k[1] * (a * a)));
                        o[e] = a * __builtin_amdgcn_rcpf(k[2] + k[2] * ex);
                    }
                    const int nb = nbs + b;
                    *(LAS u32x2*)(lds + KE_HID + (nb >> 2) * 8192 + (mt * 16 + i16) * 128 + (((2 * (nb & 3) + (g >> 1)) ^ z) * 16) + 8 * (g & 1)) =
                        (u32x2){pk_bf16(o[0], o[1]), pk_bf16(o[2], o[3])};
                    acc[b][mt] = (f32x4){0.f, 0.f, 0.f, 0.f};
                }
            }
            LGKM0();
            if (lane == 0) __hip_atomic_fetch_add(flg + (w >> 2), 1u, __ATOMIC_RELAXED, __HIP_MEMORY_SCOPE_WORKGROUP);
            if (w >= 8) __builtin_amdgcn_s_setprio(2); else if (w >= 4) __builtin_amdgcn_s_setprio(1);
        }
    }
#undef E_SPIN
#undef E_FLG
    __builtin_amdgcn_s_setprio(0);
    const rsrc_t rp = mk_rsrc(ws + WS_PART + (size_t)(panel * 8 + j) * 72 * 1024, 72 * 1024);
#pragma unroll
    for (int b = 0; b < NB; ++b)
#pragma unroll
        for (int m2 = 0; m2 < 2; ++m2) {
            const f32x4 a0 = acc[b][2 * m2], a1 = acc[b][2 * m2 + 1];
            const u32x4 v = {pk_bf16(a0[0], a0[1]), pk_bf16(a0[2], a0[3]), pk_bf16(a1[0], a1[1]), pk_bf16(a1[2], a1[3])};
            const int pr = (nbs + b) * 2 + m2;
            if (pr >= 9 * j && pr < 9 * j + 9) *(LAS u32x4*)(lds + (pr - 9 * j) * 1024 + lane * 16) = v;
            else bst16(v, rp, lane * 16, pr * 1024);
        }
    LGKM0();
    VM0(); BAR();
    publish(ws, WS_FP, panel * 32 + j, tag);
}

constexpr int KF_HT = 16384;
__device__ __forceinline__ void phase_f(char* __restrict__ ws, LAS char* lds, u16* __restrict__ out, int panel, int jj, unsigned tag) {
    const int tid = threadIdx.x, w = __builtin_amdgcn_readfirstlane(tid >> 6), lane = tid & 63;
    const int g = lane >> 4, i16 = lane & 15, q = i16 >> 2, p = lane & 3;
    const int pr = jj * 9 + (w < 9 ? w : 0), nb = pr >> 1, m2 = pr & 1, nbs = (jj * 9) >> 1;
    u32x2 xa[2];
    const int so = ((panel * 64 + m2 * 32) * D + nb * 16) * 2, vo = (i16 * D + 4 * g) * 2;
    if (w < 10) {
        const rsrc_t rh = mk_rsrc(ws + WS_HT, D * L * 2);
        const int fl = ((lane >> 4) & 1) | ((w & 1) << 1);
        blds16<SC1>(rh, lds + KF_HT + w * 1024, (lane >> 3) * 4096 + (((lane & 7) ^ (fl << 1)) * 16), (nbs * 16 + w * 8) * 4096 + panel * 128);
    }
    if (w < 9) {
        const rsrc_t r1 = mk_rsrc(ws + WS_X1, L * D * 2);
#pragma unroll
        for (int t = 0; t < 2; ++t) xa[t] = bldu2<SC1>(r1, vo, so + t * 16 * D * 2);
    }
    if (w == 11) wait_panels(ws, WS_FP, panel, WS_FP, panel, WS_FP, panel, tag);
    VM0(); BAR();
    if (w < 9) {
        const rsrc_t rp = mk_rsrc(ws + WS_PART + (size_t)(panel * 8) * 72 * 1024, 8 * 72 * 1024);
        u32x4 v[8];
#pragma unroll
        for (int k = 0; k < 7; ++k) v[k] = bldu4<SC1>(rp, lane * 16, ((k < jj ? k : k + 1) * 72 + pr) * 1024);
        v[7] = *(const LAS u32x4*)(lds + w * 1024 + lane * 16);
        const int hrow = 16 * (nb - nbs) + 4 * g + q;
        u32x2 ha[2];
#pragma unroll
        for (int t = 0; t < 2; ++t)
            ha[t] = __builtin_bit_cast(u32x2, tr_read(lds + KF_HT + hrow * 128 + (((2 * (2 * m2 + t) + (p >> 1)) ^ (fsw(hrow) << 1)) * 16) + 8 * (p & 1)));
        float s[8] = {};
#pragma unroll
        for (int k = 0; k < 8; ++k)
#pragma unroll
            for (int e = 0; e < 4; ++e) { s[2 * e] += bf_lo(v[k][e]); s[2 * e + 1] += bf_hi(v[k][e]); }
#pragma unroll
        for (int t = 0; t < 2; ++t) {
            float o[4];
#pragma unroll
            for (int k = 0; k < 4; ++k) {
                const float x1 = (k & 1) ? bf_hi(xa[t][k >> 1]) : bf_lo(xa[t][k >> 1]);
                const float h = (k & 1) ? bf_hi(ha[t][k >> 1]) : bf_lo(ha[t][k >> 1]);
                o[k] = h + (x1 + s[4 * t + k]);
            }
            __builtin_amdgcn_raw_buffer_store_b64((u32x2){pk_bf16(o[0], o[1]), pk_bf16(o[2], o[3])}, mk_rsrc(out, L * D * 2), vo, so + t * 16 * D * 2, 0);
        }
    }
}

constexpr int LDS_CTRL = 3 * CV_CH > KE_FLG + 32 ? 3 * CV_CH : KE_FLG + 32;
constexpr int LDS_BYTES = LDS_CTRL + 64;

NOPK __global__ __launch_bounds__(NTHR) void mega_kernel(Args a) {
    extern __shared__ __attribute__((aligned(16))) char smem[];
    LAS char* lds = (LAS char*)smem;
    const int tid = threadIdx.x, w = __builtin_amdgcn_readfirstlane(tid >> 6), lane = tid & 63, b = blockIdx.x;
    unsigned cpf = 0;
    if (w == 8) {
        unsigned long long pc;
        asm volatile("s_getpc_b64 %0" : "=s"(pc));
        const int line = (b >> 3) * 13 + lane;
        if (lane < 13 && line * 128 < CODE_PF) cpf = *(const GAS unsigned*)((pc & ~255ull) + (unsigned long long)line * 128);
    }
    const unsigned tagv = __hip_atomic_load((gu32*)(a.ws + WS_EPOCH), RLX_AGENT);
    const StatRow sr = row_stats_load(a, w, lane);
    __builtin_amdgcn_sched_barrier(0);
    unsigned tag;
    if (b < 192) phase_conv(a, lds, tagv, sr, tag);
    else {
        tag = __builtin_amdgcn_readfirstlane(tagv) + 1u;
        if (tid == 0) __hip_atomic_store((gu32*)(a.ws + WS_FA) + b, tag, RLX_AGENT);
        if (tid == 0) *(LAS unsigned*)(lds + LDS_FWOK) = 0u;
        row_stats(a, sr, tag, w, lane);
        LGKM0(); BAR();
        const int i = w * 64 + (b - 192);
        if (i >= 36) {
            const unsigned long long t0 = __builtin_amdgcn_s_memrealtime();
            while (__builtin_amdgcn_s_memrealtime() - t0 < 250ull + 0ull * (unsigned)w) __builtin_amdgcn_s_sleep(8);
        }
        if (i < 180) {
            const int job = i < 36 ? i : (2 + 2 * ((i - 36) / 18)) * 18 + (i - 36) % 18;
            PrepRegs P;
            prep_issue(a, job, lane, P);
            prep_finish<true, true>(a, job, lds + w * 8192, lane, tag, P);
        }
        BAR();
    }
    asm volatile("" :: "v"(cpf));
    const int je = 2 * (b & 3) + ((b >> 3) & 1);
    {
        const int panel = (b & 7) * 4 + (b >> 6), j = (b >> 3) & 7;
        const int m = (w - 8) * 128 + ((b & 7) | ((b >> 4) << 3));
        if ((j & 1) && (w == 8 || w == 9) && m < 144) phase_cd_background(a, lds, panel, (3 + 2 * (m / 18)) * 18 + m % 18, tag);
        else {
            phase_c(a.ws, lds, panel, j, tag);
            phase_d(a.ws, lds, panel, j, je, tag);
        }
    }
    phase_e(a.ws, lds, ((b >> 2) & 1) * 16 + (b >> 4), je, tag);
    phase_f(a.ws, lds, a.out, ((b >> 2) & 1) * 16 + (b >> 4), je, tag);
    if (b == 0 && w == 10) {
        wait_flags(a.ws, WS_FA, 0, 256, WS_FA, 0, 0, tag);
        if (lane == 0) __hip_atomic_store((gu32*)(a.ws + WS_EPOCH), tag, RLX_AGENT);
    }
}

extern "C" void kernel_launch(void* const* d_in, const int* in_sizes, int n_in,
                              void* d_out, int out_size, void* d_ws, size_t ws_size,
                              hipStream_t stream) {
    Args a;
    a.x = (const u16*)d_in[0];
    a.are = (const u16*)d_in[1]; a.aim = (const u16*)d_in[2];
    a.bre = (const u16*)d_in[3]; a.bim = (const u16*)d_in[4];
    a.cre = (const u16*)d_in[5]; a.cim = (const u16*)d_in[6];
    a.logdt = (const u16*)d_in[7]; a.dskip = (const u16*)d_in[8];
    a.w1 = (const u16*)d_in[10]; a.w2 = (const u16*)d_in[11];
    a.w3 = (const u16*)d_in[12]; a.w4 = (const u16*)d_in[13];
    a.ws = (char*)d_ws; a.out = (u16*)d_out;
    (void)hipFuncSetAttribute((const void*)mega_kernel, hipFuncAttributeMaxDynamicSharedMemorySize, LDS_BYTES);
    mega_kernel<<<NWG, NTHR, LDS_BYTES, stream>>>(a);
}
```

```cpp
#include <hip/hip_runtime.h>
#include <hip/hip_bf16.h>
#include <stdint.h>

typedef unsigned short u16;
typedef short bf16x8 __attribute__((ext_vector_type(8)));
typedef short s16x4 __attribute__((ext_vector_type(4)));
typedef float f32x2 __attribute__((ext_vector_type(2)));
typedef float f32x4 __attribute__((ext_vector_type(4)));
typedef float f32x16 __attribute__((ext_vector_type(16)));
typedef unsigned u32x2 __attribute__((ext_vector_type(2)));
typedef unsigned u32x4 __attribute__((ext_vector_type(4)));
typedef __bf16 bf16x2_t __attribute__((ext_vector_type(2)));
typedef _Float16 f16x2 __attribute__((ext_vector_type(2)));
typedef _Float16 f16x8 __attribute__((ext_vector_type(8)));
#define LAS __attribute__((address_space(3)))
#define GAS __attribute__((address_space(1)))
#if defined(__HIP_DEVICE_COMPILE__)
#define NOPK __attribute__((target("no-packed-fp32-ops")))
#else
#define NOPK
#endif

constexpr int L = 2048, D = 576, NS = 128, F = 4608;
constexpr int WSUB = D * D;
constexpr float LN_EPS = 1e-5f;
constexpr int NWG = 256, NTHR = 768;
constexpr int SC1 = 16;

constexpr size_t WS_EPOCH = 0;
constexpr int REP = 8;
constexpr size_t WS_FA = 256;
constexpr size_t WS_FW = WS_FA + 1024;
constexpr size_t WS_FG = WS_FW + 2048;
constexpr size_t WS_FX = WS_FG + 4096;
constexpr size_t WS_FP = WS_FX + 4096;
constexpr size_t WS_FH = WS_FP + 4096;
constexpr size_t WS_GR = 32768;
constexpr size_t WS_HT = WS_GR + (size_t)L * 8;
constexpr size_t WS_WF = WS_HT + (size_t)D * L * 2;
constexpr size_t WS_CS = WS_WF + (size_t)18 * WSUB * 2;
constexpr size_t WS_ST2 = WS_CS + (size_t)2 * (D + F) * 4;
constexpr size_t WS_G = WS_ST2 + (size_t)L * 8;
constexpr size_t WS_X1 = WS_G + (size_t)(L + 4) * D * 2;
constexpr size_t WS_PST = WS_X1 + (size_t)L * D * 2;
constexpr size_t WS_PART = WS_PST + (size_t)32 * 36 * 64 * 8;
constexpr size_t WS_END = WS_PART + (size_t)32 * 8 * 72 * 512 * 2;

__device__ __forceinline__ unsigned pk_bf16(float lo, float hi) {
    f32x2 v = {lo, hi};
    return __builtin_bit_cast(unsigned, __builtin_convertvector(v, bf16x2_t));
}
__device__ __forceinline__ float bf_lo(unsigned p) { return __uint_as_float(p << 16); }
__device__ __forceinline__ float bf_hi(unsigned p) { return __uint_as_float(p & 0xffff0000u); }
__device__ __forceinline__ float bf2f(u16 x) { return __uint_as_float((unsigned)x << 16); }
__device__ __forceinline__ float rbf(float x) { return bf_lo(pk_bf16(x, x)); }
__device__ __forceinline__ float dot2bf(unsigned a, unsigned b, float c) {
    return __builtin_amdgcn_fdot2_f32_bf16(__builtin_bit_cast(bf16x2_t, a), __builtin_bit_cast(bf16x2_t, b), c, false);
}
__device__ __forceinline__ float gelu_tanh(float x) {
    const float c0 = -2.0f * 0.7978845608028654f * 1.4426950408889634f;
    const float c1 = c0 * 0.044715f;
    float e = __builtin_amdgcn_exp2f(x * (c0 + c1 * x * x));
    return x * __builtin_amdgcn_rcpf(1.0f + e);
}
__device__ __forceinline__ s16x4 tr_read(const LAS char* p) {
    return __builtin_amdgcn_ds_read_tr16_b64_v4i16((LAS s16x4*)p);
}
__device__ __forceinline__ bf16x8 cat8(s16x4 a, s16x4 b) {
    return __builtin_shufflevector(a, b, 0, 1, 2, 3, 4, 5, 6, 7);
}
__device__ __forceinline__ f32x4 sel4(bool c, f32x4 a, f32x4 b) {
    f32x4 r;
#pragma unroll
    for (int k = 0; k < 4; ++k) { float x = a[k], y = b[k]; asm volatile("" : "+v"(x), "+v"(y)); r[k] = c ? x : y; }
    return r;
}
__device__ __forceinline__ float rsum_g(float x) {
    const unsigned u = __float_as_uint(x);
    const auto a = __builtin_amdgcn_permlane16_swap(u, u, false, false);
    const float y = __uint_as_float(a[0]) + __uint_as_float(a[1]);
    const unsigned v = __float_as_uint(y);
    const auto b = __builtin_amdgcn_permlane32_swap(v, v, false, false);
    return __uint_as_float(b[0]) + __uint_as_float(b[1]);
}
template <int CTRL> __device__ __forceinline__ float dpp_f(float x) {
    return __uint_as_float(__builtin_amdgcn_update_dpp(0u, __float_as_uint(x), CTRL, 0xf, 0xf, false));
}
__device__ __forceinline__ float rsum_64(float x) {
    x += dpp_f<0xB1>(x);
    x += dpp_f<0x4E>(x);
    x += dpp_f<0x141>(x);
    x += dpp_f<0x140>(x);
    return rsum_g(x);
}
template <int CTRL> __device__ __forceinline__ unsigned dpp_u(unsigned x) { return __builtin_amdgcn_update_dpp(0u, x, CTRL, 0xf, 0xf, false); }
__device__ __forceinline__ unsigned umax2(unsigned a, unsigned b) { return a > b ? a : b; }
__device__ __forceinline__ float rmax_64(float xf) {
    unsigned x = __float_as_uint(xf);
    x = umax2(x, dpp_u<0xB1>(x)); x = umax2(x, dpp_u<0x4E>(x)); x = umax2(x, dpp_u<0x141>(x)); x = umax2(x, dpp_u<0x140>(x));
    const auto a = __builtin_amdgcn_permlane16_swap(x, x, false, false); x = umax2(a[0], a[1]);
    const auto b = __builtin_amdgcn_permlane32_swap(x, x, false, false); x = umax2(b[0], b[1]);
    return __uint_as_float(x);
}
__device__ __forceinline__ float ln_sd(float var) { return rbf(__builtin_amdgcn_sqrtf(rbf(rbf(var) + LN_EPS))); }
__device__ __forceinline__ int fsw(int row) { return ((row >> 1) & 1) | (((row >> 3) & 1) << 1); }
#define MFMA16(a, b, c) __builtin_amdgcn_mfma_f32_16x16x32_bf16((a), (b), (c), 0, 0, 0)
#define MFMA32(a, b, c) __builtin_amdgcn_mfma_f32_32x32x16_bf16((a), (b), (c), 0, 0, 0)
#define VM0() asm volatile("s_waitcnt vmcnt(0)" ::: "memory")
#define LGKM0() asm volatile("s_waitcnt lgkmcnt(0)" ::: "memory")
#define BAR() do { asm volatile("" ::: "memory"); __builtin_amdgcn_s_barrier(); asm volatile("" ::: "memory"); } while (0)

typedef __amdgpu_buffer_rsrc_t rsrc_t;
__device__ __forceinline__ rsrc_t mk_rsrc(const void* p, unsigned bytes) { return __builtin_amdgcn_make_buffer_rsrc((void*)p, 0, bytes, 0x00020000); }
template <int AUX> __device__ __forceinline__ bf16x8 bld16(rsrc_t r, int voff, int soff) {
    return __builtin_bit_cast(bf16x8, __builtin_amdgcn_raw_buffer_load_b128(r, voff, soff, AUX));
}
template <int AUX> __device__ __forceinline__ f32x4 bldf4(rsrc_t r, int voff, int soff) {
    return __builtin_bit_cast(f32x4, __builtin_amdgcn_raw_buffer_load_b128(r, voff, soff, AUX));
}
template <int AUX> __device__ __forceinline__ u32x4 bldu4(rsrc_t r, int voff, int soff) {
    return __builtin_amdgcn_raw_buffer_load_b128(r, voff, soff, AUX);
}
template <int AUX> __device__ __forceinline__ u32x2 bldu2(rsrc_t r, int voff, int soff) {
    return __builtin_amdgcn_raw_buffer_load_b64(r, voff, soff, AUX);
}
template <int AUX> __device__ __forceinline__ void blds16(rsrc_t r, LAS char* l, int voff, int soff) {
    __builtin_amdgcn_raw_ptr_buffer_load_lds(r, (LAS void*)l, 16, voff, soff, 0, AUX);
}
__device__ __forceinline__ void bst16(u32x4 v, rsrc_t r, int voff, int soff) { __builtin_amdgcn_raw_buffer_store_b128(v, r, voff, soff, SC1); }
__device__ __forceinline__ void bst8(u32x2 v, rsrc_t r, int voff, int soff) { __builtin_amdgcn_raw_buffer_store_b64(v, r, voff, soff, SC1); }
__device__ __forceinline__ void bst4(unsigned v, rsrc_t r, int voff, int soff) { __builtin_amdgcn_raw_buffer_store_b32(v, r, voff, soff, SC1); }

struct Args {
    const u16* x; const u16 *are, *aim, *bre, *bim, *cre, *cim, *logdt, *dskip;
    const u16 *w1, *w2, *w3, *w4;
    char* ws; u16* out;
};

typedef GAS unsigned gu32;
typedef GAS unsigned long long gu64;
#define RLX_AGENT __ATOMIC_RELAXED, __HIP_MEMORY_SCOPE_AGENT
constexpr unsigned long long PREP_DELAY = 320ull;
constexpr int CODE_PF = 41984;
constexpr unsigned long long WAIT_TICKS = 400000ull;
__device__ __forceinline__ void publish(char* ws, size_t flag_off, int idx, unsigned tag) {
    if (threadIdx.x == 0) __hip_atomic_store((gu32*)(ws + flag_off) + idx, tag, RLX_AGENT);
}
__device__ __forceinline__ void publish_rep(char* ws, size_t flag_off, int idx, unsigned tag, int lane) {
    if (lane < REP) __hip_atomic_store((gu32*)(ws + flag_off) + lane * 256 + idx, tag, RLX_AGENT);
}
__device__ __forceinline__ void wait_flags(char* ws, size_t offa, int a0, int na, size_t offb, int b0, int nb, unsigned tag) {
    const int lane = threadIdx.x & 63;
    const gu32* p[4]; bool on[4];
#pragma unroll
    for (int k = 0; k < 4; ++k) {
        const int i = lane + 64 * k;
        on[k] = i < na + nb;
        p[k] = i < na ? (const gu32*)(ws + offa) + a0 + i : (const gu32*)(ws + offb) + b0 + (on[k] ? i - na : 0);
    }
    const unsigned long long t0 = __builtin_amdgcn_s_memrealtime();
    for (;;) {
        bool ok = true;
#pragma unroll
        for (int k = 0; k < 4; ++k)
            if (64 * k < na + nb) { const unsigned v = __hip_atomic_load(p[k], RLX_AGENT); ok = ok && (!on[k] || v == tag); }
        if (__all(ok)) break;
        if (__builtin_amdgcn_s_memrealtime() - t0 > WAIT_TICKS) break;
        __builtin_amdgcn_s_sleep(2);
    }
}
__device__ __forceinline__ void wait_panels(char* ws, size_t o0, int p0, size_t o1, int p1, size_t o2, int p2, unsigned tag) {
    const int lane = threadIdx.x & 63, k = (lane >> 3) % 3;
    const gu32* p = (const gu32*)(ws + (k == 0 ? o0 : k == 1 ? o1 : o2)) + (k == 0 ? p0 : k == 1 ? p1 : p2) * 32 + (lane & 7);
    const unsigned long long t0 = __builtin_amdgcn_s_memrealtime();
    for (;;) {
        if (__all(__hip_atomic_load(p, RLX_AGENT) == tag)) break;
        if (__builtin_amdgcn_s_memrealtime() - t0 > WAIT_TICKS) break;
        __builtin_amdgcn_s_sleep(2);
    }
}
struct StatRow { u32x4 v0, v1; };
__device__ __forceinline__ StatRow row_stats_load(const Args& a, int w, int lane) {
    const int row = blockIdx.x * 8 + (w & 7);
    const rsrc_t rx = mk_rsrc(a.x + (size_t)row * D, w < 8 ? D * 2 : 0);
    StatRow r;
    r.v0 = bldu4<0>(rx, lane * 16, 0); r.v1 = bldu4<0>(rx, 1024 + lane * 16, 0);
    return r;
}
__device__ __forceinline__ void row_stats(const Args& a, const StatRow& r, unsigned tag, int w, int lane) {
    if (w < 8) {
        float s1 = 0.f, s2 = 0.f;
#pragma unroll
        for (int k = 0; k < 4; ++k) {
            s1 = dot2bf(r.v0[k], 0x3f803f80u, s1); s2 = dot2bf(r.v0[k], r.v0[k], s2);
            s1 = dot2bf(r.v1[k], 0x3f803f80u, s1); s2 = dot2bf(r.v1[k], r.v1[k], s2);
        }
        s1 = rsum_64(s1); s2 = rsum_64(s2);
        const float mean = s1 * (1.0f / D), var = fmaxf(s2 * (1.0f / D) - mean * mean, 0.f);
        const unsigned val = (pk_bf16(mean, ln_sd(var)));
        if (lane == 0) __hip_atomic_store((gu64*)(a.ws + WS_GR) + blockIdx.x * 8 + w, ((unsigned long long)tag << 32) | val, RLX_AGENT);
    }
}

struct PrepRegs { u32x4 R[36]; };
__device__ __forceinline__ void prep_issue(const Args& a, int job, int lane, PrepRegs& P) {
    const int sub = job / 18, rem = job % 18, kh = rem / 9, ns = rem % 9;
    const u16* src; int ld;
    if (sub == 0) { src = a.w1; ld = D; }
    else if (sub == 1) { src = a.w2; ld = D; }
    else if ((sub & 1) == 0) { src = a.w3 + ((sub - 2) >> 1) * D; ld = F; }
    else { src = a.w4 + (size_t)((sub - 2) >> 1) * D * D; ld = D; }
    src += (size_t)(kh * 288) * ld + ns * 64;
    const rsrc_t rs = mk_rsrc(src, 288 * ld * 2);
    const int ldb = ld * 2;
    const int vo = (lane >> 3) * ldb + (lane & 7) * 16;
#pragma unroll
    for (int i = 0; i < 36; ++i) P.R[i] = bldu4<0>(rs, vo, i * 8 * ldb);
    __builtin_amdgcn_sched_barrier(0);
}
template <bool PUBLISH, bool COLSUM>
__device__ __forceinline__ void prep_finish(const Args& a, int job, LAS char* lds, int lane, unsigned tag, const PrepRegs& P) {
    const int sub = job / 18, rem = job % 18, kh = rem / 9, ns = rem % 9;
    const rsrc_t rd = mk_rsrc(a.ws + WS_WF + (size_t)sub * WSUB * 2, WSUB * 2);
    const int fl = (lane >> 4) & 1;
    const int w0 = (lane >> 3) * 128 + (((lane & 7) ^ (fl << 1)) * 16), w1 = (lane >> 3) * 128 + (((lane & 7) ^ ((fl | 2) << 1)) * 16);
    const int g = lane >> 4, i16 = lane & 15, q = i16 >> 2, p = lane & 3;
    const int f = ((q >> 1) & 1) | ((g & 1) << 1);
    LAS char* rb[4];
#pragma unroll
    for (int n = 0; n < 4; ++n) rb[n] = lds + ((8 * g + q) * 128 + (((2 * n + (p >> 1)) ^ (f << 1)) * 16) + 8 * (p & 1));
    LAS char* const wp0 = lds + w0;
    LAS char* const wp1 = lds + w1;
    const bf16x8 ones = {0x3f80, 0x3f80, 0x3f80, 0x3f80, 0x3f80, 0x3f80, 0x3f80, 0x3f80};
    f32x4 cs[4] = {};
#pragma unroll
    for (int u = 0; u < 9; ++u) {
        const int so = (u & 1) * 4096;
#pragma unroll
        for (int i = 0; i < 4; ++i) *(LAS u32x4*)(((i & 1) ? wp1 : wp0) + (so + i * 1024)) = P.R[4 * u + i];
#pragma unroll
        for (int n = 0; n < 4; ++n) {
            const bf16x8 x = cat8(tr_read(rb[n] + so), tr_read(rb[n] + (so + 512)));
            bst16(__builtin_bit_cast(u32x4, x), rd, lane * 16, ((kh * 9 + u) * 36 + ns * 4 + n) * 1024);
            if (COLSUM) cs[n] = MFMA16(ones, x, cs[n]);
        }
    }
    if (COLSUM && (sub & 1) == 0) {
        const rsrc_t rc = mk_rsrc(a.ws + WS_CS, 2 * (D + F) * 4);
#pragma unroll
        for (int n = 0; n < 4; ++n)
            if (lane < 16) bst4(__float_as_uint(cs[n][0]), rc, lane * 4, (kh * (D + F) + (sub == 0 ? 0 : D + ((sub - 2) >> 1) * D) + ns * 64 + n * 16) * 4);
    }
    if (PUBLISH) {
        VM0();
        if (lane == 0) __hip_atomic_store((gu32*)(a.ws + WS_FW) + job, tag, RLX_AGENT);
    }
}

constexpr int CV_RC = 4160;
constexpr int CV_U = 4 * CV_RC;
constexpr int CV_X = CV_U + 2288 * 2;
constexpr int CV_RED = CV_X + 4096;
constexpr int CV_CH = CV_RED + 24576;
constexpr int KG_ROW = 544;
constexpr int KG_XI = 32 * KG_ROW, KG_T = CV_RED + 4096;

struct cpx { float re, im; };
__device__ __forceinline__ cpx cmul(cpx a, cpx b) { return {a.re * b.re - a.im * b.im, a.re * b.im + a.im * b.re}; }
__device__ __forceinline__ cpx cexp_rev(float a2, float rev, float m) {
    const float e = __builtin_amdgcn_exp2f(a2 * m), p = __builtin_amdgcn_fractf(rev * m);
    return {e * __builtin_amdgcn_cosf(p), e * __builtin_amdgcn_sinf(p)};
}
__device__ __forceinline__ unsigned pk_f16(cpx c) { f32x2 v = {c.re, c.im}; return __builtin_bit_cast(unsigned, __builtin_convertvector(v, f16x2)); }

__device__ __forceinline__ void conv_put(LAS char* lds, int m, u32x4 A, u32x2 B) {
    u32x4 c1 = {__builtin_amdgcn_alignbit(A[1], A[0], 16), __builtin_amdgcn_alignbit(A[2], A[1], 16), __builtin_amdgcn_alignbit(A[3], A[2], 16), __builtin_amdgcn_alignbit(B[0], A[3], 16)};
    u32x4 c2 = {A[1], A[2], A[3], B[0]};
    u32x4 c3 = {c1[1], c1[2], c1[3], __builtin_amdgcn_alignbit(B[1], B[0], 16)};
    *(LAS u32x4*)(lds + 0 * CV_RC + m * 16) = A;
    *(LAS u32x4*)(lds + 1 * CV_RC + m * 16) = c1;
    *(LAS u32x4*)(lds + 2 * CV_RC + m * 16) = c2;
    *(LAS u32x4*)(lds + 3 * CV_RC + m * 16) = c3;
}

constexpr int LDS_FWOK = 3 * CV_CH;
__device__ __forceinline__ void phase_conv(const Args& a, LAS char* lds0, unsigned tagv, const StatRow& sr, unsigned& tag_out) {
    const int tid = threadIdx.x, w = __builtin_amdgcn_readfirstlane(tid >> 6), lane = tid & 63;
    const int b = blockIdx.x, chl = w >> 2, wq = w & 3, t8 = tid & 255, ch = 3 * b + chl;
    LAS char* lds = lds0 + chl * CV_CH;
    const int part = wq >> 1, n = (wq & 1) * 64 + lane;
    const int o = ch * NS + n, o2 = ch * NS + (n ^ 64);
    const u16 r_ld = a.logdt[ch], r_ar = a.are[o], r_ai = a.aim[o], r_ar2 = a.are[o2];
    u16 r_b[4] = {}, r_c[4] = {};
    if (part) { r_b[0] = a.bre[o]; r_b[1] = a.bim[o]; r_c[0] = a.cre[o]; r_c[1] = a.cim[o]; r_b[2] = a.bre[o2]; r_b[3] = a.bim[o2]; r_c[2] = a.cre[o2]; r_c[3] = a.cim[o2]; }
    const u16 r_ds = a.dskip[ch];
    __builtin_amdgcn_sched_barrier(0);
    const unsigned tag = __builtin_amdgcn_readfirstlane(tagv) + 1u;
    tag_out = tag;
    if (tid == 0) __hip_atomic_store((gu32*)(a.ws + WS_FA) + b, tag, RLX_AGENT);
    row_stats(a, sr, tag, w, lane);
    __builtin_amdgcn_sched_barrier(0);
    u32x2 xr[4];
    const rsrc_t rx = mk_rsrc(a.x, L * D * 2);
    const int xvo = tid * (2 * D * 2) + ((6 * b) & ~3);
#define GATHER(i) do { __builtin_amdgcn_sched_barrier(0); if ((i) < 2 || w < 4) xr[i] = bldu2<0>(rx, xvo + ((i) & 1) * D * 2, ((i) >> 1) * 1536 * D * 2); __builtin_amdgcn_sched_barrier(0); } while (0)
    GATHER(0);
    const float dt = rbf(expf(bf2f(r_ld)));
    const float are = dt * bf2f(r_ar);
    const float amax = fmaxf(are, dt * bf2f(r_ar2));
    float ap = 0.f;
    if (__any(amax > 0.f)) ap = rmax_64(fmaxf(amax, 0.f));
    const float a2 = (are - ap) * 1.4426950408889634f, rev = dt * bf2f(r_ai) * 0.15915494309189535f;
    float kscale = 1.f;
    if (part == 0) {
        cpx e[4];
        const cpx g8 = cexp_rev(a2, rev, 8.f), g32 = cexp_rev(a2, rev, 32.f);
        e[0] = {1.f, 0.f}; e[1] = g8; e[2] = cmul(g8, g8); e[3] = cmul(e[2], g8);
        LAS char* er = lds + n * 4;
#pragma unroll
        for (int t = 0; t < 8; ++t) {
            if (t == 0) GATHER(1);
            if (t == 3) GATHER(2);
            if (t == 6) GATHER(3);
#pragma unroll
            for (int c = 0; c < 4; ++c) {
                *(LAS unsigned*)(er + (4 * t + c) * KG_ROW) = pk_f16(e[c]);
                if (t < 7) e[c] = (t == 0 && c == 0) ? g32 : cmul(e[c], g32);
            }
        }
    } else {
        const float b0 = dt * bf2f(r_b[0]), b1 = dt * bf2f(r_b[1]), c0 = bf2f(r_c[0]), c1 = bf2f(r_c[1]);
        const float b2 = dt * bf2f(r_b[2]), b3 = dt * bf2f(r_b[3]), c2 = bf2f(r_c[2]), c3 = bf2f(r_c[3]);
        const float wr = c0 * b0 - c1 * b1, wi = c0 * b1 + c1 * b0, vr = c2 * b2 - c3 * b3, vi = c2 * b3 + c3 * b2;
        const float wm = rmax_64(fmaxf(fmaxf(fabsf(wr), fabsf(wi)), fmaxf(fabsf(vr), fabsf(vi))));
        const unsigned ex = (__float_as_uint(wm) >> 23) & 0xffu;
        const unsigned exc = ex < 2u ? 2u : (ex > 252u ? 252u : ex);
        const float sc = __uint_as_float((254u - exc) << 23);
        kscale = __uint_as_float(exc << 23);
        const cpx W = {wr * sc, -wi * sc};
        const cpx g1 = cexp_rev(a2, -rev, 1.f), p256 = cexp_rev(a2, -rev, 256.f);
        const cpx g2 = cmul(g1, g1), g4 = cmul(g2, g2), p512 = cmul(p256, p256);
        LAS char* xr_ = lds + KG_XI + n * 4;
#define XST(i, v) *(LAS unsigned*)(xr_ + (i) * KG_ROW) = pk_f16(v)
        GATHER(1);
        cpx x = W; XST(0, x);
#pragma unroll
        for (int i = 1; i < 8; ++i) { x = cmul(x, g1); XST(i, x); }
        GATHER(2);
        x = cmul(W, p256); XST(8, x);
#pragma unroll
        for (int i = 9; i < 12; ++i) { x = cmul(x, g2); XST(i, x); }
        cpx y = cmul(W, p512); XST(12, y); XST(13, cmul(y, g4));
        x = cmul(y, p256); XST(14, x); XST(15, cmul(x, g4));
        GATHER(3);
        y = cmul(y, p512); XST(16, y);
#pragma unroll
        for (int i = 17; i < 21; ++i) { y = cmul(y, p256); XST(i, y); }
#undef XST
#undef GATHER
        if (lane == 0 && wq == 2) *(LAS float*)(lds + KG_T - 16) = kscale;
    }
    const int p = lane & 15, g = lane >> 4;
    LAS float* T = (LAS float*)(lds + KG_T);
    int base, idx0, istr;
    if (wq & 1) {
        base = p < 4 ? 1024 + 256 * p : (p == 4 ? 2048 : 0);
        idx0 = p < 4 ? 512 + 32 * p : (p == 4 ? 640 : 680);
        istr = 1;
    } else {
        const int q4 = p & 3;
        base = p < 8 ? p : (p < 12 ? 256 + 2 * q4 : 512 + 4 * (p & 1) + 128 * (p & 2));
        idx0 = p < 8 ? p : (p < 12 ? 256 + q4 : 384 + (p & 1) + 32 * (p & 2));
        istr = p < 8 ? 8 : (p < 12 ? 4 : 2);
    }
    const int j0 = (wq >> 1) * 16 + 4 * g;
    LAS float* tw = T + idx0 + j0 * istr;
    const LAS char* ea = lds + ((wq >> 1) * 16 + p) * KG_ROW + g * 16;
    const LAS char* xa = lds + KG_XI + ((wq & 1) * 16 + p) * KG_ROW + g * 16;
    LGKM0(); BAR();
    {
        f32x4 acc = {};
        f16x8 A[8], B[8];
#pragma unroll
        for (int t = 0; t < 8; ++t) { A[t] = *(const LAS f16x8*)(ea + t * 64); B[t] = *(const LAS f16x8*)(xa + t * 64); }
        float ks = *(const LAS float*)(lds + KG_T - 16);
        __builtin_amdgcn_sched_barrier(0);
#pragma unroll
        for (int t = 0; t < 8; ++t) acc = __builtin_amdgcn_mfma_f32_16x16x32_f16(A[t], B[t], acc, 0, 0, 0);
        if (ap > 0.f) {
#pragma unroll
            for (int q = 0; q < 4; ++q) acc[q] *= __builtin_amdgcn_exp2f(ap * 1.4426950408889634f * (float)(base + 8 * (j0 + q)));
        }
#pragma unroll
        for (int q = 0; q < 4; ++q) tw[q * istr] = acc[q] * ks;
    }
    LGKM0(); BAR();
    {
        float v[12];
        float lf = (float)(2047 - t8 * 8);
        asm volatile("" : "+v"(lf));
        const unsigned tb = (unsigned)(size_t)T - 17024u * 4u;
        if (wq < 3) {
#pragma unroll
            for (int e = 0; e < 12; e += 2) {
                const unsigned bits = pk_bf16(lf - (float)e, lf - (float)(e + 1));
                v[e] = *(const LAS float*)(size_t)(((bits & 0xffffu) << 2) + tb);
                v[e + 1] = *(const LAS float*)(size_t)(((bits >> 16) << 2) + tb);
            }
        } else {
#pragma unroll
            for (int e = 0; e < 12; ++e) {
                const int l = 2047 - (t8 * 8 + e);
                const int idx = l < 128 ? (l < 0 ? 0 : l) : (int)(pk_bf16((float)l, 0.f) & 0xffffu) - 17024;
                const float t = T[idx];
                v[e] = l < 0 ? 0.f : t;
            }
        }
        conv_put(lds, t8, (u32x4){pk_bf16(v[0], v[1]), pk_bf16(v[2], v[3]), pk_bf16(v[4], v[5]), pk_bf16(v[6], v[7])}, (u32x2){pk_bf16(v[8], v[9]), pk_bf16(v[10], v[11])});
        if (t8 < 4) conv_put(lds, 256 + t8, (u32x4){0u, 0u, 0u, 0u}, (u32x2){0u, 0u});
        if (t8 >= 64 && t8 < 94) *(LAS u32x4*)(lds + CV_U + (t8 - 64) * 16) = (u32x4){0u, 0u, 0u, 0u};
    }
    {
        u32x4 gv[2];
        const rsrc_t rgn = mk_rsrc(a.ws + WS_GR, L * 8);
        const unsigned long long t0 = __builtin_amdgcn_s_memrealtime();
        for (;;) {
            gv[0] = bldu4<SC1>(rgn, tid * 16, 0);
            gv[1] = w < 4 ? bldu4<SC1>(rgn, tid * 16, 1536 * 8) : gv[0];
            const bool ok = gv[0][1] == tag && gv[0][3] == tag && gv[1][1] == tag && gv[1][3] == tag;
            if (__all(ok)) break;
            if (__builtin_amdgcn_s_memrealtime() - t0 > WAIT_TICKS) break;
            __builtin_amdgcn_s_sleep(2);
            asm volatile("" ::: "memory");
        }
#pragma unroll
        for (int i = 0; i < 2; ++i) {
            if (i == 0 || w < 4) {
                const int row = 2 * tid + 1536 * i;
                u32x2 x0 = xr[2 * i], x1 = xr[2 * i + 1];
                if (b & 1) {
                    x0 = (u32x2){__builtin_amdgcn_alignbit(x0[1], x0[0], 16), x0[1] >> 16};
                    x1 = (u32x2){__builtin_amdgcn_alignbit(x1[1], x1[0], 16), x1[1] >> 16};
                }
                const float m0 = bf_lo(gv[i][0]), m1 = bf_lo(gv[i][2]);
                const float s0 = __builtin_amdgcn_rcpf(bf_hi(gv[i][0])), s1 = __builtin_amdgcn_rcpf(bf_hi(gv[i][2]));
#pragma unroll
                for (int c = 0; c < 3; ++c) {
                    const unsigned xp = c == 0 ? __builtin_amdgcn_perm(x1[0], x0[0], 0x05040100u) : c == 1 ? __builtin_amdgcn_perm(x1[0], x0[0], 0x07060302u)
                                                                                                            : __builtin_amdgcn_perm(x1[1], x0[1], 0x05040100u);
                    const unsigned dd = pk_bf16(bf_lo(xp) - m0, bf_hi(xp) - m1);
                    *(LAS unsigned*)(lds0 + c * CV_CH + CV_U + 480 + row * 2) = pk_bf16(bf_lo(dd) * s0, bf_hi(dd) * s1);
                    *(LAS unsigned*)(lds0 + c * CV_CH + CV_X + row * 2) = xp;
                }
            }
        }
    }
    LGKM0(); BAR();
    unsigned fwv = tag;
    if (w == 11 && lane < 36) fwv = __hip_atomic_load((gu32*)(a.ws + WS_FW) + lane, RLX_AGENT);
    f32x4 acc[8];
#pragma unroll
    for (int c = 0; c < 8; ++c) acc[c] = (f32x4){0.f, 0.f, 0.f, 0.f};
    const int cpy = (3 - p) & 3;
#pragma unroll
    for (int rr = 0; rr < 2; ++rr) {
        const int r = 2 * wq + rr;
        const LAS char* ab = lds + cpy * CV_RC + 2 * (2031 - 32 * r + 8 * g - p - cpy) - 512 * 7;
        const LAS char* fb = lds + CV_U + 2 * (240 - 32 * r + 16 * p - 16 + 8 * g);
        bf16x8 A[8], Fm[8];
#pragma unroll
        for (int s = 0; s < 8; ++s) {
            const s16x4 lo = *(const volatile LAS s16x4*)(ab + 512 * (7 - s));
            const s16x4 hi = *(const volatile LAS s16x4*)(ab + 512 * (7 - s) + 8);
            A[s] = cat8(lo, hi);
        }
#pragma unroll
        for (int e = 0; e < 8; ++e) Fm[e] = *(const LAS bf16x8*)(fb + 512 * e);
#pragma unroll
        for (int c = 0; c < 8; ++c)
#pragma unroll
            for (int s = 0; s <= c; ++s) acc[c] = MFMA16(A[s], Fm[c - s], acc[c]);
    }
    LAS f32x4* red = (LAS f32x4*)(lds + CV_RED) + lane;
    f32x4 own[2];
#define RED_CASE(WQ) { _Pragma("unroll") for (int c = 0; c < 8; ++c) if ((c >> 1) != WQ) red[((c >> 1) * 6 + (c & 1) * 3 + (WQ > (c >> 1) ? WQ - 1 : WQ)) * 64] = acc[c]; \
                       own[0] = acc[2 * WQ]; own[1] = acc[2 * WQ + 1]; }
    if (wq == 0) RED_CASE(0) else if (wq == 1) RED_CASE(1) else if (wq == 2) RED_CASE(2) else RED_CASE(3)
#undef RED_CASE
    if (w == 11) { const bool okw = __all(fwv == tag); if (lane == 0) *(LAS unsigned*)(lds0 + LDS_FWOK) = okw ? 1u : 0u; }
    LGKM0(); BAR();
    const rsrc_t rh = mk_rsrc(a.ws + WS_HT + (size_t)ch * L * 2, L * 2);
    const float dsk = bf2f(r_ds);
#pragma unroll
    for (int i = 0; i < 2; ++i) {
        f32x4 y = own[i];
#pragma unroll
        for (int k = 0; k < 3; ++k) y += red[(wq * 6 + i * 3 + k) * 64];
        const int l0 = 256 * (2 * wq + i) + 16 * p + 4 * g;
        const u32x2 uu = *(const LAS u32x2*)(lds + CV_U + 480 + 2 * l0), xv = *(const LAS u32x2*)(lds + CV_X + 2 * l0);
        float o[4];
#pragma unroll
        for (int k = 0; k < 4; ++k) {
            const float u = (k & 1) ? bf_hi(uu[k >> 1]) : bf_lo(uu[k >> 1]);
            const float x = (k & 1) ? bf_hi(xv[k >> 1]) : bf_lo(xv[k >> 1]);
            o[k] = x + __builtin_fmaf(u, dsk, y[k]);
        }
        bst8((u32x2){pk_bf16(o[0], o[1]), pk_bf16(o[2], o[3])}, rh, 2 * l0, 0);
    }
    VM0(); BAR();
    if (w == 0) publish_rep(a.ws, WS_FH, b, tag, lane);
}

__device__ __forceinline__ int nb0_of(int j) { return (9 * j + 1) >> 1; }

constexpr int OUT_ROW = 176;
constexpr int KC_XB = 73728, KC_ST = KC_XB + 20 * 1024, KC_OUT = KC_ST + 512;
__device__ __forceinline__ void phase_c(char* __restrict__ ws, LAS char* lds, int panel, int j, unsigned tag) {
    const int tid = threadIdx.x, w = __builtin_amdgcn_readfirstlane(tid >> 6), lane = tid & 63;
    const int tok0 = panel * 64, nbs = nb0_of(j), nblk = nb0_of(j + 1) - nbs;
    const int g = lane >> 4, i16 = lane & 15, q = i16 >> 2, p = lane & 3;
    const int b = w >> 1, kh = w & 1;
    const bool act = w < 10 && b < nblk;
    const int nb = nbs + (act ? b : 0);
    if (w == 0 && *(const LAS unsigned*)(lds + LDS_FWOK) == 0u) wait_flags(ws, WS_FW, 0, 36, WS_FW, 0, 0, tag);
    BAR();
    bf16x8 W[9];
    f32x4 cs0, cs1;
    if (w < 10) {
        const rsrc_t rw = mk_rsrc(ws + WS_WF, WSUB * 2);
#pragma unroll
        for (int i = 0; i < 9; ++i) W[i] = bld16<SC1>(rw, lane * 16, ((kh * 9 + i) * 36 + nb) * 1024);
        const rsrc_t rc = mk_rsrc(ws + WS_CS, 2 * (D + F) * 4);
        cs0 = bldf4<SC1>(rc, g * 16, nb * 64); cs1 = bldf4<SC1>(rc, g * 16, nb * 64 + (D + F) * 4);
    }
    if (w == 11) wait_flags(ws, WS_FH, (blockIdx.x & (REP - 1)) * 256, 192, WS_FH, 0, 0, tag);
    BAR();
    {
        const rsrc_t rh = mk_rsrc(ws + WS_HT, D * L * 2);
        const int fl = (lane >> 4) & 1;
        const int v0 = (lane >> 3) * 4096 + (((lane & 7) ^ (fl << 1)) * 16), v1 = (lane >> 3) * 4096 + (((lane & 7) ^ ((fl | 2) << 1)) * 16);
#pragma unroll
        for (int k = 0; k < 6; ++k) { const int idx = 6 * w + k; blds16<SC1>(rh, lds + idx * 1024, (k & 1) ? v1 : v0, idx * 32768 + tok0 * 2); }
    }
    VM0(); BAR();
    const int f = ((q >> 1) & 1) | ((g & 1) << 1);
    const int lo_ = (8 * g + q) * 128 + 8 * (p & 1);
    f32x4 acc[4] = {};
    LAS f32x2* st = (LAS f32x2*)(lds + KC_ST);
    LAS f32x4* xb = (LAS f32x4*)(lds + KC_XB);
    if (w < 10) {
        const LAS char* hb[4];
#pragma unroll
        for (int mt = 0; mt < 4; ++mt) hb[mt] = lds + kh * 9 * 4096 + lo_ + (((2 * mt + (p >> 1)) ^ (f << 1)) * 16);
        bf16x8 H[2][2][4];
#pragma unroll
        for (int c = 0; c < 6; ++c) {
            if (c < 5) {
#pragma unroll
                for (int i = 0; i < 2; ++i)
                    if (2 * c + i < 9) {
#pragma unroll
                        for (int mt = 0; mt < 4; ++mt) H[c & 1][i][mt] = cat8(tr_read(hb[mt] + (2 * c + i) * 4096), tr_read(hb[mt] + (2 * c + i) * 4096 + 512));
                    }
            }
            __builtin_amdgcn_sched_barrier(0);
            if (c > 0) {
#pragma unroll
                for (int i = 0; i < 2; ++i)
                    if (2 * (c - 1) + i < 9) {
#pragma unroll
                        for (int mt = 0; mt < 4; ++mt) acc[mt] = MFMA16(W[2 * (c - 1) + i], H[(c - 1) & 1][i][mt], acc[mt]);
                    }
            }
            __builtin_amdgcn_sched_barrier(0);
        }
        xb[((w * 2 + 0) * 64) + lane] = sel4(kh, acc[0], acc[2]);
        xb[((w * 2 + 1) * 64) + lane] = sel4(kh, acc[1], acc[3]);
    } else {
        __builtin_amdgcn_s_setprio(3);
        const bf16x8 ones = {0x3f80, 0x3f80, 0x3f80, 0x3f80, 0x3f80, 0x3f80, 0x3f80, 0x3f80};
        const int m0 = 2 * (w - 10);
        const LAS char* h0 = lds + lo_ + (((2 * m0 + (p >> 1)) ^ (f << 1)) * 16);
        const LAS char* h1 = lds + lo_ + (((2 * m0 + 2 + (p >> 1)) ^ (f << 1)) * 16);
        bf16x8 H[2][3][2];
#pragma unroll
        for (int c = 0; c < 7; ++c) {
            if (c < 6) {
#pragma unroll
                for (int i = 0; i < 3; ++i) {
                    H[c & 1][i][0] = cat8(tr_read(h0 + (3 * c + i) * 4096), tr_read(h0 + (3 * c + i) * 4096 + 512));
                    H[c & 1][i][1] = cat8(tr_read(h1 + (3 * c + i) * 4096), tr_read(h1 + (3 * c + i) * 4096 + 512));
                }
            }
            __builtin_amdgcn_sched_barrier(0);
            if (c > 0) {
#pragma unroll
                for (int i = 0; i < 3; ++i) {
                    const bf16x8 H0 = H[(c - 1) & 1][i][0], H1 = H[(c - 1) & 1][i][1];
                    acc[0] = MFMA16(ones, H0, acc[0]); acc[1] = MFMA16(H0, H0, acc[1]);
                    acc[2] = MFMA16(ones, H1, acc[2]); acc[3] = MFMA16(H1, H1, acc[3]);
                }
            }
            __builtin_amdgcn_sched_barrier(0);
        }
        const float sm[2] = {acc[0][0], acc[2][0]};
        if (g == q) {
            const rsrc_t r2 = mk_rsrc(ws + WS_ST2, L * 8);
#pragma unroll
            for (int t = 0; t < 2; ++t) {
                const f32x4 d = acc[2 * t + 1];
                const float s2 = p == 0 ? d[0] : p == 1 ? d[1] : p == 2 ? d[2] : d[3];
                const float mean = sm[t] * (1.0f / D), var = fmaxf(s2 * (1.0f / D) - mean * mean, 0.f);
                const float mb = rbf(mean), rs = __builtin_amdgcn_rcpf(ln_sd(var));
                const float m2 = rbf((mean - mb) * rs), rs2 = __builtin_amdgcn_rcpf(ln_sd(var * rs * rs));
                st[(m0 + t) * 16 + i16] = (f32x2){rs * rs2, (mb * rs + m2) * rs2};
                if (j == 0) bst8((u32x2){__float_as_uint(mb), __float_as_uint(rs)}, r2, (tok0 + (m0 + t) * 16 + i16) * 8, 0);
            }
        }
    }
    __builtin_amdgcn_s_setprio(0);
    LGKM0(); BAR();
    const rsrc_t rg = mk_rsrc(ws + WS_G, (L + 4) * D * 2);
    LAS char* og = lds + KC_OUT;
    if (act) {
        const f32x4 cs = cs0 + cs1;
#pragma unroll
        for (int t = 0; t < 2; ++t) {
            const f32x4 a = sel4(kh, acc[2 + t], acc[t]) + xb[((w ^ 1) * 2 + t) * 64 + lane];
            const f32x2 ab = st[(2 * kh + t) * 16 + i16];
            float o[4];
#pragma unroll
            for (int k = 0; k < 4; ++k) o[k] = gelu_tanh(ab[0] * a[k] - ab[1] * cs[k]);
            *(LAS u32x2*)(og + (kh * 32 + t * 16 + i16) * OUT_ROW + b * 32 + g * 8) = (u32x2){pk_bf16(o[0], o[1]), pk_bf16(o[2], o[3])};
        }
    }
    LGKM0(); BAR();
    {
        const int nc = 2 * nblk, row = nblk == 5 ? tid / 10 : tid >> 3, c = tid - row * nc;
        if (row < 64) bst16(*(const LAS u32x4*)(og + row * OUT_ROW + c * 16), rg, row * (D * 2) + c * 16, ((2 + tok0) * D + nbs * 16) * 2);
        if ((panel == 0 || panel == 31) && tid >= 704 && tid < 704 + 2 * nc) {
            const int z = tid - 704, zr = z >= nc ? 1 : 0;
            bst16((u32x4){0u, 0u, 0u, 0u}, rg, zr * (D * 2) + (z - zr * nc) * 16, ((panel == 0 ? 0 : L + 2) * D + nbs * 16) * 2);
        }
    }
    VM0(); BAR();
    publish(ws, WS_FG, panel * 32 + j, tag);
}

constexpr int KD_HT = 73728, KD_XB = KD_HT + 10240, KD_OUT = KD_XB + 20 * 1024;
__device__ __forceinline__ void phase_d(char* __restrict__ ws, LAS char* lds, int panel, int j, int je, unsigned tag) {
    const int tid = threadIdx.x, w = __builtin_amdgcn_readfirstlane(tid >> 6), lane = tid & 63;
    const int tok0 = panel * 64, nbs = nb0_of(j), nblk = nb0_of(j + 1) - nbs;
    const int g = lane >> 4, i16 = lane & 15, q = i16 >> 2, p = lane & 3;
    const int b = w >> 1, kh = w & 1;
    const bool act = w < 10 && b < nblk;
    const int nb = nbs + (act ? b : 0);
    bf16x8 W[9];
    if (w < 10) {
        const rsrc_t rw = mk_rsrc(ws + WS_WF + WSUB * 2, WSUB * 2);
#pragma unroll
        for (int i = 0; i < 9; ++i) W[i] = bld16<SC1>(rw, lane * 16, ((kh * 9 + i) * 36 + nb) * 1024);
    }
    if (w < 2 * nblk) {
        const rsrc_t rh = mk_rsrc(ws + WS_HT, D * L * 2);
        const int fl = ((lane >> 4) & 1) | ((w & 1) << 1);
        blds16<SC1>(rh, lds + KD_HT + w * 1024, (lane >> 3) * 4096 + (((lane & 7) ^ (fl << 1)) * 16), (nbs * 16 + w * 8) * 4096 + tok0 * 2);
    }
    if (w == 11) {
        const int p0 = panel > 0 ? panel - 1 : 0, p1 = panel < 31 ? panel + 1 : 31;
        wait_panels(ws, WS_FG, p0, WS_FG, panel, WS_FG, p1, tag);
    }
    BAR();
    f32x2 st[2];
    {
        const rsrc_t rg = mk_rsrc(ws + WS_G, (L + 4) * D * 2);
        const int c7 = (lane & 7) ^ ((lane >> 3) & 6), vb = (lane >> 3) * 1152 + c7 * 16;
#pragma unroll
        for (int k = 0; k < 6; ++k) {
            const int idx = 6 * w + k, cg = idx >> 3, c = cg * 8 + c7;
            const int o2 = (0x4031 >> (((c * 57) >> 10) * 4)) & 15;
            blds16<SC1>(rg, lds + idx * 1024, vb + o2 * 1152, (tok0 + (idx & 7) * 8) * 1152 + cg * 128);
        }
        const rsrc_t r2 = mk_rsrc(ws + WS_ST2, L * 8);
#pragma unroll
        for (int t = 0; t < 2; ++t) st[t] = __builtin_bit_cast(f32x2, bldu2<SC1>(r2, i16 * 8, (tok0 + kh * 32 + t * 16) * 8));
    }
    VM0();
    asm volatile("" : "+v"(st[0]), "+v"(st[1]));
    BAR();
    f32x4 acc[4] = {};
    LAS f32x4* xb = (LAS f32x4*)(lds + KD_XB);
    if (w < 10) {
        const int z = i16 & 6;
        const LAS char* a0 = lds + i16 * 128 + ((g ^ z) * 16), * a1 = lds + i16 * 128 + (((4 + g) ^ z) * 16);
        const LAS char* e0 = kh ? a1 + 4 * 8192 : a0;
        const LAS char* e1 = kh ? a0 + 5 * 8192 : a1;
        bf16x8 A[2][2][4];
#pragma unroll
        for (int c = 0; c < 6; ++c) {
            if (c < 5) {
#pragma unroll
                for (int ii = 0; ii < 2; ++ii) {
                    const int i = 2 * c + ii;
                    if (i < 9) {
#pragma unroll
                        for (int mt = 0; mt < 4; ++mt) A[c & 1][ii][mt] = *(const LAS bf16x8*)(((i & 1) ? e1 : e0) + (i >> 1) * 8192 + mt * 2048);
                    }
                }
            }
            __builtin_amdgcn_sched_barrier(0);
            if (c > 0) {
#pragma unroll
                for (int ii = 0; ii < 2; ++ii)
                    if (2 * (c - 1) + ii < 9) {
#pragma unroll
                        for (int mt = 0; mt < 4; ++mt) acc[mt] = MFMA16(W[2 * (c - 1) + ii], A[(c - 1) & 1][ii][mt], acc[mt]);
                    }
            }
            __builtin_amdgcn_sched_barrier(0);
        }
        xb[((w * 2 + 0) * 64) + lane] = sel4(kh, acc[0], acc[2]);
        xb[((w * 2 + 1) * 64) + lane] = sel4(kh, acc[1], acc[3]);
    }
    LGKM0(); BAR();
    LAS char* ox = lds + KD_OUT;
    if (w < 10) {
        const int bl = act ? b : 0;
        const int hrow = 16 * bl + 4 * g + q;
        const LAS char* hb = lds + KD_HT + hrow * 128 + 8 * (p & 1);
        const rsrc_t rp = mk_rsrc(ws + WS_PST, 32 * 36 * 64 * 8);
#pragma unroll
        for (int t = 0; t < 2; ++t) {
            const int mt = 2 * kh + t;
            const f32x4 a = sel4(kh, acc[2 + t], acc[t]) + xb[((w ^ 1) * 2 + t) * 64 + lane];
            const u32x2 hu = __builtin_bit_cast(u32x2, tr_read(hb + (((2 * mt + (p >> 1)) ^ (fsw(hrow) << 1)) * 16)));
            const float rs = st[t][1], nm = -st[t][0] * rs;
            u32x2 ov;
#pragma unroll
            for (int k = 0; k < 2; ++k)
                ov[k] = pk_bf16(__builtin_fmaf(bf_lo(hu[k]), rs, nm) + a[2 * k], __builtin_fmaf(bf_hi(hu[k]), rs, nm) + a[2 * k + 1]);
            float s1 = dot2bf(ov[1], 0x3f803f80u, dot2bf(ov[0], 0x3f803f80u, 0.f)), s2 = dot2bf(ov[1], ov[1], dot2bf(ov[0], ov[0], 0.f));
            s1 = rsum_g(s1); s2 = rsum_g(s2);
            if (act) {
                *(LAS u32x2*)(ox + (mt * 16 + i16) * OUT_ROW + b * 32 + g * 8) = ov;
                if (lane < 16) bst8((u32x2){__float_as_uint(s1), __float_as_uint(s2)}, rp, lane * 8, ((panel * 36 + nb) * 64 + mt * 16) * 8);
            }
        }
    }
    LGKM0(); BAR();
    {
        const int nc = 2 * nblk, row = nblk == 5 ? tid / 10 : tid >> 3, c = tid - row * nc;
        if (row < 64) {
            const int so = (tok0 * D + nbs * 16) * 2, vo = row * (D * 2) + c * 16;
            bst16(*(const LAS u32x4*)(ox + row * OUT_ROW + c * 16), mk_rsrc(ws + WS_X1, L * D * 2), vo, so);
        }
    }
    if (w == 10) wait_flags(ws, WS_FW, (2 + 2 * je) * 18, 18, WS_FW, 0, 0, tag);
    VM0(); BAR();
    publish(ws, WS_FX, panel * 32 + j, tag);
}

constexpr int BG_LDS = 131072;
__device__ __forceinline__ void phase_cd_background(const Args& a, LAS char* lds, int panel, int job, unsigned tag) {
    char* ws = a.ws;
    const int tid = threadIdx.x, w = __builtin_amdgcn_readfirstlane(tid >> 6), lane = tid & 63, tok0 = panel * 64;
    BAR(); BAR();
    {
        const rsrc_t rh = mk_rsrc(ws + WS_HT, D * L * 2);
        const int fl = (lane >> 4) & 1;
        const int v0 = (lane >> 3) * 4096 + (((lane & 7) ^ (fl << 1)) * 16), v1 = (lane >> 3) * 4096 + (((lane & 7) ^ ((fl | 2) << 1)) * 16);
#pragma unroll
        for (int k = 0; k < 6; ++k) { const int idx = 6 * w + k; blds16<SC1>(rh, lds + idx * 1024, (k & 1) ? v1 : v0, idx * 32768 + tok0 * 2); }
    }
    VM0(); BAR();
    PrepRegs P;
    prep_issue(a, job, lane, P);
    BAR(); BAR(); BAR();
    prep_finish<false, false>(a, job, lds + BG_LDS + (w - 8) * 8192, lane, tag, P);
    BAR();
    {
        const rsrc_t rg = mk_rsrc(ws + WS_G, (L + 4) * D * 2);
        const int c7 = (lane & 7) ^ ((lane >> 3) & 6), vb = (lane >> 3) * 1152 + c7 * 16;
#pragma unroll
        for (int k = 0; k < 6; ++k) {
            const int idx = 6 * w + k, cg = idx >> 3, c = cg * 8 + c7;
            const int o2 = (0x4031 >> (((c * 57) >> 10) * 4)) & 15;
            blds16<SC1>(rg, lds + idx * 1024, vb + o2 * 1152, (tok0 + (idx & 7) * 8) * 1152 + cg * 128);
        }
    }
    VM0();
    if (lane == 0) __hip_atomic_store((gu32*)(ws + WS_FW) + job, tag, RLX_AGENT);
    BAR(); BAR(); BAR(); BAR();
}

constexpr int KE_HID = 73728, KE_ST = 2 * 73728, KE_FLG = KE_ST + 1024;
constexpr int NB = 3, RING = 4;
__device__ __forceinline__ void phase_e(char* __restrict__ ws, LAS char* lds, int panel, int j, unsigned tag) {
    const int w = __builtin_amdgcn_readfirstlane(threadIdx.x >> 6), lane = threadIdx.x & 63;
    constexpr int S1 = 6, S2 = 12;
    const int tok0 = panel * 64, g = lane >> 4, i16 = lane & 15, nbs = NB * w;
    const rsrc_t rw = mk_rsrc(ws + WS_WF + (size_t)(2 + 2 * j) * WSUB * 2, 2 * WSUB * 2);
    const int wv = lane * 16, wsb = nbs * 1024;
    bf16x8 W[RING][NB];
#pragma unroll
    for (int s = 0; s < RING; ++s)
#pragma unroll
        for (int b = 0; b < NB; ++b) W[s][b] = bld16<SC1>(rw, wv + b * 1024, wsb + s * 36864);
    f32x4 acc[NB][4];
    {
        const rsrc_t rc = mk_rsrc(ws + WS_CS, 2 * (D + F) * 4);
#pragma unroll
        for (int b = 0; b < NB; ++b) {
            acc[b][0] = bldf4<SC1>(rc, g * 16 + b * 64, (D + j * D + nbs * 16) * 4);
            acc[b][1] = bldf4<SC1>(rc, g * 16 + b * 64, (D + j * D + nbs * 16 + D + F) * 4);
        }
    }
    __builtin_amdgcn_sched_barrier(0);
    unsigned fw4 = tag;
    if (w == 10 && lane < 18) fw4 = __hip_atomic_load((gu32*)(ws + WS_FW) + (3 + 2 * j) * 18 + lane, RLX_AGENT);
    if (w == 11) wait_panels(ws, WS_FX, panel, WS_FX, panel, WS_FX, panel, tag);
    BAR();
    u32x2 pv[36];
    if (w == 11) {
        const rsrc_t rp = mk_rsrc(ws + WS_PST + (size_t)panel * 36 * 512, 36 * 512);
#pragma unroll
        for (int k = 0; k < 36; ++k) pv[k] = bldu2<SC1>(rp, lane * 8, k * 512);
    }
    {
        const rsrc_t rx = mk_rsrc(ws + WS_X1 + (size_t)tok0 * D * 2, 64 * D * 2);
        const int vo = (lane >> 3) * 1152 + (((lane & 7) ^ ((lane >> 3) & 6)) * 16);
#pragma unroll
        for (int k = 0; k < 6; ++k) {
            const int idx = 6 * w + k;
            blds16<SC1>(rx, lds + idx * 1024, vo, (idx & 7) * 9216 + (idx >> 3) * 128);
        }
    }
    __builtin_amdgcn_sched_barrier(0);
    LAS f32x4* st = (LAS f32x4*)(lds + KE_ST);
    LAS unsigned* flg = (LAS unsigned*)(lds + KE_FLG);
    if (w == 11) {
        float a1[4] = {}, a2[4] = {};
#pragma unroll
        for (int k = 0; k < 36; ++k) { a1[k & 3] += __uint_as_float(pv[k][0]); a2[k & 3] += __uint_as_float(pv[k][1]); }
        const float s1 = (a1[0] + a1[1]) + (a1[2] + a1[3]), s2 = (a2[0] + a2[1]) + (a2[2] + a2[3]);
        const float mean = s1 * (1.0f / D), var = fmaxf(s2 * (1.0f / D) - mean * mean, 0.f);
        const float mb = rbf(mean), sb = ln_sd(var), al = __builtin_amdgcn_rcpf(sb);
        const float k0 = -2.0f * 0.7978845608028654f * 1.4426950408889634f;
        st[lane] = (f32x4){k0 * al, k0 * 0.044715f * al * al * al, sb, -mb};
        if (lane < 3) flg[lane] = 0u;
    }
    if (w == 10) { const bool ok4 = __all(fw4 == tag); if (lane == 0) flg[3] = ok4 ? 1u : 0u; }
    VM0(); LGKM0(); BAR();
    const int z = i16 & 6;
    const int o0 = i16 * 128 + (g ^ z) * 16, o1 = i16 * 128 + ((4 + g) ^ z) * 16;
    {
        float nm[4];
#pragma unroll
        for (int mt = 0; mt < 4; ++mt) nm[mt] = st[mt * 16 + i16][3];
#pragma unroll
        for (int b = 0; b < NB; ++b) {
            const f32x4 cs = acc[b][0] + acc[b][1];
#pragma unroll
            for (int mt = 0; mt < 4; ++mt) acc[b][mt] = cs * nm[mt];
        }
    }
#define E_SPIN(cond) do { while (cond) __builtin_amdgcn_s_sleep(1); __builtin_amdgcn_sched_barrier(0); asm volatile("" ::: "memory"); } while (0)
#define E_FLG(i) __hip_atomic_load(flg + (i), __ATOMIC_RELAXED, __HIP_MEMORY_SCOPE_WORKGROUP)
    bf16x8 A[2][4];
#pragma unroll
    for (int ph = 0; ph < 2; ++ph) {
        const LAS char* ab = lds + ph * KE_HID;
        if (ph == 1) E_SPIN(E_FLG(0) < 4u);
#pragma unroll
        for (int mt = 0; mt < 4; ++mt) A[0][mt] = *(const LAS bf16x8*)(ab + o0 + mt * 2048);
#pragma unroll
        for (int s = 0; s < 18; ++s) {
            if (ph == 1 && s + 1 == S1) E_SPIN(E_FLG(1) < 4u);
            if (ph == 1 && s + 1 == S2) E_SPIN(E_FLG(2) < 4u);
            if (ph == 0 && s == 2 && w == 8 && E_FLG(3) == 0u) {
                wait_flags(ws, WS_FW, (3 + 2 * j) * 18, 18, WS_FW, 0, 0, tag);
                if (lane == 0) __hip_atomic_store(flg + 3, 1u, __ATOMIC_RELAXED, __HIP_MEMORY_SCOPE_WORKGROUP);
            }
            if (ph == 0 && s == 18 - RING) E_SPIN(E_FLG(3) == 0u);
            if (s + 1 < 18) {
#pragma unroll
                for (int mt = 0; mt < 4; ++mt) A[(s + 1) & 1][mt] = *(const LAS bf16x8*)(ab + (((s + 1) & 1) ? o1 : o0) + mt * 2048 + ((s + 1) >> 1) * 8192);
            }
            const int t = ph * 18 + s;
#pragma unroll
            for (int b = 0; b < NB; ++b) {
#pragma unroll
                for (int mt = 0; mt < 4; ++mt) acc[b][mt] = MFMA16(W[t % RING][b], A[s & 1][mt], acc[b][mt]);
                if (t + RING < 36) W[t % RING][b] = bld16<SC1>(rw, wv + b * 1024, wsb + (t + RING) * 36864);
            }
            __builtin_amdgcn_sched_barrier(0);
        }
        if (ph == 0) {
#pragma unroll
            for (int mt = 0; mt < 4; ++mt) {
                const f32x4 k = st[mt * 16 + i16];
#pragma unroll
                for (int b = 0; b < NB; ++b) {
                    float o[4];
#pragma unroll
                    for (int e = 0; e < 4; ++e) {
                        const float a = acc[b][mt][e];
                        const float ex = __builtin_amdgcn_exp2f(a * (k[0] + k[1] * (a * a)));
                        o[e] = a * __builtin_amdgcn_rcpf(k[2] + k[2] * ex);
                    }
                    const int nb = nbs + b;
                    *(LAS u32x2*)(lds + KE_HID + (nb >> 2) * 8192 + (mt * 16 + i16) * 128 + (((2 * (nb & 3) + (g >> 1)) ^ z) * 16) + 8 * (g & 1)) =
                        (u32x2){pk_bf16(o[0], o[1]), pk_bf16(o[2], o[3])};
                    acc[b][mt] = (f32x4){0.f, 0.f, 0.f, 0.f};
                }
            }
            LGKM0();
            if (lane == 0) __hip_atomic_fetch_add(flg + (w >> 2), 1u, __ATOMIC_RELAXED, __HIP_MEMORY_SCOPE_WORKGROUP);
            if (w >= 8) __builtin_amdgcn_s_setprio(2); else if (w >= 4) __builtin_amdgcn_s_setprio(1);
        }
    }
#undef E_SPIN
#undef E_FLG
    __builtin_amdgcn_s_setprio(0);
    const rsrc_t rp = mk_rsrc(ws + WS_PART + (size_t)(panel * 8 + j) * 72 * 1024, 72 * 1024);
#pragma unroll
    for (int b = 0; b < NB; ++b)
#pragma unroll
        for (int m2 = 0; m2 < 2; ++m2) {
            const f32x4 a0 = acc[b][2 * m2], a1 = acc[b][2 * m2 + 1];
            const u32x4 v = {pk_bf16(a0[0], a0[1]), pk_bf16(a0[2], a0[3]), pk_bf16(a1[0], a1[1]), pk_bf16(a1[2], a1[3])};
            const int pr = (nbs + b) * 2 + m2;
            if (pr >= 9 * j && pr < 9 * j + 9) *(LAS u32x4*)(lds + (pr - 9 * j) * 1024 + lane * 16) = v;
            else bst16(v, rp, lane * 16, pr * 1024);
        }
    LGKM0();
    VM0(); BAR();
    publish(ws, WS_FP, panel * 32 + j, tag);
}

constexpr int KF_HT = 16384;
__device__ __forceinline__ void phase_f(char* __restrict__ ws, LAS char* lds, u16* __restrict__ out, int panel, int jj, unsigned tag) {
    const int tid = threadIdx.x, w = __builtin_amdgcn_readfirstlane(tid >> 6), lane = tid & 63;
    const int g = lane >> 4, i16 = lane & 15, q = i16 >> 2, p = lane & 3;
    const int pr = jj * 9 + (w < 9 ? w : 0), nb = pr >> 1, m2 = pr & 1, nbs = (jj * 9) >> 1;
    u32x2 xa[2];
    const int so = ((panel * 64 + m2 * 32) * D + nb * 16) * 2, vo = (i16 * D + 4 * g) * 2;
    if (w < 10) {
        const rsrc_t rh = mk_rsrc(ws + WS_HT, D * L * 2);
        const int fl = ((lane >> 4) & 1) | ((w & 1) << 1);
        blds16<SC1>(rh, lds + KF_HT + w * 1024, (lane >> 3) * 4096 + (((lane & 7) ^ (fl << 1)) * 16), (nbs * 16 + w * 8) * 4096 + panel * 128);
    }
    if (w < 9) {
        const rsrc_t r1 = mk_rsrc(ws + WS_X1, L * D * 2);
#pragma unroll
        for (int t = 0; t < 2; ++t) xa[t] = bldu2<SC1>(r1, vo, so + t * 16 * D * 2);
    }
    if (w == 11) wait_panels(ws, WS_FP, panel, WS_FP, panel, WS_FP, panel, tag);
    VM0(); BAR();
    if (w < 9) {
        const rsrc_t rp = mk_rsrc(ws + WS_PART + (size_t)(panel * 8) * 72 * 1024, 8 * 72 * 1024);
        u32x4 v[8];
#pragma unroll
        for (int k = 0; k < 7; ++k) v[k] = bldu4<SC1>(rp, lane * 16, ((k < jj ? k : k + 1) * 72 + pr) * 1024);
        v[7] = *(const LAS u32x4*)(lds + w * 1024 + lane * 16);
        const int hrow = 16 * (nb - nbs) + 4 * g + q;
        u32x2 ha[2];
#pragma unroll
        for (int t = 0; t < 2; ++t)
            ha[t] = __builtin_bit_cast(u32x2, tr_read(lds + KF_HT + hrow * 128 + (((2 * (2 * m2 + t) + (p >> 1)) ^ (fsw(hrow) << 1)) * 16) + 8 * (p & 1)));
        float s[8] = {};
#pragma unroll
        for (int k = 0; k < 8; ++k)
#pragma unroll
            for (int e = 0; e < 4; ++e) { s[2 * e] += bf_lo(v[k][e]); s[2 * e + 1] += bf_hi(v[k][e]); }
#pragma unroll
        for (int t = 0; t < 2; ++t) {
            float o[4];
#pragma unroll
            for (int k = 0; k < 4; ++k) {
                const float x1 = (k & 1) ? bf_hi(xa[t][k >> 1]) : bf_lo(xa[t][k >> 1]);
                const float h = (k & 1) ? bf_hi(ha[t][k >> 1]) : bf_lo(ha[t][k >> 1]);
                o[k] = h + (x1 + s[4 * t + k]);
            }
            __builtin_amdgcn_raw_buffer_store_b64((u32x2){pk_bf16(o[0], o[1]), pk_bf16(o[2], o[3])}, mk_rsrc(out, L * D * 2), vo, so + t * 16 * D * 2, 0);
        }
    }
}

constexpr int LDS_CTRL = 3 * CV_CH > KE_FLG + 32 ? 3 * CV_CH : KE_FLG + 32;
constexpr int LDS_BYTES = LDS_CTRL + 64;

NOPK __global__ __launch_bounds__(NTHR) void mega_kernel(Args a) {
    extern __shared__ __attribute__((aligned(16))) char smem[];
    LAS char* lds = (LAS char*)smem;
    const int tid = threadIdx.x, w = __builtin_amdgcn_readfirstlane(tid >> 6), lane = tid & 63, b = blockIdx.x;
    unsigned cpf = 0;
    if (w == 8) {
        unsigned long long pc;
        asm volatile("s_getpc_b64 %0" : "=s"(pc));
        const int line = (b >> 3) * 13 + lane;
        if (lane < 13 && line * 128 < CODE_PF) cpf = *(const GAS unsigned*)((pc & ~255ull) + (unsigned long long)line * 128);
    }
    const unsigned tagv = __hip_atomic_load((gu32*)(a.ws + WS_EPOCH), RLX_AGENT);
    const StatRow sr = row_stats_load(a, w, lane);
    __builtin_amdgcn_sched_barrier(0);
    unsigned tag;
    if (b < 192) phase_conv(a, lds, tagv, sr, tag);
    else {
        tag = __builtin_amdgcn_readfirstlane(tagv) + 1u;
        if (tid == 0) __hip_atomic_store((gu32*)(a.ws + WS_FA) + b, tag, RLX_AGENT);
        if (tid == 0) *(LAS unsigned*)(lds + LDS_FWOK) = 0u;
        row_stats(a, sr, tag, w, lane);
        LGKM0(); BAR();
        const int i = w * 64 + (b - 192);
        if (i >= 36) {
            const unsigned long long t0 = __builtin_amdgcn_s_memrealtime();
            while (__builtin_amdgcn_s_memrealtime() - t0 < PREP_DELAY) __builtin_amdgcn_s_sleep(8);
        }
        if (i < 180) {
            const int job = i < 36 ? i : (2 + 2 * ((i - 36) / 18)) * 18 + (i - 36) % 18;
            PrepRegs P;
            prep_issue(a, job, lane, P);
            prep_finish<true, true>(a, job, lds + w * 8192, lane, tag, P);
        }
        BAR();
    }
    asm volatile("" :: "v"(cpf));
    const int je = 2 * (b & 3) + ((b >> 3) & 1);
    {
        const int panel = (b & 7) * 4 + (b >> 6), j = (b >> 3) & 7;
        const int m = (w - 8) * 128 + ((b & 7) | ((b >> 4) << 3));
        if ((j & 1) && (w == 8 || w == 9) && m < 144) phase_cd_background(a, lds, panel, (3 + 2 * (m / 18)) * 18 + m % 18, tag);
        else {
            phase_c(a.ws, lds, panel, j, tag);
            phase_d(a.ws, lds, panel, j, je, tag);
        }
    }
    phase_e(a.ws, lds, ((b >> 2) & 1) * 16 + (b >> 4), je, tag);
    phase_f(a.ws, lds, a.out, ((b >> 2) & 1) * 16 + (b >> 4), je, tag);
    if (b == 0 && w == 10) {
        wait_flags(a.ws, WS_FA, 0, 256, WS_FA, 0, 0, tag);
        if (lane == 0) __hip_atomic_store((gu32*)(a.ws + WS_EPOCH), tag, RLX_AGENT);
    }
}

extern "C" void kernel_launch(void* const* d_in, const int* in_sizes, int n_in,
                              void* d_out, int out_size, void* d_ws, size_t ws_size,
                              hipStream_t stream) {
    Args a;
    a.x = (const u16*)d_in[0];
    a.are = (const u16*)d_in[1]; a.aim = (const u16*)d_in[2];
    a.bre = (const u16*)d_in[3]; a.bim = (const u16*)d_in[4];
    a.cre = (const u16*)d_in[5]; a.cim = (const u16*)d_in[6];
    a.logdt = (const u16*)d_in[7]; a.dskip = (const u16*)d_in[8];
    a.w1 = (const u16*)d_in[10]; a.w2 = (const u16*)d_in[11];
    a.w3 = (const u16*)d_in[12]; a.w4 = (const u16*)d_in[13];
    a.ws = (char*)d_ws; a.out = (u16*)d_out;
    (void)hipFuncSetAttribute((const void*)mega_kernel, hipFuncAttributeMaxDynamicSharedMemorySize, LDS_BYTES);
    mega_kernel<<<NWG, NTHR, LDS_BYTES, stream>>>(a);
}
```

```cpp
#include <hip/hip_runtime.h>
#include <hip/hip_bf16.h>
#include <stdint.h>

typedef unsigned short u16;
typedef short bf16x8 __attribute__((ext_vector_type(8)));
typedef short s16x4 __attribute__((ext_vector_type(4)));
typedef float f32x2 __attribute__((ext_vector_type(2)));
typedef float f32x4 __attribute__((ext_vector_type(4)));
typedef float f32x16 __attribute__((ext_vector_type(16)));
typedef unsigned u32x2 __attribute__((ext_vector_type(2)));
typedef unsigned u32x4 __attribute__((ext_vector_type(4)));
typedef __bf16 bf16x2_t __attribute__((ext_vector_type(2)));
typedef _Float16 f16x2 __attribute__((ext_vector_type(2)));
typedef _Float16 f16x8 __attribute__((ext_vector_type(8)));
#define LAS __attribute__((address_space(3)))
#define GAS __attribute__((address_space(1)))
#if defined(__HIP_DEVICE_COMPILE__)
#define NOPK __attribute__((target("no-packed-fp32-ops")))
#else
#define NOPK
#endif

constexpr int L = 2048, D = 576, NS = 128, F = 4608;
constexpr int WSUB = D * D;
constexpr float LN_EPS = 1e-5f;
constexpr int NWG = 256, NTHR = 768;
constexpr int SC1 = 16;

constexpr size_t WS_EPOCH = 0;
constexpr int REP = 8;
constexpr size_t WS_FA = 256;
constexpr size_t WS_FW = WS_FA + 1024;
constexpr size_t WS_FG = WS_FW + 2048;
constexpr size_t WS_FX = WS_FG + 4096;
constexpr size_t WS_FP = WS_FX + 4096;
constexpr size_t WS_FH = WS_FP + 4096;
constexpr size_t WS_GR = 32768;
constexpr size_t WS_HT = WS_GR + (size_t)L * 8;
constexpr size_t WS_WF = WS_HT + (size_t)D * L * 2;
constexpr size_t WS_CS = WS_WF + (size_t)18 * WSUB * 2;
constexpr size_t WS_ST2 = WS_CS + (size_t)2 * (D + F) * 4;
constexpr size_t WS_G = WS_ST2 + (size_t)L * 8;
constexpr size_t WS_X1 = WS_G + (size_t)(L + 4) * D * 2;
constexpr size_t WS_PST = WS_X1 + (size_t)L * D * 2;
constexpr size_t WS_PART = WS_PST + (size_t)32 * 36 * 64 * 8;
constexpr size_t WS_END = WS_PART + (size_t)32 * 8 * 72 * 512 * 2;

__device__ __forceinline__ unsigned pk_bf16(float lo, float hi) {
    f32x2 v = {lo, hi};
    return __builtin_bit_cast(unsigned, __builtin_convertvector(v, bf16x2_t));
}
__device__ __forceinline__ float bf_lo(unsigned p) { return __uint_as_float(p << 16); }
__device__ __forceinline__ float bf_hi(unsigned p) { return __uint_as_float(p & 0xffff0000u); }
__device__ __forceinline__ float bf2f(u16 x) { return __uint_as_float((unsigned)x << 16); }
__device__ __forceinline__ float rbf(float x) { return bf_lo(pk_bf16(x, x)); }
__device__ __forceinline__ float dot2bf(unsigned a, unsigned b, float c) {
    return __builtin_amdgcn_fdot2_f32_bf16(__builtin_bit_cast(bf16x2_t, a), __builtin_bit_cast(bf16x2_t, b), c, false);
}
__device__ __forceinline__ float gelu_tanh(float x) {
    const float c0 = -2.0f * 0.7978845608028654f * 1.4426950408889634f;
    const float c1 = c0 * 0.044715f;
    float e = __builtin_amdgcn_exp2f(x * (c0 + c1 * x * x));
    return x * __builtin_amdgcn_rcpf(1.0f + e);
}
__device__ __forceinline__ s16x4 tr_read(const LAS char* p) {
    return __builtin_amdgcn_ds_read_tr16_b64_v4i16((LAS s16x4*)p);
}
__device__ __forceinline__ bf16x8 cat8(s16x4 a, s16x4 b) {
    return __builtin_shufflevector(a, b, 0, 1, 2, 3, 4, 5, 6, 7);
}
__device__ __forceinline__ f32x4 sel4(bool c, f32x4 a, f32x4 b) {
    f32x4 r;
#pragma unroll
    for (int k = 0; k < 4; ++k) { float x = a[k], y = b[k]; asm volatile("" : "+v"(x), "+v"(y)); r[k] = c ? x : y; }
    return r;
}
__device__ __forceinline__ float rsum_g(float x) {
    const unsigned u = __float_as_uint(x);
    const auto a = __builtin_amdgcn_permlane16_swap(u, u, false, false);
    const float y = __uint_as_float(a[0]) + __uint_as_float(a[1]);
    const unsigned v = __float_as_uint(y);
    const auto b = __builtin_amdgcn_permlane32_swap(v, v, false, false);
    return __uint_as_float(b[0]) + __uint_as_float(b[1]);
}
template <int CTRL> __device__ __forceinline__ float dpp_f(float x) {
    return __uint_as_float(__builtin_amdgcn_update_dpp(0u, __float_as_uint(x), CTRL, 0xf, 0xf, false));
}
__device__ __forceinline__ float rsum_64(float x) {
    x += dpp_f<0xB1>(x);
    x += dpp_f<0x4E>(x);
    x += dpp_f<0x141>(x);
    x += dpp_f<0x140>(x);
    return rsum_g(x);
}
template <int CTRL> __device__ __forceinline__ unsigned dpp_u(unsigned x) { return __builtin_amdgcn_update_dpp(0u, x, CTRL, 0xf, 0xf, false); }
__device__ __forceinline__ unsigned umax2(unsigned a, unsigned b) { return a > b ? a : b; }
__device__ __forceinline__ float rmax_64(float xf) {
    unsigned x = __float_as_uint(xf);
    x = umax2(x, dpp_u<0xB1>(x)); x = umax2(x, dpp_u<0x4E>(x)); x = umax2(x, dpp_u<0x141>(x)); x = umax2(x, dpp_u<0x140>(x));
    const auto a = __builtin_amdgcn_permlane16_swap(x, x, false, false); x = umax2(a[0], a[1]);
    const auto b = __builtin_amdgcn_permlane32_swap(x, x, false, false); x = umax2(b[0], b[1]);
    return __uint_as_float(x);
}
__device__ __forceinline__ float ln_sd(float var) { return rbf(__builtin_amdgcn_sqrtf(rbf(rbf(var) + LN_EPS))); }
__device__ __forceinline__ int fsw(int row) { return ((row >> 1) & 1) | (((row >> 3) & 1) << 1); }
#define MFMA16(a, b, c) __builtin_amdgcn_mfma_f32_16x16x32_bf16((a), (b), (c), 0, 0, 0)
#define MFMA32(a, b, c) __builtin_amdgcn_mfma_f32_32x32x16_bf16((a), (b), (c), 0, 0, 0)
#define VM0() asm volatile("s_waitcnt vmcnt(0)" ::: "memory")
#define LGKM0() asm volatile("s_waitcnt lgkmcnt(0)" ::: "memory")
#define BAR() do { asm volatile("" ::: "memory"); __builtin_amdgcn_s_barrier(); asm volatile("" ::: "memory"); } while (0)

typedef __amdgpu_buffer_rsrc_t rsrc_t;
__device__ __forceinline__ rsrc_t mk_rsrc(const void* p, unsigned bytes) { return __builtin_amdgcn_make_buffer_rsrc((void*)p, 0, bytes, 0x00020000); }
template <int AUX> __device__ __forceinline__ bf16x8 bld16(rsrc_t r, int voff, int soff) {
    return __builtin_bit_cast(bf16x8, __builtin_amdgcn_raw_buffer_load_b128(r, voff, soff, AUX));
}
template <int AUX> __device__ __forceinline__ f32x4 bldf4(rsrc_t r, int voff, int soff) {
    return __builtin_bit_cast(f32x4, __builtin_amdgcn_raw_buffer_load_b128(r, voff, soff, AUX));
}
template <int AUX> __device__ __forceinline__ u32x4 bldu4(rsrc_t r, int voff, int soff) {
    return __builtin_amdgcn_raw_buffer_load_b128(r, voff, soff, AUX);
}
template <int AUX> __device__ __forceinline__ u32x2 bldu2(rsrc_t r, int voff, int soff) {
    return __builtin_amdgcn_raw_buffer_load_b64(r, voff, soff, AUX);
}
template <int AUX> __device__ __forceinline__ void blds16(rsrc_t r, LAS char* l, int voff, int soff) {
    __builtin_amdgcn_raw_ptr_buffer_load_lds(r, (LAS void*)l, 16, voff, soff, 0, AUX);
}
__device__ __forceinline__ void bst16(u32x4 v, rsrc_t r, int voff, int soff) { __builtin_amdgcn_raw_buffer_store_b128(v, r, voff, soff, SC1); }
__device__ __forceinline__ void bst8(u32x2 v, rsrc_t r, int voff, int soff) { __builtin_amdgcn_raw_buffer_store_b64(v, r, voff, soff, SC1); }
__device__ __forceinline__ void bst4(unsigned v, rsrc_t r, int voff, int soff) { __builtin_amdgcn_raw_buffer_store_b32(v, r, voff, soff, SC1); }

struct Args {
    const u16* x; const u16 *are, *aim, *bre, *bim, *cre, *cim, *logdt, *dskip;
    const u16 *w1, *w2, *w3, *w4;
    char* ws; u16* out;
};

typedef GAS unsigned gu32;
typedef GAS unsigned long long gu64;
#define RLX_AGENT __ATOMIC_RELAXED, __HIP_MEMORY_SCOPE_AGENT
constexpr unsigned long long PREP_DELAY = 280ull;
constexpr int CODE_PF = 41984;
constexpr unsigned long long WAIT_TICKS = 400000ull;
__device__ __forceinline__ void publish(char* ws, size_t flag_off, int idx, unsigned tag) {
    if (threadIdx.x == 0) __hip_atomic_store((gu32*)(ws + flag_off) + idx, tag, RLX_AGENT);
}
__device__ __forceinline__ void publish_rep(char* ws, size_t flag_off, int idx, unsigned tag, int lane) {
    if (lane < REP) __hip_atomic_store((gu32*)(ws + flag_off) + lane * 256 + idx, tag, RLX_AGENT);
}
__device__ __forceinline__ void wait_flags(char* ws, size_t offa, int a0, int na, size_t offb, int b0, int nb, unsigned tag) {
    const int lane = threadIdx.x & 63;
    const gu32* p[4]; bool on[4];
#pragma unroll
    for (int k = 0; k < 4; ++k) {
        const int i = lane + 64 * k;
        on[k] = i < na + nb;
        p[k] = i < na ? (const gu32*)(ws + offa) + a0 + i : (const gu32*)(ws + offb) + b0 + (on[k] ? i - na : 0);
    }
    const unsigned long long t0 = __builtin_amdgcn_s_memrealtime();
    for (;;) {
        bool ok = true;
#pragma unroll
        for (int k = 0; k < 4; ++k)
            if (64 * k < na + nb) { const unsigned v = __hip_atomic_load(p[k], RLX_AGENT); ok = ok && (!on[k] || v == tag); }
        if (__all(ok)) break;
        if (__builtin_amdgcn_s_memrealtime() - t0 > WAIT_TICKS) break;
        __builtin_amdgcn_s_sleep(2);
    }
}
__device__ __forceinline__ void wait_panels(char* ws, size_t o0, int p0, size_t o1, int p1, size_t o2, int p2, unsigned tag) {
    const int lane = threadIdx.x & 63, k = (lane >> 3) % 3;
    const gu32* p = (const gu32*)(ws + (k == 0 ? o0 : k == 1 ? o1 : o2)) + (k == 0 ? p0 : k == 1 ? p1 : p2) * 32 + (lane & 7);
    const unsigned long long t0 = __builtin_amdgcn_s_memrealtime();
    for (;;) {
        if (__all(__hip_atomic_load(p, RLX_AGENT) == tag)) break;
        if (__builtin_amdgcn_s_memrealtime() - t0 > WAIT_TICKS) break;
        __builtin_amdgcn_s_sleep(2);
    }
}
struct StatRow { u32x4 v0, v1; };
__device__ __forceinline__ StatRow row_stats_load(const Args& a, int w, int lane) {
    const int row = blockIdx.x * 8 + (w & 7);
    const rsrc_t rx = mk_rsrc(a.x + (size_t)row * D, w < 8 ? D * 2 : 0);
    StatRow r;
    r.v0 = bldu4<0>(rx, lane * 16, 0); r.v1 = bldu4<0>(rx, 1024 + lane * 16, 0);
    return r;
}
__device__ __forceinline__ void row_stats(const Args& a, const StatRow& r, unsigned tag, int w, int lane) {
    if (w < 8) {
        float s1 = 0.f, s2 = 0.f;
#pragma unroll
        for (int k = 0; k < 4; ++k) {
            s1 = dot2bf(r.v0[k], 0x3f803f80u, s1); s2 = dot2bf(r.v0[k], r.v0[k], s2);
            s1 = dot2bf(r.v1[k], 0x3f803f80u, s1); s2 = dot2bf(r.v1[k], r.v1[k], s2);
        }
        s1 = rsum_64(s1); s2 = rsum_64(s2);
        const float mean = s1 * (1.0f / D), var = fmaxf(s2 * (1.0f / D) - mean * mean, 0.f);
        const unsigned val = (pk_bf16(mean, ln_sd(var)));
        if (lane == 0) __hip_atomic_store((gu64*)(a.ws + WS_GR) + blockIdx.x * 8 + w, ((unsigned long long)tag << 32) | val, RLX_AGENT);
    }
}

struct PrepRegs { u32x4 R[36]; };
__device__ __forceinline__ void prep_issue(const Args& a, int job, int lane, PrepRegs& P) {
    const int sub = job / 18, rem = job % 18, kh = rem / 9, ns = rem % 9;
    const u16* src; int ld;
    if (sub == 0) { src = a.w1; ld = D; }
    else if (sub == 1) { src = a.w2; ld = D; }
    else if ((sub & 1) == 0) { src = a.w3 + ((sub - 2) >> 1) * D; ld = F; }
    else { src = a.w4 + (size_t)((sub - 2) >> 1) * D * D; ld = D; }
    src += (size_t)(kh * 288) * ld + ns * 64;
    const rsrc_t rs = mk_rsrc(src, 288 * ld * 2);
    const int ldb = ld * 2;
    const int vo = (lane >> 3) * ldb + (lane & 7) * 16;
#pragma unroll
    for (int i = 0; i < 36; ++i) P.R[i] = bldu4<0>(rs, vo, i * 8 * ldb);
    __builtin_amdgcn_sched_barrier(0);
}
template <bool PUBLISH, bool COLSUM>
__device__ __forceinline__ void prep_finish(const Args& a, int job, LAS char* lds, int lane, unsigned tag, const PrepRegs& P) {
    const int sub = job / 18, rem = job % 18, kh = rem / 9, ns = rem % 9;
    const rsrc_t rd = mk_rsrc(a.ws + WS_WF + (size_t)sub * WSUB * 2, WSUB * 2);
    const int fl = (lane >> 4) & 1;
    const int w0 = (lane >> 3) * 128 + (((lane & 7) ^ (fl << 1)) * 16), w1 = (lane >> 3) * 128 + (((lane & 7) ^ ((fl | 2) << 1)) * 16);
    const int g = lane >> 4, i16 = lane & 15, q = i16 >> 2, p = lane & 3;
    const int f = ((q >> 1) & 1) | ((g & 1) << 1);
    LAS char* rb[4];
#pragma unroll
    for (int n = 0; n < 4; ++n) rb[n] = lds + ((8 * g + q) * 128 + (((2 * n + (p >> 1)) ^ (f << 1)) * 16) + 8 * (p & 1));
    LAS char* const wp0 = lds + w0;
    LAS char* const wp1 = lds + w1;
    const bf16x8 ones = {0x3f80, 0x3f80, 0x3f80, 0x3f80, 0x3f80, 0x3f80, 0x3f80, 0x3f80};
    f32x4 cs[4] = {};
#pragma unroll
    for (int u = 0; u < 9; ++u) {
        const int so = (u & 1) * 4096;
#pragma unroll
        for (int i = 0; i < 4; ++i) *(LAS u32x4*)(((i & 1) ? wp1 : wp0) + (so + i * 1024)) = P.R[4 * u + i];
#pragma unroll
        for (int n = 0; n < 4; ++n) {
            const bf16x8 x = cat8(tr_read(rb[n] + so), tr_read(rb[n] + (so + 512)));
            bst16(__builtin_bit_cast(u32x4, x), rd, lane * 16, ((kh * 9 + u) * 36 + ns * 4 + n) * 1024);
            if (COLSUM) cs[n] = MFMA16(ones, x, cs[n]);
        }
    }
    if (COLSUM && (sub & 1) == 0) {
        const rsrc_t rc = mk_rsrc(a.ws + WS_CS, 2 * (D + F) * 4);
#pragma unroll
        for (int n = 0; n < 4; ++n)
            if (lane < 16) bst4(__float_as_uint(cs[n][0]), rc, lane * 4, (kh * (D + F) + (sub == 0 ? 0 : D + ((sub - 2) >> 1) * D) + ns * 64 + n * 16) * 4);
    }
    if (PUBLISH) {
        VM0();
        if (lane == 0) __hip_atomic_store((gu32*)(a.ws + WS_FW) + job, tag, RLX_AGENT);
    }
}

constexpr int CV_RC = 4160;
constexpr int CV_U = 4 * CV_RC;
constexpr int CV_X = CV_U + 2288 * 2;
constexpr int CV_RED = CV_X + 4096;
constexpr int CV_CH = CV_RED + 24576;
constexpr int KG_ROW = 544;
constexpr int KG_XI = 32 * KG_ROW, KG_T = CV_RED + 4096;

struct cpx { float re, im; };
__device__ __forceinline__ cpx cmul(cpx a, cpx b) { return {a.re * b.re - a.im * b.im, a.re * b.im + a.im * b.re}; }
__device__ __forceinline__ cpx cexp_rev(float a2, float rev, float m) {
    const float e = __builtin_amdgcn_exp2f(a2 * m), p = __builtin_amdgcn_fractf(rev * m);
    return {e * __builtin_amdgcn_cosf(p), e * __builtin_amdgcn_sinf(p)};
}
__device__ __forceinline__ unsigned pk_f16(cpx c) { f32x2 v = {c.re, c.im}; return __builtin_bit_cast(unsigned, __builtin_convertvector(v, f16x2)); }

__device__ __forceinline__ void conv_put(LAS char* lds, int m, u32x4 A, u32x2 B) {
    u32x4 c1 = {__builtin_amdgcn_alignbit(A[1], A[0], 16), __builtin_amdgcn_alignbit(A[2], A[1], 16), __builtin_amdgcn_alignbit(A[3], A[2], 16), __builtin_amdgcn_alignbit(B[0], A[3], 16)};
    u32x4 c2 = {A[1], A[2], A[3], B[0]};
    u32x4 c3 = {c1[1], c1[2], c1[3], __builtin_amdgcn_alignbit(B[1], B[0], 16)};
    *(LAS u32x4*)(lds + 0 * CV_RC + m * 16) = A;
    *(LAS u32x4*)(lds + 1 * CV_RC + m * 16) = c1;
    *(LAS u32x4*)(lds + 2 * CV_RC + m * 16) = c2;
    *(LAS u32x4*)(lds + 3 * CV_RC + m * 16) = c3;
}

constexpr int LDS_FWOK = 3 * CV_CH;
__device__ __forceinline__ void phase_conv(const Args& a, LAS char* lds0, unsigned tagv, const StatRow& sr, unsigned& tag_out) {
    const int tid = threadIdx.x, w = __builtin_amdgcn_readfirstlane(tid >> 6), lane = tid & 63;
    const int b = blockIdx.x, chl = w >> 2, wq = w & 3, t8 = tid & 255, ch = 3 * b + chl;
    LAS char* lds = lds0 + chl * CV_CH;
    const int part = wq >> 1, n = (wq & 1) * 64 + lane;
    const int o = ch * NS + n, o2 = ch * NS + (n ^ 64);
    const u16 r_ld = a.logdt[ch], r_ar = a.are[o], r_ai = a.aim[o], r_ar2 = a.are[o2];
    u16 r_b[4] = {}, r_c[4] = {};
    if (part) { r_b[0] = a.bre[o]; r_b[1] = a.bim[o]; r_c[0] = a.cre[o]; r_c[1] = a.cim[o]; r_b[2] = a.bre[o2]; r_b[3] = a.bim[o2]; r_c[2] = a.cre[o2]; r_c[3] = a.cim[o2]; }
    const u16 r_ds = a.dskip[ch];
    __builtin_amdgcn_sched_barrier(0);
    const unsigned tag = __builtin_amdgcn_readfirstlane(tagv) + 1u;
    tag_out = tag;
    if (tid == 0) __hip_atomic_store((gu32*)(a.ws + WS_FA) + b, tag, RLX_AGENT);
    row_stats(a, sr, tag, w, lane);
    __builtin_amdgcn_sched_barrier(0);
    u32x2 xr[4];
    const rsrc_t rx = mk_rsrc(a.x, L * D * 2);
    const int xvo = tid * (2 * D * 2) + ((6 * b) & ~3);
#define GATHER(i) do { __builtin_amdgcn_sched_barrier(0); if ((i) < 2 || w < 4) xr[i] = bldu2<0>(rx, xvo + ((i) & 1) * D * 2, ((i) >> 1) * 1536 * D * 2); __builtin_amdgcn_sched_barrier(0); } while (0)
    GATHER(0);
    const float dt = rbf(expf(bf2f(r_ld)));
    const float are = dt * bf2f(r_ar);
    const float amax = fmaxf(are, dt * bf2f(r_ar2));
    float ap = 0.f;
    if (__any(amax > 0.f)) ap = rmax_64(fmaxf(amax, 0.f));
    const float a2 = (are - ap) * 1.4426950408889634f, rev = dt * bf2f(r_ai) * 0.15915494309189535f;
    float kscale = 1.f;
    if (part == 0) {
        cpx e[4];
        const cpx g8 = cexp_rev(a2, rev, 8.f), g32 = cexp_rev(a2, rev, 32.f);
        e[0] = {1.f, 0.f}; e[1] = g8; e[2] = cmul(g8, g8); e[3] = cmul(e[2], g8);
        LAS char* er = lds + n * 4;
#pragma unroll
        for (int t = 0; t < 8; ++t) {
            if (t == 0) GATHER(1);
            if (t == 3) GATHER(2);
            if (t == 6) GATHER(3);
#pragma unroll
            for (int c = 0; c < 4; ++c) {
                *(LAS unsigned*)(er + (4 * t + c) * KG_ROW) = pk_f16(e[c]);
                if (t < 7) e[c] = (t == 0 && c == 0) ? g32 : cmul(e[c], g32);
            }
        }
    } else {
        const float b0 = dt * bf2f(r_b[0]), b1 = dt * bf2f(r_b[1]), c0 = bf2f(r_c[0]), c1 = bf2f(r_c[1]);
        const float b2 = dt * bf2f(r_b[2]), b3 = dt * bf2f(r_b[3]), c2 = bf2f(r_c[2]), c3 = bf2f(r_c[3]);
        const float wr = c0 * b0 - c1 * b1, wi = c0 * b1 + c1 * b0, vr = c2 * b2 - c3 * b3, vi = c2 * b3 + c3 * b2;
        const float wm = rmax_64(fmaxf(fmaxf(fabsf(wr), fabsf(wi)), fmaxf(fabsf(vr), fabsf(vi))));
        const unsigned ex = (__float_as_uint(wm) >> 23) & 0xffu;
        const unsigned exc = ex < 2u ? 2u : (ex > 252u ? 252u : ex);
        const float sc = __uint_as_float((254u - exc) << 23);
        kscale = __uint_as_float(exc << 23);
        const cpx W = {wr * sc, -wi * sc};
        const cpx g1 = cexp_rev(a2, -rev, 1.f), p256 = cexp_rev(a2, -rev, 256.f);
        const cpx g2 = cmul(g1, g1), g4 = cmul(g2, g2), p512 = cmul(p256, p256);
        LAS char* xr_ = lds + KG_XI + n * 4;
#define XST(i, v) *(LAS unsigned*)(xr_ + (i) * KG_ROW) = pk_f16(v)
        GATHER(1);
        cpx x = W; XST(0, x);
#pragma unroll
        for (int i = 1; i < 8; ++i) { x = cmul(x, g1); XST(i, x); }
        GATHER(2);
        x = cmul(W, p256); XST(8, x);
#pragma unroll
        for (int i = 9; i < 12; ++i) { x = cmul(x, g2); XST(i, x); }
        cpx y = cmul(W, p512); XST(12, y); XST(13, cmul(y, g4));
        x = cmul(y, p256); XST(14, x); XST(15, cmul(x, g4));
        GATHER(3);
        y = cmul(y, p512); XST(16, y);
#pragma unroll
        for (int i = 17; i < 21; ++i) { y = cmul(y, p256); XST(i, y); }
#undef XST
#undef GATHER
        if (lane == 0 && wq == 2) *(LAS float*)(lds + KG_T - 16) = kscale;
    }
    const int p = lane & 15, g = lane >> 4;
    LAS float* T = (LAS float*)(lds + KG_T);
    int base, idx0, istr;
    if (wq & 1) {
        base = p < 4 ? 1024 + 256 * p : (p == 4 ? 2048 : 0);
        idx0 = p < 4 ? 512 + 32 * p : (p == 4 ? 640 : 680);
        istr = 1;
    } else {
        const int q4 = p & 3;
        base = p < 8 ? p : (p < 12 ? 256 + 2 * q4 : 512 + 4 * (p & 1) + 128 * (p & 2));
        idx0 = p < 8 ? p : (p < 12 ? 256 + q4 : 384 + (p & 1) + 32 * (p & 2));
        istr = p < 8 ? 8 : (p < 12 ? 4 : 2);
    }
    const int j0 = (wq >> 1) * 16 + 4 * g;
    LAS float* tw = T + idx0 + j0 * istr;
    const LAS char* ea = lds + ((wq >> 1) * 16 + p) * KG_ROW + g * 16;
    const LAS char* xa = lds + KG_XI + ((wq & 1) * 16 + p) * KG_ROW + g * 16;
    LGKM0(); BAR();
    {
        f32x4 acc = {};
        f16x8 A[8], B[8];
#pragma unroll
        for (int t = 0; t < 8; ++t) { A[t] = *(const LAS f16x8*)(ea + t * 64); B[t] = *(const LAS f16x8*)(xa + t * 64); }
        float ks = *(const LAS float*)(lds + KG_T - 16);
        __builtin_amdgcn_sched_barrier(0);
#pragma unroll
        for (int t = 0; t < 8; ++t) acc = __builtin_amdgcn_mfma_f32_16x16x32_f16(A[t], B[t], acc, 0, 0, 0);
        if (ap > 0.f) {
#pragma unroll
            for (int q = 0; q < 4; ++q) acc[q] *= __builtin_amdgcn_exp2f(ap * 1.4426950408889634f * (float)(base + 8 * (j0 + q)));
        }
#pragma unroll
        for (int q = 0; q < 4; ++q) tw[q * istr] = acc[q] * ks;
    }
    LGKM0(); BAR();
    {
        float v[12];
        float lf = (float)(2047 - t8 * 8);
        asm volatile("" : "+v"(lf));
        const unsigned tb = (unsigned)(size_t)T - 17024u * 4u;
        if (wq < 3) {
#pragma unroll
            for (int e = 0; e < 12; e += 2) {
                const unsigned bits = pk_bf16(lf - (float)e, lf - (float)(e + 1));
                v[e] = *(const LAS float*)(size_t)(((bits & 0xffffu) << 2) + tb);
                v[e + 1] = *(const LAS float*)(size_t)(((bits >> 16) << 2) + tb);
            }
        } else {
#pragma unroll
            for (int e = 0; e < 12; ++e) {
                const int l = 2047 - (t8 * 8 + e);
                const int idx = l < 128 ? (l < 0 ? 0 : l) : (int)(pk_bf16((float)l, 0.f) & 0xffffu) - 17024;
                const float t = T[idx];
                v[e] = l < 0 ? 0.f : t;
            }
        }
        conv_put(lds, t8, (u32x4){pk_bf16(v[0], v[1]), pk_bf16(v[2], v[3]), pk_bf16(v[4], v[5]), pk_bf16(v[6], v[7])}, (u32x2){pk_bf16(v[8], v[9]), pk_bf16(v[10], v[11])});
        if (t8 < 4) conv_put(lds, 256 + t8, (u32x4){0u, 0u, 0u, 0u}, (u32x2){0u, 0u});
        if (t8 >= 64 && t8 < 94) *(LAS u32x4*)(lds + CV_U + (t8 - 64) * 16) = (u32x4){0u, 0u, 0u, 0u};
    }
    {
        u32x4 gv[2];
        const rsrc_t rgn = mk_rsrc(a.ws + WS_GR, L * 8);
        const unsigned long long t0 = __builtin_amdgcn_s_memrealtime();
        for (;;) {
            gv[0] = bldu4<SC1>(rgn, tid * 16, 0);
            gv[1] = w < 4 ? bldu4<SC1>(rgn, tid * 16, 1536 * 8) : gv[0];
            const bool ok = gv[0][1] == tag && gv[0][3] == tag && gv[1][1] == tag && gv[1][3] == tag;
            if (__all(ok)) break;
            if (__builtin_amdgcn_s_memrealtime() - t0 > WAIT_TICKS) break;
            __builtin_amdgcn_s_sleep(2);
            asm volatile("" ::: "memory");
        }
#pragma unroll
        for (int i = 0; i < 2; ++i) {
            if (i == 0 || w < 4) {
                const int row = 2 * tid + 1536 * i;
                u32x2 x0 = xr[2 * i], x1 = xr[2 * i + 1];
                if (b & 1) {
                    x0 = (u32x2){__builtin_amdgcn_alignbit(x0[1], x0[0], 16), x0[1] >> 16};
                    x1 = (u32x2){__builtin_amdgcn_alignbit(x1[1], x1[0], 16), x1[1] >> 16};
                }
                const float m0 = bf_lo(gv[i][0]), m1 = bf_lo(gv[i][2]);
                const float s0 = __builtin_amdgcn_rcpf(bf_hi(gv[i][0])), s1 = __builtin_amdgcn_rcpf(bf_hi(gv[i][2]));
#pragma unroll
                for (int c = 0; c < 3; ++c) {
                    const unsigned xp = c == 0 ? __builtin_amdgcn_perm(x1[0], x0[0], 0x05040100u) : c == 1 ? __builtin_amdgcn_perm(x1[0], x0[0], 0x07060302u)
                                                                                                            : __builtin_amdgcn_perm(x1[1], x0[1], 0x05040100u);
                    const unsigned dd = pk_bf16(bf_lo(xp) - m0, bf_hi(xp) - m1);
                    *(LAS unsigned*)(lds0 + c * CV_CH + CV_U + 480 + row * 2) = pk_bf16(bf_lo(dd) * s0, bf_hi(dd) * s1);
                    *(LAS unsigned*)(lds0 + c * CV_CH + CV_X + row * 2) = xp;
                }
            }
        }
    }
    LGKM0(); BAR();
    unsigned fwv = tag;
    if (w == 11 && lane < 36) fwv = __hip_atomic_load((gu32*)(a.ws + WS_FW) + lane, RLX_AGENT);
    f32x4 acc[8];
#pragma unroll
    for (int c = 0; c < 8; ++c) acc[c] = (f32x4){0.f, 0.f, 0.f, 0.f};
    const int cpy = (3 - p) & 3;
#pragma unroll
    for (int rr = 0; rr < 2; ++rr) {
        const int r = 2 * wq + rr;
        const LAS char* ab = lds + cpy * CV_RC + 2 * (2031 - 32 * r + 8 * g - p - cpy) - 512 * 7;
        const LAS char* fb = lds + CV_U + 2 * (240 - 32 * r + 16 * p - 16 + 8 * g);
        bf16x8 A[8], Fm[8];
#pragma unroll
        for (int s = 0; s < 8; ++s) {
            const s16x4 lo = *(const volatile LAS s16x4*)(ab + 512 * (7 - s));
            const s16x4 hi = *(const volatile LAS s16x4*)(ab + 512 * (7 - s) + 8);
            A[s] = cat8(lo, hi);
        }
#pragma unroll
        for (int e = 0; e < 8; ++e) Fm[e] = *(const LAS bf16x8*)(fb + 512 * e);
#pragma unroll
        for (int c = 0; c < 8; ++c)
#pragma unroll
            for (int s = 0; s <= c; ++s) acc[c] = MFMA16(A[s], Fm[c - s], acc[c]);
    }
    LAS f32x4* red = (LAS f32x4*)(lds + CV_RED) + lane;
    f32x4 own[2];
#define RED_CASE(WQ) { _Pragma("unroll") for (int c = 0; c < 8; ++c) if ((c >> 1) != WQ) red[((c >> 1) * 6 + (c & 1) * 3 + (WQ > (c >> 1) ? WQ - 1 : WQ)) * 64] = acc[c]; \
                       own[0] = acc[2 * WQ]; own[1] = acc[2 * WQ + 1]; }
    if (wq == 0) RED_CASE(0) else if (wq == 1) RED_CASE(1) else if (wq == 2) RED_CASE(2) else RED_CASE(3)
#undef RED_CASE
    if (w == 11) { const bool okw = __all(fwv == tag); if (lane == 0) *(LAS unsigned*)(lds0 + LDS_FWOK) = okw ? 1u : 0u; }
    LGKM0(); BAR();
    const rsrc_t rh = mk_rsrc(a.ws + WS_HT + (size_t)ch * L * 2, L * 2);
    const float dsk = bf2f(r_ds);
#pragma unroll
    for (int i = 0; i < 2; ++i) {
        f32x4 y = own[i];
#pragma unroll
        for (int k = 0; k < 3; ++k) y += red[(wq * 6 + i * 3 + k) * 64];
        const int l0 = 256 * (2 * wq + i) + 16 * p + 4 * g;
        const u32x2 uu = *(const LAS u32x2*)(lds + CV_U + 480 + 2 * l0), xv = *(const LAS u32x2*)(lds + CV_X + 2 * l0);
        float o[4];
#pragma unroll
        for (int k = 0; k < 4; ++k) {
            const float u = (k & 1) ? bf_hi(uu[k >> 1]) : bf_lo(uu[k >> 1]);
            const float x = (k & 1) ? bf_hi(xv[k >> 1]) : bf_lo(xv[k >> 1]);
            o[k] = x + __builtin_fmaf(u, dsk, y[k]);
        }
        bst8((u32x2){pk_bf16(o[0], o[1]), pk_bf16(o[2], o[3])}, rh, 2 * l0, 0);
    }
    VM0(); BAR();
    if (w == 0) publish_rep(a.ws, WS_FH, b, tag, lane);
}

__device__ __forceinline__ int nb0_of(int j) { return (9 * j + 1) >> 1; }

constexpr int OUT_ROW = 176;
constexpr int KC_XB = 73728, KC_ST = KC_XB + 20 * 1024, KC_OUT = KC_ST + 512;
__device__ __forceinline__ void phase_c(char* __restrict__ ws, LAS char* lds, int panel, int j, unsigned tag) {
    const int tid = threadIdx.x, w = __builtin_amdgcn_readfirstlane(tid >> 6), lane = tid & 63;
    const int tok0 = panel * 64, nbs = nb0_of(j), nblk = nb0_of(j + 1) - nbs;
    const int g = lane >> 4, i16 = lane & 15, q = i16 >> 2, p = lane & 3;
    const int b = w >> 1, kh = w & 1;
    const bool act = w < 10 && b < nblk;
    const int nb = nbs + (act ? b : 0);
    if (w == 0 && *(const LAS unsigned*)(lds + LDS_FWOK) == 0u) wait_flags(ws, WS_FW, 0, 36, WS_FW, 0, 0, tag);
    BAR();
    bf16x8 W[9];
    f32x4 cs0, cs1;
    if (w < 10) {
        const rsrc_t rw = mk_rsrc(ws + WS_WF, WSUB * 2);
#pragma unroll
        for (int i = 0; i < 9; ++i) W[i] = bld16<SC1>(rw, lane * 16, ((kh * 9 + i) * 36 + nb) * 1024);
        const rsrc_t rc = mk_rsrc(ws + WS_CS, 2 * (D + F) * 4);
        cs0 = bldf4<SC1>(rc, g * 16, nb * 64); cs1 = bldf4<SC1>(rc, g * 16, nb * 64 + (D + F) * 4);
    }
    if (w == 11) wait_flags(ws, WS_FH, (blockIdx.x & (REP - 1)) * 256, 192, WS_FH, 0, 0, tag);
    BAR();
    {
        const rsrc_t rh = mk_rsrc(ws + WS_HT, D * L * 2);
        const int fl = (lane >> 4) & 1;
        const int v0 = (lane >> 3) * 4096 + (((lane & 7) ^ (fl << 1)) * 16), v1 = (lane >> 3) * 4096 + (((lane & 7) ^ ((fl | 2) << 1)) * 16);
#pragma unroll
        for (int k = 0; k < 6; ++k) { const int idx = 6 * w + k; blds16<SC1>(rh, lds + idx * 1024, (k & 1) ? v1 : v0, idx * 32768 + tok0 * 2); }
    }
    VM0(); BAR();
    const int f = ((q >> 1) & 1) | ((g & 1) << 1);
    const int lo_ = (8 * g + q) * 128 + 8 * (p & 1);
    f32x4 acc[4] = {};
    LAS f32x2* st = (LAS f32x2*)(lds + KC_ST);
    LAS f32x4* xb = (LAS f32x4*)(lds + KC_XB);
    if (w < 10) {
        const LAS char* hb[4];
#pragma unroll
        for (int mt = 0; mt < 4; ++mt) hb[mt] = lds + kh * 9 * 4096 + lo_ + (((2 * mt + (p >> 1)) ^ (f << 1)) * 16);
        bf16x8 H[2][2][4];
#pragma unroll
        for (int c = 0; c < 6; ++c) {
            if (c < 5) {
#pragma unroll
                for (int i = 0; i < 2; ++i)
                    if (2 * c + i < 9) {
#pragma unroll
                        for (int mt = 0; mt < 4; ++mt) H[c & 1][i][mt] = cat8(tr_read(hb[mt] + (2 * c + i) * 4096), tr_read(hb[mt] + (2 * c + i) * 4096 + 512));
                    }
            }
            __builtin_amdgcn_sched_barrier(0);
            if (c > 0) {
#pragma unroll
                for (int i = 0; i < 2; ++i)
                    if (2 * (c - 1) + i < 9) {
#pragma unroll
                        for (int mt = 0; mt < 4; ++mt) acc[mt] = MFMA16(W[2 * (c - 1) + i], H[(c - 1) & 1][i][mt], acc[mt]);
                    }
            }
            __builtin_amdgcn_sched_barrier(0);
        }
        xb[((w * 2 + 0) * 64) + lane] = sel4(kh, acc[0], acc[2]);
        xb[((w * 2 + 1) * 64) + lane] = sel4(kh, acc[1], acc[3]);
    } else {
        __builtin_amdgcn_s_setprio(3);
        const bf16x8 ones = {0x3f80, 0x3f80, 0x3f80, 0x3f80, 0x3f80, 0x3f80, 0x3f80, 0x3f80};
        const int m0 = 2 * (w - 10);
        const LAS char* h0 = lds + lo_ + (((2 * m0 + (p >> 1)) ^ (f << 1)) * 16);
        const LAS char* h1 = lds + lo_ + (((2 * m0 + 2 + (p >> 1)) ^ (f << 1)) * 16);
        bf16x8 H[2][3][2];
#pragma unroll
        for (int c = 0; c < 7; ++c) {
            if (c < 6) {
#pragma unroll
                for (int i = 0; i < 3; ++i) {
                    H[c & 1][i][0] = cat8(tr_read(h0 + (3 * c + i) * 4096), tr_read(h0 + (3 * c + i) * 4096 + 512));
                    H[c & 1][i][1] = cat8(tr_read(h1 + (3 * c + i) * 4096), tr_read(h1 + (3 * c + i) * 4096 + 512));
                }
            }
            __builtin_amdgcn_sched_barrier(0);
            if (c > 0) {
#pragma unroll
                for (int i = 0; i < 3; ++i) {
                    const bf16x8 H0 = H[(c - 1) & 1][i][0], H1 = H[(c - 1) & 1][i][1];
                    acc[0] = MFMA16(ones, H0, acc[0]); acc[1] = MFMA16(H0, H0, acc[1]);
                    acc[2] = MFMA16(ones, H1, acc[2]); acc[3] = MFMA16(H1, H1, acc[3]);
                }
            }
            __builtin_amdgcn_sched_barrier(0);
        }
        const float sm[2] = {acc[0][0], acc[2][0]};
        if (g == q) {
            const rsrc_t r2 = mk_rsrc(ws + WS_ST2, L * 8);
#pragma unroll
            for (int t = 0; t < 2; ++t) {
                const f32x4 d = acc[2 * t + 1];
                const float s2 = p == 0 ? d[0] : p == 1 ? d[1] : p == 2 ? d[2] : d[3];
                const float mean = sm[t] * (1.0f / D), var = fmaxf(s2 * (1.0f / D) - mean * mean, 0.f);
                const float mb = rbf(mean), rs = __builtin_amdgcn_rcpf(ln_sd(var));
                const float m2 = rbf((mean - mb) * rs), rs2 = __builtin_amdgcn_rcpf(ln_sd(var * rs * rs));
                st[(m0 + t) * 16 + i16] = (f32x2){rs * rs2, (mb * rs + m2) * rs2};
                if (j == 0) bst8((u32x2){__float_as_uint(mb), __float_as_uint(rs)}, r2, (tok0 + (m0 + t) * 16 + i16) * 8, 0);
            }
        }
    }
    __builtin_amdgcn_s_setprio(0);
    LGKM0(); BAR();
    const rsrc_t rg = mk_rsrc(ws + WS_G, (L + 4) * D * 2);
    LAS char* og = lds + KC_OUT;
    if (act) {
        const f32x4 cs = cs0 + cs1;
#pragma unroll
        for (int t = 0; t < 2; ++t) {
            const f32x4 a = sel4(kh, acc[2 + t], acc[t]) + xb[((w ^ 1) * 2 + t) * 64 + lane];
            const f32x2 ab = st[(2 * kh + t) * 16 + i16];
            float o[4];
#pragma unroll
            for (int k = 0; k < 4; ++k) o[k] = gelu_tanh(ab[0] * a[k] - ab[1] * cs[k]);
            *(LAS u32x2*)(og + (kh * 32 + t * 16 + i16) * OUT_ROW + b * 32 + g * 8) = (u32x2){pk_bf16(o[0], o[1]), pk_bf16(o[2], o[3])};
        }
    }
    LGKM0(); BAR();
    {
        const int nc = 2 * nblk, row = nblk == 5 ? tid / 10 : tid >> 3, c = tid - row * nc;
        if (row < 64) bst16(*(const LAS u32x4*)(og + row * OUT_ROW + c * 16), rg, row * (D * 2) + c * 16, ((2 + tok0) * D + nbs * 16) * 2);
        if ((panel == 0 || panel == 31) && tid >= 704 && tid < 704 + 2 * nc) {
            const int z = tid - 704, zr = z >= nc ? 1 : 0;
            bst16((u32x4){0u, 0u, 0u, 0u}, rg, zr * (D * 2) + (z - zr * nc) * 16, ((panel == 0 ? 0 : L + 2) * D + nbs * 16) * 2);
        }
    }
    VM0(); BAR();
    publish(ws, WS_FG, panel * 32 + j, tag);
}

constexpr int KD_HT = 73728, KD_XB = KD_HT + 10240, KD_OUT = KD_XB + 20 * 1024;
__device__ __forceinline__ void phase_d(char* __restrict__ ws, LAS char* lds, int panel, int j, int je, unsigned tag) {
    const int tid = threadIdx.x, w = __builtin_amdgcn_readfirstlane(tid >> 6), lane = tid & 63;
    const int tok0 = panel * 64, nbs = nb0_of(j), nblk = nb0_of(j + 1) - nbs;
    const int g = lane >> 4, i16 = lane & 15, q = i16 >> 2, p = lane & 3;
    const int b = w >> 1, kh = w & 1;
    const bool act = w < 10 && b < nblk;
    const int nb = nbs + (act ? b : 0);
    bf16x8 W[9];
    if (w < 10) {
        const rsrc_t rw = mk_rsrc(ws + WS_WF + WSUB * 2, WSUB * 2);
#pragma unroll
        for (int i = 0; i < 9; ++i) W[i] = bld16<SC1>(rw, lane * 16, ((kh * 9 + i) * 36 + nb) * 1024);
    }
    if (w < 2 * nblk) {
        const rsrc_t rh = mk_rsrc(ws + WS_HT, D * L * 2);
        const int fl = ((lane >> 4) & 1) | ((w & 1) << 1);
        blds16<SC1>(rh, lds + KD_HT + w * 1024, (lane >> 3) * 4096 + (((lane & 7) ^ (fl << 1)) * 16), (nbs * 16 + w * 8) * 4096 + tok0 * 2);
    }
    if (w == 11) {
        const int p0 = panel > 0 ? panel - 1 : 0, p1 = panel < 31 ? panel + 1 : 31;
        wait_panels(ws, WS_FG, p0, WS_FG, panel, WS_FG, p1, tag);
    }
    BAR();
    f32x2 st[2];
    {
        const rsrc_t rg = mk_rsrc(ws + WS_G, (L + 4) * D * 2);
        const int c7 = (lane & 7) ^ ((lane >> 3) & 6), vb = (lane >> 3) * 1152 + c7 * 16;
#pragma unroll
        for (int k = 0; k < 6; ++k) {
            const int idx = 6 * w + k, cg = idx >> 3, c = cg * 8 + c7;
            const int o2 = (0x4031 >> (((c * 57) >> 10) * 4)) & 15;
            blds16<SC1>(rg, lds + idx * 1024, vb + o2 * 1152, (tok0 + (idx & 7) * 8) * 1152 + cg * 128);
        }
        const rsrc_t r2 = mk_rsrc(ws + WS_ST2, L * 8);
#pragma unroll
        for (int t = 0; t < 2; ++t) st[t] = __builtin_bit_cast(f32x2, bldu2<SC1>(r2, i16 * 8, (tok0 + kh * 32 + t * 16) * 8));
    }
    VM0();
    asm volatile("" : "+v"(st[0]), "+v"(st[1]));
    BAR();
    f32x4 acc[4] = {};
    LAS f32x4* xb = (LAS f32x4*)(lds + KD_XB);
    if (w < 10) {
        const int z = i16 & 6;
        const LAS char* a0 = lds + i16 * 128 + ((g ^ z) * 16), * a1 = lds + i16 * 128 + (((4 + g) ^ z) * 16);
        const LAS char* e0 = kh ? a1 + 4 * 8192 : a0;
        const LAS char* e1 = kh ? a0 + 5 * 8192 : a1;
        bf16x8 A[2][2][4];
#pragma unroll
        for (int c = 0; c < 6; ++c) {
            if (c < 5) {
#pragma unroll
                for (int ii = 0; ii < 2; ++ii) {
                    const int i = 2 * c + ii;
                    if (i < 9) {
#pragma unroll
                        for (int mt = 0; mt < 4; ++mt) A[c & 1][ii][mt] = *(const LAS bf16x8*)(((i & 1) ? e1 : e0) + (i >> 1) * 8192 + mt * 2048);
                    }
                }
            }
            __builtin_amdgcn_sched_barrier(0);
            if (c > 0) {
#pragma unroll
                for (int ii = 0; ii < 2; ++ii)
                    if (2 * (c - 1) + ii < 9) {
#pragma unroll
                        for (int mt = 0; mt < 4; ++mt) acc[mt] = MFMA16(W[2 * (c - 1) + ii], A[(c - 1) & 1][ii][mt], acc[mt]);
                    }
            }
            __builtin_amdgcn_sched_barrier(0);
        }
        xb[((w * 2 + 0) * 64) + lane] = sel4(kh, acc[0], acc[2]);
        xb[((w * 2 + 1) * 64) + lane] = sel4(kh, acc[1], acc[3]);
    }
    LGKM0(); BAR();
    LAS char* ox = lds + KD_OUT;
    if (w < 10) {
        const int bl = act ? b : 0;
        const int hrow = 16 * bl + 4 * g + q;
        const LAS char* hb = lds + KD_HT + hrow * 128 + 8 * (p & 1);
        const rsrc_t rp = mk_rsrc(ws + WS_PST, 32 * 36 * 64 * 8);
#pragma unroll
        for (int t = 0; t < 2; ++t) {
            const int mt = 2 * kh + t;
            const f32x4 a = sel4(kh, acc[2 + t], acc[t]) + xb[((w ^ 1) * 2 + t) * 64 + lane];
            const u32x2 hu = __builtin_bit_cast(u32x2, tr_read(hb + (((2 * mt + (p >> 1)) ^ (fsw(hrow) << 1)) * 16)));
            const float rs = st[t][1], nm = -st[t][0] * rs;
            u32x2 ov;
#pragma unroll
            for (int k = 0; k < 2; ++k)
                ov[k] = pk_bf16(__builtin_fmaf(bf_lo(hu[k]), rs, nm) + a[2 * k], __builtin_fmaf(bf_hi(hu[k]), rs, nm) + a[2 * k + 1]);
            float s1 = dot2bf(ov[1], 0x3f803f80u, dot2bf(ov[0], 0x3f803f80u, 0.f)), s2 = dot2bf(ov[1], ov[1], dot2bf(ov[0], ov[0], 0.f));
            s1 = rsum_g(s1); s2 = rsum_g(s2);
            if (act) {
                *(LAS u32x2*)(ox + (mt * 16 + i16) * OUT_ROW + b * 32 + g * 8) = ov;
                if (lane < 16) bst8((u32x2){__float_as_uint(s1), __float_as_uint(s2)}, rp, lane * 8, ((panel * 36 + nb) * 64 + mt * 16) * 8);
            }
        }
    }
    LGKM0(); BAR();
    {
        const int nc = 2 * nblk, row = nblk == 5 ? tid / 10 : tid >> 3, c = tid - row * nc;
        if (row < 64) {
            const int so = (tok0 * D + nbs * 16) * 2, vo = row * (D * 2) + c * 16;
            bst16(*(const LAS u32x4*)(ox + row * OUT_ROW + c * 16), mk_rsrc(ws + WS_X1, L * D * 2), vo, so);
        }
    }
    if (w == 10) wait_flags(ws, WS_FW, (2 + 2 * je) * 18, 18, WS_FW, 0, 0, tag);
    VM0(); BAR();
    publish(ws, WS_FX, panel * 32 + j, tag);
}

constexpr int BG_LDS = 131072;
__device__ __forceinline__ void phase_cd_background(const Args& a, LAS char* lds, int panel, int job, unsigned tag) {
    char* ws = a.ws;
    const int tid = threadIdx.x, w = __builtin_amdgcn_readfirstlane(tid >> 6), lane = tid & 63, tok0 = panel * 64;
    BAR(); BAR();
    {
        const rsrc_t rh = mk_rsrc(ws + WS_HT, D * L * 2);
        const int fl = (lane >> 4) & 1;
        const int v0 = (lane >> 3) * 4096 + (((lane & 7) ^ (fl << 1)) * 16), v1 = (lane >> 3) * 4096 + (((lane & 7) ^ ((fl | 2) << 1)) * 16);
#pragma unroll
        for (int k = 0; k < 6; ++k) { const int idx = 6 * w + k; blds16<SC1>(rh, lds + idx * 1024, (k & 1) ? v1 : v0, idx * 32768 + tok0 * 2); }
    }
    VM0(); BAR();
    PrepRegs P;
    prep_issue(a, job, lane, P);
    BAR(); BAR(); BAR();
    prep_finish<false, false>(a, job, lds + BG_LDS + (w - 8) * 8192, lane, tag, P);
    BAR();
    {
        const rsrc_t rg = mk_rsrc(ws + WS_G, (L + 4) * D * 2);
        const int c7 = (lane & 7) ^ ((lane >> 3) & 6), vb = (lane >> 3) * 1152 + c7 * 16;
#pragma unroll
        for (int k = 0; k < 6; ++k) {
            const int idx = 6 * w + k, cg = idx >> 3, c = cg * 8 + c7;
            const int o2 = (0x4031 >> (((c * 57) >> 10) * 4)) & 15;
            blds16<SC1>(rg, lds + idx * 1024, vb + o2 * 1152, (tok0 + (idx & 7) * 8) * 1152 + cg * 128);
        }
    }
    VM0();
    if (lane == 0) __hip_atomic_store((gu32*)(ws + WS_FW) + job, tag, RLX_AGENT);
    BAR(); BAR(); BAR(); BAR();
}

constexpr int KE_HID = 73728, KE_ST = 2 * 73728, KE_FLG = KE_ST + 1024;
constexpr int NB = 3, RING = 4;
__device__ __forceinline__ void phase_e(char* __restrict__ ws, LAS char* lds, int panel, int j, unsigned tag) {
    const int w = __builtin_amdgcn_readfirstlane(threadIdx.x >> 6), lane = threadIdx.x & 63;
    constexpr int S1 = 6, S2 = 12;
    const int tok0 = panel * 64, g = lane >> 4, i16 = lane & 15, nbs = NB * w;
    const rsrc_t rw = mk_rsrc(ws + WS_WF + (size_t)(2 + 2 * j) * WSUB * 2, 2 * WSUB * 2);
    const int wv = lane * 16, wsb = nbs * 1024;
    bf16x8 W[RING][NB];
#pragma unroll
    for (int s = 0; s < RING; ++s)
#pragma unroll
        for (int b = 0; b < NB; ++b) W[s][b] = bld16<SC1>(rw, wv + b * 1024, wsb + s * 36864);
    f32x4 acc[NB][4];
    {
        const rsrc_t rc = mk_rsrc(ws + WS_CS, 2 * (D + F) * 4);
#pragma unroll
        for (int b = 0; b < NB; ++b) {
            acc[b][0] = bldf4<SC1>(rc, g * 16 + b * 64, (D + j * D + nbs * 16) * 4);
            acc[b][1] = bldf4<SC1>(rc, g * 16 + b * 64, (D + j * D + nbs * 16 + D + F) * 4);
        }
    }
    __builtin_amdgcn_sched_barrier(0);
    unsigned fw4 = tag;
    if (w == 10 && lane < 18) fw4 = __hip_atomic_load((gu32*)(ws + WS_FW) + (3 + 2 * j) * 18 + lane, RLX_AGENT);
    if (w == 11) wait_panels(ws, WS_FX, panel, WS_FX, panel, WS_FX, panel, tag);
    BAR();
    u32x2 pv[36];
    if (w == 11) {
        const rsrc_t rp = mk_rsrc(ws + WS_PST + (size_t)panel * 36 * 512, 36 * 512);
#pragma unroll
        for (int k = 0; k < 36; ++k) pv[k] = bldu2<SC1>(rp, lane * 8, k * 512);
    }
    {
        const rsrc_t rx = mk_rsrc(ws + WS_X1 + (size_t)tok0 * D * 2, 64 * D * 2);
        const int vo = (lane >> 3) * 1152 + (((lane & 7) ^ ((lane >> 3) & 6)) * 16);
#pragma unroll
        for (int k = 0; k < 6; ++k) {
            const int idx = 6 * w + k;
            blds16<SC1>(rx, lds + idx * 1024, vo, (idx & 7) * 9216 + (idx >> 3) * 128);
        }
    }
    __builtin_amdgcn_sched_barrier(0);
    LAS f32x4* st = (LAS f32x4*)(lds + KE_ST);
    LAS unsigned* flg = (LAS unsigned*)(lds + KE_FLG);
    if (w == 11) {
        float a1[4] = {}, a2[4] = {};
#pragma unroll
        for (int k = 0; k < 36; ++k) { a1[k & 3] += __uint_as_float(pv[k][0]); a2[k & 3] += __uint_as_float(pv[k][1]); }
        const float s1 = (a1[0] + a1[1]) + (a1[2] + a1[3]), s2 = (a2[0] + a2[1]) + (a2[2] + a2[3]);
        const float mean = s1 * (1.0f / D), var = fmaxf(s2 * (1.0f / D) - mean * mean, 0.f);
        const float mb = rbf(mean), sb = ln_sd(var), al = __builtin_amdgcn_rcpf(sb);
        const float k0 = -2.0f * 0.7978845608028654f * 1.4426950408889634f;
        st[lane] = (f32x4){k0 * al, k0 * 0.044715f * al * al * al, sb, -mb};
        if (lane < 3) flg[lane] = 0u;
    }
    if (w == 10) { const bool ok4 = __all(fw4 == tag); if (lane == 0) flg[3] = ok4 ? 1u : 0u; }
    VM0(); LGKM0(); BAR();
    const int z = i16 & 6;
    const int o0 = i16 * 128 + (g ^ z) * 16, o1 = i16 * 128 + ((4 + g) ^ z) * 16;
    {
        float nm[4];
#pragma unroll
        for (int mt = 0; mt < 4; ++mt) nm[mt] = st[mt * 16 + i16][3];
#pragma unroll
        for (int b = 0; b < NB; ++b) {
            const f32x4 cs = acc[b][0] + acc[b][1];
#pragma unroll
            for (int mt = 0; mt < 4; ++mt) acc[b][mt] = cs * nm[mt];
        }
    }
#define E_SPIN(cond) do { while (cond) __builtin_amdgcn_s_sleep(1); __builtin_amdgcn_sched_barrier(0); asm volatile("" ::: "memory"); } while (0)
#define E_FLG(i) __hip_atomic_load(flg + (i), __ATOMIC_RELAXED, __HIP_MEMORY_SCOPE_WORKGROUP)
    bf16x8 A[2][4];
#pragma unroll
    for (int ph = 0; ph < 2; ++ph) {
        const LAS char* ab = lds + ph * KE_HID;
        if (ph == 1) E_SPIN(E_FLG(0) < 4u);
#pragma unroll
        for (int mt = 0; mt < 4; ++mt) A[0][mt] = *(const LAS bf16x8*)(ab + o0 + mt * 2048);
#pragma unroll
        for (int s = 0; s < 18; ++s) {
            if (ph == 1 && s + 1 == S1) E_SPIN(E_FLG(1) < 4u);
            if (ph == 1 && s + 1 == S2) E_SPIN(E_FLG(2) < 4u);
            if (ph == 0 && s == 2 && w == 8 && E_FLG(3) == 0u) {
                wait_flags(ws, WS_FW, (3 + 2 * j) * 18, 18, WS_FW, 0, 0, tag);
                if (lane == 0) __hip_atomic_store(flg + 3, 1u, __ATOMIC_RELAXED, __HIP_MEMORY_SCOPE_WORKGROUP);
            }
            if (ph == 0 && s == 18 - RING) E_SPIN(E_FLG(3) == 0u);
            if (s + 1 < 18) {
#pragma unroll
                for (int mt = 0; mt < 4; ++mt) A[(s + 1) & 1][mt] = *(const LAS bf16x8*)(ab + (((s + 1) & 1) ? o1 : o0) + mt * 2048 + ((s + 1) >> 1) * 8192);
            }
            const int t = ph * 18 + s;
#pragma unroll
            for (int b = 0; b < NB; ++b) {
#pragma unroll
                for (int mt = 0; mt < 4; ++mt) acc[b][mt] = MFMA16(W[t % RING][b], A[s & 1][mt], acc[b][mt]);
                if (t + RING < 36) W[t % RING][b] = bld16<SC1>(rw, wv + b * 1024, wsb + (t + RING) * 36864);
            }
            __builtin_amdgcn_sched_barrier(0);
        }
        if (ph == 0) {
#pragma unroll
            for (int mt = 0; mt < 4; ++mt) {
                const f32x4 k = st[mt * 16 + i16];
#pragma unroll
                for (int b = 0; b < NB; ++b) {
                    float o[4];
#pragma unroll
                    for (int e = 0; e < 4; ++e) {
                        const float a = acc[b][mt][e];
                        const float ex = __builtin_amdgcn_exp2f(a * (k[0] + k[1] * (a * a)));
                        o[e] = a * __builtin_amdgcn_rcpf(k[2] + k[2] * ex);
                    }
                    const int nb = nbs + b;
                    *(LAS u32x2*)(lds + KE_HID + (nb >> 2) * 8192 + (mt * 16 + i16) * 128 + (((2 * (nb & 3) + (g >> 1)) ^ z) * 16) + 8 * (g & 1)) =
                        (u32x2){pk_bf16(o[0], o[1]), pk_bf16(o[2], o[3])};
                    acc[b][mt] = (f32x4){0.f, 0.f, 0.f, 0.f};
                }
            }
            LGKM0();
            if (lane == 0) __hip_atomic_fetch_add(flg + (w >> 2), 1u, __ATOMIC_RELAXED, __HIP_MEMORY_SCOPE_WORKGROUP);
            if (w >= 8) __builtin_amdgcn_s_setprio(2); else if (w >= 4) __builtin_amdgcn_s_setprio(1);
        }
    }
#undef E_SPIN
#undef E_FLG
    __builtin_amdgcn_s_setprio(0);
    const rsrc_t rp = mk_rsrc(ws + WS_PART + (size_t)(panel * 8 + j) * 72 * 1024, 72 * 1024);
#pragma unroll
    for (int b = 0; b < NB; ++b)
#pragma unroll
        for (int m2 = 0; m2 < 2; ++m2) {
            const f32x4 a0 = acc[b][2 * m2], a1 = acc[b][2 * m2 + 1];
            const u32x4 v = {pk_bf16(a0[0], a0[1]), pk_bf16(a0[2], a0[3]), pk_bf16(a1[0], a1[1]), pk_bf16(a1[2], a1[3])};
            const int pr = (nbs + b) * 2 + m2;
            if (pr >= 9 * j && pr < 9 * j + 9) *(LAS u32x4*)(lds + (pr - 9 * j) * 1024 + lane * 16) = v;
            else bst16(v, rp, lane * 16, pr * 1024);
        }
    LGKM0();
    VM0(); BAR();
    publish(ws, WS_FP, panel * 32 + j, tag);
}

constexpr int KF_HT = 16384;
__device__ __forceinline__ void phase_f(char* __restrict__ ws, LAS char* lds, u16* __restrict__ out, int panel, int jj, unsigned tag) {
    const int tid = threadIdx.x, w = __builtin_amdgcn_readfirstlane(tid >> 6), lane = tid & 63;
    const int g = lane >> 4, i16 = lane & 15, q = i16 >> 2, p = lane & 3;
    const int pr = jj * 9 + (w < 9 ? w : 0), nb = pr >> 1, m2 = pr & 1, nbs = (jj * 9) >> 1;
    u32x2 xa[2];
    const int so = ((panel * 64 + m2 * 32) * D + nb * 16) * 2, vo = (i16 * D + 4 * g) * 2;
    if (w < 10) {
        const rsrc_t rh = mk_rsrc(ws + WS_HT, D * L * 2);
        const int fl = ((lane >> 4) & 1) | ((w & 1) << 1);
        blds16<SC1>(rh, lds + KF_HT + w * 1024, (lane >> 3) * 4096 + (((lane & 7) ^ (fl << 1)) * 16), (nbs * 16 + w * 8) * 4096 + panel * 128);
    }
    if (w < 9) {
        const rsrc_t r1 = mk_rsrc(ws + WS_X1, L * D * 2);
#pragma unroll
        for (int t = 0; t < 2; ++t) xa[t] = bldu2<SC1>(r1, vo, so + t * 16 * D * 2);
    }
    if (w == 11) wait_panels(ws, WS_FP, panel, WS_FP, panel, WS_FP, panel, tag);
    VM0(); BAR();
    if (w < 9) {
        const rsrc_t rp = mk_rsrc(ws + WS_PART + (size_t)(panel * 8) * 72 * 1024, 8 * 72 * 1024);
        u32x4 v[8];
#pragma unroll
        for (int k = 0; k < 7; ++k) v[k] = bldu4<SC1>(rp, lane * 16, ((k < jj ? k : k + 1) * 72 + pr) * 1024);
        v[7] = *(const LAS u32x4*)(lds + w * 1024 + lane * 16);
        const int hrow = 16 * (nb - nbs) + 4 * g + q;
        u32x2 ha[2];
#pragma unroll
        for (int t = 0; t < 2; ++t)
            ha[t] = __builtin_bit_cast(u32x2, tr_read(lds + KF_HT + hrow * 128 + (((2 * (2 * m2 + t) + (p >> 1)) ^ (fsw(hrow) << 1)) * 16) + 8 * (p & 1)));
        float s[8] = {};
#pragma unroll
        for (int k = 0; k < 8; ++k)
#pragma unroll
            for (int e = 0; e < 4; ++e) { s[2 * e] += bf_lo(v[k][e]); s[2 * e + 1] += bf_hi(v[k][e]); }
#pragma unroll
        for (int t = 0; t < 2; ++t) {
            float o[4];
#pragma unroll
            for (int k = 0; k < 4; ++k) {
                const float x1 = (k & 1) ? bf_hi(xa[t][k >> 1]) : bf_lo(xa[t][k >> 1]);
                const float h = (k & 1) ? bf_hi(ha[t][k >> 1]) : bf_lo(ha[t][k >> 1]);
                o[k] = h + (x1 + s[4 * t + k]);
            }
            __builtin_amdgcn_raw_buffer_store_b64((u32x2){pk_bf16(o[0], o[1]), pk_bf16(o[2], o[3])}, mk_rsrc(out, L * D * 2), vo, so + t * 16 * D * 2, 0);
        }
    }
}

constexpr int LDS_CTRL = 3 * CV_CH > KE_FLG + 32 ? 3 * CV_CH : KE_FLG + 32;
constexpr int LDS_BYTES = LDS_CTRL + 64;

NOPK __global__ __launch_bounds__(NTHR) void mega_kernel(Args a) {
    extern __shared__ __attribute__((aligned(16))) char smem[];
    LAS char* lds = (LAS char*)smem;
    const int tid = threadIdx.x, w = __builtin_amdgcn_readfirstlane(tid >> 6), lane = tid & 63, b = blockIdx.x;
    unsigned cpf = 0;
    if (w == 8) {
        unsigned long long pc;
        asm volatile("s_getpc_b64 %0" : "=s"(pc));
        const int line = (b >> 3) * 13 + lane;
        if (lane < 13 && line * 128 < CODE_PF) cpf = *(const GAS unsigned*)((pc & ~255ull) + (unsigned long long)line * 128);
    }
    const unsigned tagv = __hip_atomic_load((gu32*)(a.ws + WS_EPOCH), RLX_AGENT);
    const StatRow sr = row_stats_load(a, w, lane);
    __builtin_amdgcn_sched_barrier(0);
    unsigned tag;
    if (b < 192) phase_conv(a, lds, tagv, sr, tag);
    else {
        tag = __builtin_amdgcn_readfirstlane(tagv) + 1u;
        if (tid == 0) __hip_atomic_store((gu32*)(a.ws + WS_FA) + b, tag, RLX_AGENT);
        if (tid == 0) *(LAS unsigned*)(lds + LDS_FWOK) = 0u;
        row_stats(a, sr, tag, w, lane);
        LGKM0(); BAR();
        if (w >= 3) {
            const int pg = (((b - 192) >> 3) * 9 + (w - 3)) * 64 + lane;
            unsigned tv = 0;
            if (pg * 65536 < (int)WS_END) tv = __hip_atomic_load((gu32*)(a.ws + (size_t)pg * 65536), RLX_AGENT);
            else if (pg < 1024 && (pg - 960) >= 0 && (pg - 960) * 65536 < L * D * 2) tv = __hip_atomic_load((gu32*)((char*)a.out + (size_t)(pg - 960) * 65536), RLX_AGENT);
            asm volatile("" :: "v"(tv));
        }
        const int i = w * 64 + (b - 192);
        if (i >= 36) {
            const unsigned long long t0 = __builtin_amdgcn_s_memrealtime();
            while (__builtin_amdgcn_s_memrealtime() - t0 < PREP_DELAY) __builtin_amdgcn_s_sleep(8);
        }
        if (i < 180) {
            const int job = i < 36 ? i : (2 + 2 * ((i - 36) / 18)) * 18 + (i - 36) % 18;
            PrepRegs P;
            prep_issue(a, job, lane, P);
            prep_finish<true, true>(a, job, lds + w * 8192, lane, tag, P);
        }
        BAR();
    }
    asm volatile("" :: "v"(cpf));
    const int je = 2 * (b & 3) + ((b >> 3) & 1);
    {
        const int panel = (b & 7) * 4 + (b >> 6), j = (b >> 3) & 7;
        const int m = (w - 8) * 128 + ((b & 7) | ((b >> 4) << 3));
        if ((j & 1) && (w == 8 || w == 9) && m < 144) phase_cd_background(a, lds, panel, (3 + 2 * (m / 18)) * 18 + m % 18, tag);
        else {
            phase_c(a.ws, lds, panel, j, tag);
            phase_d(a.ws, lds, panel, j, je, tag);
        }
    }
    phase_e(a.ws, lds, ((b >> 2) & 1) * 16 + (b >> 4), je, tag);
    phase_f(a.ws, lds, a.out, ((b >> 2) & 1) * 16 + (b >> 4), je, tag);
    if (b == 0 && w == 10) {
        wait_flags(a.ws, WS_FA, 0, 256, WS_FA, 0, 0, tag);
        if (lane == 0) __hip_atomic_store((gu32*)(a.ws + WS_EPOCH), tag, RLX_AGENT);
    }
}

extern "C" void kernel_launch(void* const* d_in, const int* in_sizes, int n_in,
                              void* d_out, int out_size, void* d_ws, size_t ws_size,
                              hipStream_t stream) {
    Args a;
    a.x = (const u16*)d_in[0];
    a.are = (const u16*)d_in[1]; a.aim = (const u16*)d_in[2];
    a.bre = (const u16*)d_in[3]; a.bim = (const u16*)d_in[4];
    a.cre = (const u16*)d_in[5]; a.cim = (const u16*)d_in[6];
    a.logdt = (const u16*)d_in[7]; a.dskip = (const u16*)d_in[8];
    a.w1 = (const u16*)d_in[10]; a.w2 = (const u16*)d_in[11];
    a.w3 = (const u16*)d_in[12]; a.w4 = (const u16*)d_in[13];
    a.ws = (char*)d_ws; a.out = (u16*)d_out;
    (void)hipFuncSetAttribute((const void*)mega_kernel, hipFuncAttributeMaxDynamicSharedMemorySize, LDS_BYTES);
    mega_kernel<<<NWG, NTHR, LDS_BYTES, stream>>>(a);
}
```

```cpp
#include <hip/hip_runtime.h>
#include <hip/hip_bf16.h>
#include <stdint.h>

typedef unsigned short u16;
typedef short bf16x8 __attribute__((ext_vector_type(8)));
typedef short s16x4 __attribute__((ext_vector_type(4)));
typedef float f32x2 __attribute__((ext_vector_type(2)));
typedef float f32x4 __attribute__((ext_vector_type(4)));
typedef float f32x16 __attribute__((ext_vector_type(16)));
typedef unsigned u32x2 __attribute__((ext_vector_type(2)));
typedef unsigned u32x4 __attribute__((ext_vector_type(4)));
typedef __bf16 bf16x2_t __attribute__((ext_vector_type(2)));
typedef _Float16 f16x2 __attribute__((ext_vector_type(2)));
typedef _Float16 f16x8 __attribute__((ext_vector_type(8)));
#define LAS __attribute__((address_space(3)))
#define GAS __attribute__((address_space(1)))
#if defined(__HIP_DEVICE_COMPILE__)
#define NOPK __attribute__((target("no-packed-fp32-ops")))
#else
#define NOPK
#endif

constexpr int L = 2048, D = 576, NS = 128, F = 4608;
constexpr int WSUB = D * D;
constexpr float LN_EPS = 1e-5f;
constexpr int NWG = 256, NTHR = 768;
constexpr int SC1 = 16;

constexpr size_t WS_EPOCH = 0;
constexpr int REP = 8;
constexpr size_t WS_FA = 256;
constexpr size_t WS_FW = WS_FA + 1024;
constexpr size_t WS_FG = WS_FW + 2048;
constexpr size_t WS_FX = WS_FG + 4096;
constexpr size_t WS_FP = WS_FX + 4096;
constexpr size_t WS_FH = WS_FP + 4096;
constexpr size_t WS_GR = 32768;
constexpr size_t WS_HT = WS_GR + (size_t)L * 8;
constexpr size_t WS_WF = WS_HT + (size_t)D * L * 2;
constexpr size_t WS_CS = WS_WF + (size_t)18 * WSUB * 2;
constexpr size_t WS_ST2 = WS_CS + (size_t)2 * (D + F) * 4;
constexpr size_t WS_G = WS_ST2 + (size_t)L * 8;
constexpr size_t WS_X1 = WS_G + (size_t)(L + 4) * D * 2;
constexpr size_t WS_PST = WS_X1 + (size_t)L * D * 2;
constexpr size_t WS_PART = WS_PST + (size_t)32 * 36 * 64 * 8;
constexpr size_t WS_END = WS_PART + (size_t)32 * 8 * 72 * 512 * 2;

__device__ __forceinline__ unsigned pk_bf16(float lo, float hi) {
    f32x2 v = {lo, hi};
    return __builtin_bit_cast(unsigned, __builtin_convertvector(v, bf16x2_t));
}
__device__ __forceinline__ float bf_lo(unsigned p) { return __uint_as_float(p << 16); }
__device__ __forceinline__ float bf_hi(unsigned p) { return __uint_as_float(p & 0xffff0000u); }
__device__ __forceinline__ float bf2f(u16 x) { return __uint_as_float((unsigned)x << 16); }
__device__ __forceinline__ float rbf(float x) { return bf_lo(pk_bf16(x, x)); }
__device__ __forceinline__ float dot2bf(unsigned a, unsigned b, float c) {
    return __builtin_amdgcn_fdot2_f32_bf16(__builtin_bit_cast(bf16x2_t, a), __builtin_bit_cast(bf16x2_t, b), c, false);
}
__device__ __forceinline__ float gelu_tanh(float x) {
    const float c0 = -2.0f * 0.7978845608028654f * 1.4426950408889634f;
    const float c1 = c0 * 0.044715f;
    float e = __builtin_amdgcn_exp2f(x * (c0 + c1 * x * x));
    return x * __builtin_amdgcn_rcpf(1.0f + e);
}
__device__ __forceinline__ s16x4 tr_read(const LAS char* p) {
    return __builtin_amdgcn_ds_read_tr16_b64_v4i16((LAS s16x4*)p);
}
__device__ __forceinline__ bf16x8 cat8(s16x4 a, s16x4 b) {
    return __builtin_shufflevector(a, b, 0, 1, 2, 3, 4, 5, 6, 7);
}
__device__ __forceinline__ f32x4 sel4(bool c, f32x4 a, f32x4 b) {
    f32x4 r;
#pragma unroll
    for (int k = 0; k < 4; ++k) { float x = a[k], y = b[k]; asm volatile("" : "+v"(x), "+v"(y)); r[k] = c ? x : y; }
    return r;
}
__device__ __forceinline__ float rsum_g(float x) {
    const unsigned u = __float_as_uint(x);
    const auto a = __builtin_amdgcn_permlane16_swap(u, u, false, false);
    const float y = __uint_as_float(a[0]) + __uint_as_float(a[1]);
    const unsigned v = __float_as_uint(y);
    const auto b = __builtin_amdgcn_permlane32_swap(v, v, false, false);
    return __uint_as_float(b[0]) + __uint_as_float(b[1]);
}
template <int CTRL> __device__ __forceinline__ float dpp_f(float x) {
    return __uint_as_float(__builtin_amdgcn_update_dpp(0u, __float_as_uint(x), CTRL, 0xf, 0xf, false));
}
__device__ __forceinline__ float rsum_64(float x) {
    x += dpp_f<0xB1>(x);
    x += dpp_f<0x4E>(x);
    x += dpp_f<0x141>(x);
    x += dpp_f<0x140>(x);
    return rsum_g(x);
}
template <int CTRL> __device__ __forceinline__ unsigned dpp_u(unsigned x) { return __builtin_amdgcn_update_dpp(0u, x, CTRL, 0xf, 0xf, false); }
__device__ __forceinline__ unsigned umax2(unsigned a, unsigned b) { return a > b ? a : b; }
__device__ __forceinline__ float rmax_64(float xf) {
    unsigned x = __float_as_uint(xf);
    x = umax2(x, dpp_u<0xB1>(x)); x = umax2(x, dpp_u<0x4E>(x)); x = umax2(x, dpp_u<0x141>(x)); x = umax2(x, dpp_u<0x140>(x));
    const auto a = __builtin_amdgcn_permlane16_swap(x, x, false, false); x = umax2(a[0], a[1]);
    const auto b = __builtin_amdgcn_permlane32_swap(x, x, false, false); x = umax2(b[0], b[1]);
    return __uint_as_float(x);
}
__device__ __forceinline__ float ln_sd(float var) { return rbf(__builtin_amdgcn_sqrtf(rbf(rbf(var) + LN_EPS))); }
__device__ __forceinline__ int fsw(int row) { return ((row >> 1) & 1) | (((row >> 3) & 1) << 1); }
#define MFMA16(a, b, c) __builtin_amdgcn_mfma_f32_16x16x32_bf16((a), (b), (c), 0, 0, 0)
#define MFMA32(a, b, c) __builtin_amdgcn_mfma_f32_32x32x16_bf16((a), (b), (c), 0, 0, 0)
#define VM0() asm volatile("s_waitcnt vmcnt(0)" ::: "memory")
#define LGKM0() asm volatile("s_waitcnt lgkmcnt(0)" ::: "memory")
#define BAR() do { asm volatile("" ::: "memory"); __builtin_amdgcn_s_barrier(); asm volatile("" ::: "memory"); } while (0)

typedef __amdgpu_buffer_rsrc_t rsrc_t;
__device__ __forceinline__ rsrc_t mk_rsrc(const void* p, unsigned bytes) { return __builtin_amdgcn_make_buffer_rsrc((void*)p, 0, bytes, 0x00020000); }
template <int AUX> __device__ __forceinline__ bf16x8 bld16(rsrc_t r, int voff, int soff) {
    return __builtin_bit_cast(bf16x8, __builtin_amdgcn_raw_buffer_load_b128(r, voff, soff, AUX));
}
template <int AUX> __device__ __forceinline__ f32x4 bldf4(rsrc_t r, int voff, int soff) {
    return __builtin_bit_cast(f32x4, __builtin_amdgcn_raw_buffer_load_b128(r, voff, soff, AUX));
}
template <int AUX> __device__ __forceinline__ u32x4 bldu4(rsrc_t r, int voff, int soff) {
    return __builtin_amdgcn_raw_buffer_load_b128(r, voff, soff, AUX);
}
template <int AUX> __device__ __forceinline__ u32x2 bldu2(rsrc_t r, int voff, int soff) {
    return __builtin_amdgcn_raw_buffer_load_b64(r, voff, soff, AUX);
}
template <int AUX> __device__ __forceinline__ void blds16(rsrc_t r, LAS char* l, int voff, int soff) {
    __builtin_amdgcn_raw_ptr_buffer_load_lds(r, (LAS void*)l, 16, voff, soff, 0, AUX);
}
__device__ __forceinline__ void bst16(u32x4 v, rsrc_t r, int voff, int soff) { __builtin_amdgcn_raw_buffer_store_b128(v, r, voff, soff, SC1); }
__device__ __forceinline__ void bst8(u32x2 v, rsrc_t r, int voff, int soff) { __builtin_amdgcn_raw_buffer_store_b64(v, r, voff, soff, SC1); }
__device__ __forceinline__ void bst4(unsigned v, rsrc_t r, int voff, int soff) { __builtin_amdgcn_raw_buffer_store_b32(v, r, voff, soff, SC1); }

struct Args {
    const u16* x; const u16 *are, *aim, *bre, *bim, *cre, *cim, *logdt, *dskip;
    const u16 *w1, *w2, *w3, *w4;
    char* ws; u16* out;
};

typedef GAS unsigned gu32;
typedef GAS unsigned long long gu64;
#define RLX_AGENT __ATOMIC_RELAXED, __HIP_MEMORY_SCOPE_AGENT
constexpr unsigned long long PREP_DELAY = 280ull;
constexpr int CODE_PF = 41984;
constexpr unsigned long long WAIT_TICKS = 400000ull;
__device__ __forceinline__ void publish(char* ws, size_t flag_off, int idx, unsigned tag) {
    if (threadIdx.x == 0) __hip_atomic_store((gu32*)(ws + flag_off) + idx, tag, RLX_AGENT);
}
__device__ __forceinline__ void publish_rep(char* ws, size_t flag_off, int idx, unsigned tag, int lane) {
    if (lane < REP) __hip_atomic_store((gu32*)(ws + flag_off) + lane * 256 + idx, tag, RLX_AGENT);
}
__device__ __forceinline__ void wait_flags(char* ws, size_t offa, int a0, int na, size_t offb, int b0, int nb, unsigned tag) {
    const int lane = threadIdx.x & 63;
    const gu32* p[4]; bool on[4];
#pragma unroll
    for (int k = 0; k < 4; ++k) {
        const int i = lane + 64 * k;
        on[k] = i < na + nb;
        p[k] = i < na ? (const gu32*)(ws + offa) + a0 + i : (const gu32*)(ws + offb) + b0 + (on[k] ? i - na : 0);
    }
    const unsigned long long t0 = __builtin_amdgcn_s_memrealtime();
    for (;;) {
        bool ok = true;
#pragma unroll
        for (int k = 0; k < 4; ++k)
            if (64 * k < na + nb) { const unsigned v = __hip_atomic_load(p[k], RLX_AGENT); ok = ok && (!on[k] || v == tag); }
        if (__all(ok)) break;
        if (__builtin_amdgcn_s_memrealtime() - t0 > WAIT_TICKS) break;
        __builtin_amdgcn_s_sleep(2);
    }
}
__device__ __forceinline__ void wait_panels(char* ws, size_t o0, int p0, size_t o1, int p1, size_t o2, int p2, unsigned tag) {
    const int lane = threadIdx.x & 63, k = (lane >> 3) % 3;
    const gu32* p = (const gu32*)(ws + (k == 0 ? o0 : k == 1 ? o1 : o2)) + (k == 0 ? p0 : k == 1 ? p1 : p2) * 32 + (lane & 7);
    const unsigned long long t0 = __builtin_amdgcn_s_memrealtime();
    for (;;) {
        if (__all(__hip_atomic_load(p, RLX_AGENT) == tag)) break;
        if (__builtin_amdgcn_s_memrealtime() - t0 > WAIT_TICKS) break;
        __builtin_amdgcn_s_sleep(2);
    }
}
struct StatRow { u32x4 v0, v1; };
__device__ __forceinline__ StatRow row_stats_load(const Args& a, int w, int lane) {
    const int row = blockIdx.x * 8 + (w & 7);
    const rsrc_t rx = mk_rsrc(a.x + (size_t)row * D, w < 8 ? D * 2 : 0);
    StatRow r;
    r.v0 = bldu4<0>(rx, lane * 16, 0); r.v1 = bldu4<0>(rx, 1024 + lane * 16, 0);
    return r;
}
__device__ __forceinline__ void row_stats(const Args& a, const StatRow& r, unsigned tag, int w, int lane) {
    if (w < 8) {
        float s1 = 0.f, s2 = 0.f;
#pragma unroll
        for (int k = 0; k < 4; ++k) {
            s1 = dot2bf(r.v0[k], 0x3f803f80u, s1); s2 = dot2bf(r.v0[k], r.v0[k], s2);
            s1 = dot2bf(r.v1[k], 0x3f803f80u, s1); s2 = dot2bf(r.v1[k], r.v1[k], s2);
        }
        s1 = rsum_64(s1); s2 = rsum_64(s2);
        const float mean = s1 * (1.0f / D), var = fmaxf(s2 * (1.0f / D) - mean * mean, 0.f);
        const unsigned val = (pk_bf16(mean, ln_sd(var)));
        if (lane == 0) __hip_atomic_store((gu64*)(a.ws + WS_GR) + blockIdx.x * 8 + w, ((unsigned long long)tag << 32) | val, RLX_AGENT);
    }
}

struct PrepRegs { u32x4 R[36]; };
__device__ __forceinline__ void prep_issue(const Args& a, int job, int lane, PrepRegs& P) {
    const int sub = job / 18, rem = job % 18, kh = rem / 9, ns = rem % 9;
    const u16* src; int ld;
    if (sub == 0) { src = a.w1; ld = D; }
    else if (sub == 1) { src = a.w2; ld = D; }
    else if ((sub & 1) == 0) { src = a.w3 + ((sub - 2) >> 1) * D; ld = F; }
    else { src = a.w4 + (size_t)((sub - 2) >> 1) * D * D; ld = D; }
    src += (size_t)(kh * 288) * ld + ns * 64;
    const rsrc_t rs = mk_rsrc(src, 288 * ld * 2);
    const int ldb = ld * 2;
    const int vo = (lane >> 3) * ldb + (lane & 7) * 16;
#pragma unroll
    for (int i = 0; i < 36; ++i) P.R[i] = bldu4<0>(rs, vo, i * 8 * ldb);
    __builtin_amdgcn_sched_barrier(0);
}
template <bool PUBLISH, bool COLSUM>
__device__ __forceinline__ void prep_finish(const Args& a, int job, LAS char* lds, int lane, unsigned tag, const PrepRegs& P) {
    const int sub = job / 18, rem = job % 18, kh = rem / 9, ns = rem % 9;
    const rsrc_t rd = mk_rsrc(a.ws + WS_WF + (size_t)sub * WSUB * 2, WSUB * 2);
    const int fl = (lane >> 4) & 1;
    const int w0 = (lane >> 3) * 128 + (((lane & 7) ^ (fl << 1)) * 16), w1 = (lane >> 3) * 128 + (((lane & 7) ^ ((fl | 2) << 1)) * 16);
    const int g = lane >> 4, i16 = lane & 15, q = i16 >> 2, p = lane & 3;
    const int f = ((q >> 1) & 1) | ((g & 1) << 1);
    LAS char* rb[4];
#pragma unroll
    for (int n = 0; n < 4; ++n) rb[n] = lds + ((8 * g + q) * 128 + (((2 * n + (p >> 1)) ^ (f << 1)) * 16) + 8 * (p & 1));
    LAS char* const wp0 = lds + w0;
    LAS char* const wp1 = lds + w1;
    const bf16x8 ones = {0x3f80, 0x3f80, 0x3f80, 0x3f80, 0x3f80, 0x3f80, 0x3f80, 0x3f80};
    f32x4 cs[4] = {};
#pragma unroll
    for (int u = 0; u < 9; ++u) {
        const int so = (u & 1) * 4096;
#pragma unroll
        for (int i = 0; i < 4; ++i) *(LAS u32x4*)(((i & 1) ? wp1 : wp0) + (so + i * 1024)) = P.R[4 * u + i];
#pragma unroll
        for (int n = 0; n < 4; ++n) {
            const bf16x8 x = cat8(tr_read(rb[n] + so), tr_read(rb[n] + (so + 512)));
            bst16(__builtin_bit_cast(u32x4, x), rd, lane * 16, ((kh * 9 + u) * 36 + ns * 4 + n) * 1024);
            if (COLSUM) cs[n] = MFMA16(ones, x, cs[n]);
        }
    }
    if (COLSUM && (sub & 1) == 0) {
        const rsrc_t rc = mk_rsrc(a.ws + WS_CS, 2 * (D + F) * 4);
#pragma unroll
        for (int n = 0; n < 4; ++n)
            if (lane < 16) bst4(__float_as_uint(cs[n][0]), rc, lane * 4, (kh * (D + F) + (sub == 0 ? 0 : D + ((sub - 2) >> 1) * D) + ns * 64 + n * 16) * 4);
    }
    if (PUBLISH) {
        VM0();
        if (lane == 0) __hip_atomic_store((gu32*)(a.ws + WS_FW) + job, tag, RLX_AGENT);
    }
}

constexpr int CV_RC = 4160;
constexpr int CV_U = 4 * CV_RC;
constexpr int CV_X = CV_U + 2288 * 2;
constexpr int CV_RED = CV_X + 4096;
constexpr int CV_CH = CV_RED + 24576;
constexpr int KG_ROW = 544;
constexpr int KG_XI = 32 * KG_ROW, KG_T = CV_RED + 4096;

struct cpx { float re, im; };
__device__ __forceinline__ cpx cmul(cpx a, cpx b) { return {a.re * b.re - a.im * b.im, a.re * b.im + a.im * b.re}; }
__device__ __forceinline__ cpx cexp_rev(float a2, float rev, float m) {
    const float e = __builtin_amdgcn_exp2f(a2 * m), p = __builtin_amdgcn_fractf(rev * m);
    return {e * __builtin_amdgcn_cosf(p), e * __builtin_amdgcn_sinf(p)};
}
__device__ __forceinline__ unsigned pk_f16(cpx c) { f32x2 v = {c.re, c.im}; return __builtin_bit_cast(unsigned, __builtin_convertvector(v, f16x2)); }

__device__ __forceinline__ void conv_put(LAS char* lds, int m, u32x4 A, u32x2 B) {
    u32x4 c1 = {__builtin_amdgcn_alignbit(A[1], A[0], 16), __builtin_amdgcn_alignbit(A[2], A[1], 16), __builtin_amdgcn_alignbit(A[3], A[2], 16), __builtin_amdgcn_alignbit(B[0], A[3], 16)};
    u32x4 c2 = {A[1], A[2], A[3], B[0]};
    u32x4 c3 = {c1[1], c1[2], c1[3], __builtin_amdgcn_alignbit(B[1], B[0], 16)};
    *(LAS u32x4*)(lds + 0 * CV_RC + m * 16) = A;
    *(LAS u32x4*)(lds + 1 * CV_RC + m * 16) = c1;
    *(LAS u32x4*)(lds + 2 * CV_RC + m * 16) = c2;
    *(LAS u32x4*)(lds + 3 * CV_RC + m * 16) = c3;
}

constexpr int LDS_FWOK = 3 * CV_CH;
__device__ __forceinline__ void phase_conv(const Args& a, LAS char* lds0, unsigned tagv, const StatRow& sr, unsigned& tag_out) {
    const int tid = threadIdx.x, w = __builtin_amdgcn_readfirstlane(tid >> 6), lane = tid & 63;
    const int b = blockIdx.x, chl = w >> 2, wq = w & 3, t8 = tid & 255, ch = 3 * b + chl;
    LAS char* lds = lds0 + chl * CV_CH;
    const int part = wq >> 1, n = (wq & 1) * 64 + lane;
    const int o = ch * NS + n, o2 = ch * NS + (n ^ 64);
    const u16 r_ld = a.logdt[ch], r_ar = a.are[o], r_ai = a.aim[o], r_ar2 = a.are[o2];
    u16 r_b[4] = {}, r_c[4] = {};
    if (part) { r_b[0] = a.bre[o]; r_b[1] = a.bim[o]; r_c[0] = a.cre[o]; r_c[1] = a.cim[o]; r_b[2] = a.bre[o2]; r_b[3] = a.bim[o2]; r_c[2] = a.cre[o2]; r_c[3] = a.cim[o2]; }
    const u16 r_ds = a.dskip[ch];
    __builtin_amdgcn_sched_barrier(0);
    const unsigned tag = __builtin_amdgcn_readfirstlane(tagv) + 1u;
    tag_out = tag;
    if (tid == 0) __hip_atomic_store((gu32*)(a.ws + WS_FA) + b, tag, RLX_AGENT);
    row_stats(a, sr, tag, w, lane);
    __builtin_amdgcn_sched_barrier(0);
    u32x2 xr[4];
    const rsrc_t rx = mk_rsrc(a.x, L * D * 2);
    const int xvo = tid * (2 * D * 2) + ((6 * b) & ~3);
#define GATHER(i) do { __builtin_amdgcn_sched_barrier(0); if ((i) < 2 || w < 4) xr[i] = bldu2<0>(rx, xvo + ((i) & 1) * D * 2, ((i) >> 1) * 1536 * D * 2); __builtin_amdgcn_sched_barrier(0); } while (0)
    GATHER(0);
    const float dt = rbf(expf(bf2f(r_ld)));
    const float are = dt * bf2f(r_ar);
    const float amax = fmaxf(are, dt * bf2f(r_ar2));
    float ap = 0.f;
    if (__any(amax > 0.f)) ap = rmax_64(fmaxf(amax, 0.f));
    const float a2 = (are - ap) * 1.4426950408889634f, rev = dt * bf2f(r_ai) * 0.15915494309189535f;
    float kscale = 1.f;
    if (part == 0) {
        cpx e[4];
        const cpx g8 = cexp_rev(a2, rev, 8.f), g32 = cexp_rev(a2, rev, 32.f);
        e[0] = {1.f, 0.f}; e[1] = g8; e[2] = cmul(g8, g8); e[3] = cmul(e[2], g8);
        LAS char* er = lds + n * 4;
#pragma unroll
        for (int t = 0; t < 8; ++t) {
            if (t == 0) GATHER(1);
            if (t == 3) GATHER(2);
            if (t == 6) GATHER(3);
#pragma unroll
            for (int c = 0; c < 4; ++c) {
                *(LAS unsigned*)(er + (4 * t + c) * KG_ROW) = pk_f16(e[c]);
                if (t < 7) e[c] = (t == 0 && c == 0) ? g32 : cmul(e[c], g32);
            }
        }
    } else {
        const float b0 = dt * bf2f(r_b[0]), b1 = dt * bf2f(r_b[1]), c0 = bf2f(r_c[0]), c1 = bf2f(r_c[1]);
        const float b2 = dt * bf2f(r_b[2]), b3 = dt * bf2f(r_b[3]), c2 = bf2f(r_c[2]), c3 = bf2f(r_c[3]);
        const float wr = c0 * b0 - c1 * b1, wi = c0 * b1 + c1 * b0, vr = c2 * b2 - c3 * b3, vi = c2 * b3 + c3 * b2;
        const float wm = rmax_64(fmaxf(fmaxf(fabsf(wr), fabsf(wi)), fmaxf(fabsf(vr), fabsf(vi))));
        const unsigned ex = (__float_as_uint(wm) >> 23) & 0xffu;
        const unsigned exc = ex < 2u ? 2u : (ex > 252u ? 252u : ex);
        const float sc = __uint_as_float((254u - exc) << 23);
        kscale = __uint_as_float(exc << 23);
        const cpx W = {wr * sc, -wi * sc};
        const cpx g1 = cexp_rev(a2, -rev, 1.f), p256 = cexp_rev(a2, -rev, 256.f);
        const cpx g2 = cmul(g1, g1), g4 = cmul(g2, g2), p512 = cmul(p256, p256);
        LAS char* xr_ = lds + KG_XI + n * 4;
#define XST(i, v) *(LAS unsigned*)(xr_ + (i) * KG_ROW) = pk_f16(v)
        GATHER(1);
        cpx x = W; XST(0, x);
#pragma unroll
        for (int i = 1; i < 8; ++i) { x = cmul(x, g1); XST(i, x); }
        GATHER(2);
        x = cmul(W, p256); XST(8, x);
#pragma unroll
        for (int i = 9; i < 12; ++i) { x = cmul(x, g2); XST(i, x); }
        cpx y = cmul(W, p512); XST(12, y); XST(13, cmul(y, g4));
        x = cmul(y, p256); XST(14, x); XST(15, cmul(x, g4));
        GATHER(3);
        y = cmul(y, p512); XST(16, y);
#pragma unroll
        for (int i = 17; i < 21; ++i) { y = cmul(y, p256); XST(i, y); }
#undef XST
#undef GATHER
        if (lane == 0 && wq == 2) *(LAS float*)(lds + KG_T - 16) = kscale;
    }
    const int p = lane & 15, g = lane >> 4;
    LAS float* T = (LAS float*)(lds + KG_T);
    int base, idx0, istr;
    if (wq & 1) {
        base = p < 4 ? 1024 + 256 * p : (p == 4 ? 2048 : 0);
        idx0 = p < 4 ? 512 + 32 * p : (p == 4 ? 640 : 680);
        istr = 1;
    } else {
        const int q4 = p & 3;
        base = p < 8 ? p : (p < 12 ? 256 + 2 * q4 : 512 + 4 * (p & 1) + 128 * (p & 2));
        idx0 = p < 8 ? p : (p < 12 ? 256 + q4 : 384 + (p & 1) + 32 * (p & 2));
        istr = p < 8 ? 8 : (p < 12 ? 4 : 2);
    }
    const int j0 = (wq >> 1) * 16 + 4 * g;
    LAS float* tw = T + idx0 + j0 * istr;
    const LAS char* ea = lds + ((wq >> 1) * 16 + p) * KG_ROW + g * 16;
    const LAS char* xa = lds + KG_XI + ((wq & 1) * 16 + p) * KG_ROW + g * 16;
    LGKM0(); BAR();
    {
        f32x4 acc = {};
        f16x8 A[8], B[8];
#pragma unroll
        for (int t = 0; t < 8; ++t) { A[t] = *(const LAS f16x8*)(ea + t * 64); B[t] = *(const LAS f16x8*)(xa + t * 64); }
        float ks = *(const LAS float*)(lds + KG_T - 16);
        __builtin_amdgcn_sched_barrier(0);
#pragma unroll
        for (int t = 0; t < 8; ++t) acc = __builtin_amdgcn_mfma_f32_16x16x32_f16(A[t], B[t], acc, 0, 0, 0);
        if (ap > 0.f) {
#pragma unroll
            for (int q = 0; q < 4; ++q) acc[q] *= __builtin_amdgcn_exp2f(ap * 1.4426950408889634f * (float)(base + 8 * (j0 + q)));
        }
#pragma unroll
        for (int q = 0; q < 4; ++q) tw[q * istr] = acc[q] * ks;
    }
    LGKM0(); BAR();
    {
        float v[12];
        float lf = (float)(2047 - t8 * 8);
        asm volatile("" : "+v"(lf));
        const unsigned tb = (unsigned)(size_t)T - 17024u * 4u;
        if (wq < 3) {
#pragma unroll
            for (int e = 0; e < 12; e += 2) {
                const unsigned bits = pk_bf16(lf - (float)e, lf - (float)(e + 1));
                v[e] = *(const LAS float*)(size_t)(((bits & 0xffffu) << 2) + tb);
                v[e + 1] = *(const LAS float*)(size_t)(((bits >> 16) << 2) + tb);
            }
        } else {
#pragma unroll
            for (int e = 0; e < 12; ++e) {
                const int l = 2047 - (t8 * 8 + e);
                const int idx = l < 128 ? (l < 0 ? 0 : l) : (int)(pk_bf16((float)l, 0.f) & 0xffffu) - 17024;
                const float t = T[idx];
                v[e] = l < 0 ? 0.f : t;
            }
        }
        conv_put(lds, t8, (u32x4){pk_bf16(v[0], v[1]), pk_bf16(v[2], v[3]), pk_bf16(v[4], v[5]), pk_bf16(v[6], v[7])}, (u32x2){pk_bf16(v[8], v[9]), pk_bf16(v[10], v[11])});
        if (t8 < 4) conv_put(lds, 256 + t8, (u32x4){0u, 0u, 0u, 0u}, (u32x2){0u, 0u});
        if (t8 >= 64 && t8 < 94) *(LAS u32x4*)(lds + CV_U + (t8 - 64) * 16) = (u32x4){0u, 0u, 0u, 0u};
    }
    {
        u32x4 gv[2];
        const rsrc_t rgn = mk_rsrc(a.ws + WS_GR, L * 8);
        const unsigned long long t0 = __builtin_amdgcn_s_memrealtime();
        for (;;) {
            gv[0] = bldu4<SC1>(rgn, tid * 16, 0);
            gv[1] = w < 4 ? bldu4<SC1>(rgn, tid * 16, 1536 * 8) : gv[0];
            const bool ok = gv[0][1] == tag && gv[0][3] == tag && gv[1][1] == tag && gv[1][3] == tag;
            if (__all(ok)) break;
            if (__builtin_amdgcn_s_memrealtime() - t0 > WAIT_TICKS) break;
            __builtin_amdgcn_s_sleep(2);
            asm volatile("" ::: "memory");
        }
#pragma unroll
        for (int i = 0; i < 2; ++i) {
            if (i == 0 || w < 4) {
                const int row = 2 * tid + 1536 * i;
                u32x2 x0 = xr[2 * i], x1 = xr[2 * i + 1];
                if (b & 1) {
                    x0 = (u32x2){__builtin_amdgcn_alignbit(x0[1], x0[0], 16), x0[1] >> 16};
                    x1 = (u32x2){__builtin_amdgcn_alignbit(x1[1], x1[0], 16), x1[1] >> 16};
                }
                const float m0 = bf_lo(gv[i][0]), m1 = bf_lo(gv[i][2]);
                const float s0 = __builtin_amdgcn_rcpf(bf_hi(gv[i][0])), s1 = __builtin_amdgcn_rcpf(bf_hi(gv[i][2]));
#pragma unroll
                for (int c = 0; c < 3; ++c) {
                    const unsigned xp = c == 0 ? __builtin_amdgcn_perm(x1[0], x0[0], 0x05040100u) : c == 1 ? __builtin_amdgcn_perm(x1[0], x0[0], 0x07060302u)
                                                                                                            : __builtin_amdgcn_perm(x1[1], x0[1], 0x05040100u);
                    const unsigned dd = pk_bf16(bf_lo(xp) - m0, bf_hi(xp) - m1);
                    *(LAS unsigned*)(lds0 + c * CV_CH + CV_U + 480 + row * 2) = pk_bf16(bf_lo(dd) * s0, bf_hi(dd) * s1);
                    *(LAS unsigned*)(lds0 + c * CV_CH + CV_X + row * 2) = xp;
                }
            }
        }
    }
    LGKM0(); BAR();
    unsigned fwv = tag;
    if (w == 11 && lane < 36) fwv = __hip_atomic_load((gu32*)(a.ws + WS_FW) + lane, RLX_AGENT);
    f32x4 acc[8];
#pragma unroll
    for (int c = 0; c < 8; ++c) acc[c] = (f32x4){0.f, 0.f, 0.f, 0.f};
    const int cpy = (3 - p) & 3;
#pragma unroll
    for (int rr = 0; rr < 2; ++rr) {
        const int r = 2 * wq + rr;
        const LAS char* ab = lds + cpy * CV_RC + 2 * (2031 - 32 * r + 8 * g - p - cpy) - 512 * 7;
        const LAS char* fb = lds + CV_U + 2 * (240 - 32 * r + 16 * p - 16 + 8 * g);
        bf16x8 A[8], Fm[8];
#pragma unroll
        for (int s = 0; s < 8; ++s) {
            const s16x4 lo = *(const volatile LAS s16x4*)(ab + 512 * (7 - s));
            const s16x4 hi = *(const volatile LAS s16x4*)(ab + 512 * (7 - s) + 8);
            A[s] = cat8(lo, hi);
        }
#pragma unroll
        for (int e = 0; e < 8; ++e) Fm[e] = *(const LAS bf16x8*)(fb + 512 * e);
#pragma unroll
        for (int c = 0; c < 8; ++c)
#pragma unroll
            for (int s = 0; s <= c; ++s) acc[c] = MFMA16(A[s], Fm[c - s], acc[c]);
    }
    LAS f32x4* red = (LAS f32x4*)(lds + CV_RED) + lane;
    f32x4 own[2];
#define RED_CASE(WQ) { _Pragma("unroll") for (int c = 0; c < 8; ++c) if ((c >> 1) != WQ) red[((c >> 1) * 6 + (c & 1) * 3 + (WQ > (c >> 1) ? WQ - 1 : WQ)) * 64] = acc[c]; \
                       own[0] = acc[2 * WQ]; own[1] = acc[2 * WQ + 1]; }
    if (wq == 0) RED_CASE(0) else if (wq == 1) RED_CASE(1) else if (wq == 2) RED_CASE(2) else RED_CASE(3)
#undef RED_CASE
    if (w == 11) { const bool okw = __all(fwv == tag); if (lane == 0) *(LAS unsigned*)(lds0 + LDS_FWOK) = okw ? 1u : 0u; }
    LGKM0(); BAR();
    const rsrc_t rh = mk_rsrc(a.ws + WS_HT + (size_t)ch * L * 2, L * 2);
    const float dsk = bf2f(r_ds);
#pragma unroll
    for (int i = 0; i < 2; ++i) {
        f32x4 y = own[i];
#pragma unroll
        for (int k = 0; k < 3; ++k) y += red[(wq * 6 + i * 3 + k) * 64];
        const int l0 = 256 * (2 * wq + i) + 16 * p + 4 * g;
        const u32x2 uu = *(const LAS u32x2*)(lds + CV_U + 480 + 2 * l0), xv = *(const LAS u32x2*)(lds + CV_X + 2 * l0);
        float o[4];
#pragma unroll
        for (int k = 0; k < 4; ++k) {
            const float u = (k & 1) ? bf_hi(uu[k >> 1]) : bf_lo(uu[k >> 1]);
            const float x = (k & 1) ? bf_hi(xv[k >> 1]) : bf_lo(xv[k >> 1]);
            o[k] = x + __builtin_fmaf(u, dsk, y[k]);
        }
        bst8((u32x2){pk_bf16(o[0], o[1]), pk_bf16(o[2], o[3])}, rh, 2 * l0, 0);
    }
    VM0(); BAR();
    if (w == 0) publish_rep(a.ws, WS_FH, b, tag, lane);
}

__device__ __forceinline__ int nb0_of(int j) { return (9 * j + 1) >> 1; }

constexpr int OUT_ROW = 176;
constexpr int KC_XB = 73728, KC_ST = KC_XB + 20 * 1024, KC_OUT = KC_ST + 512;
__device__ __forceinline__ void phase_c(char* __restrict__ ws, LAS char* lds, int panel, int j, unsigned tag) {
    const int tid = threadIdx.x, w = __builtin_amdgcn_readfirstlane(tid >> 6), lane = tid & 63;
    const int tok0 = panel * 64, nbs = nb0_of(j), nblk = nb0_of(j + 1) - nbs;
    const int g = lane >> 4, i16 = lane & 15, q = i16 >> 2, p = lane & 3;
    const int b = w >> 1, kh = w & 1;
    const bool act = w < 10 && b < nblk;
    const int nb = nbs + (act ? b : 0);
    if (w == 0 && *(const LAS unsigned*)(lds + LDS_FWOK) == 0u) wait_flags(ws, WS_FW, 0, 36, WS_FW, 0, 0, tag);
    BAR();
    bf16x8 W[9];
    f32x4 cs0, cs1;
    if (w < 10) {
        const rsrc_t rw = mk_rsrc(ws + WS_WF, WSUB * 2);
#pragma unroll
        for (int i = 0; i < 9; ++i) W[i] = bld16<SC1>(rw, lane * 16, ((kh * 9 + i) * 36 + nb) * 1024);
        const rsrc_t rc = mk_rsrc(ws + WS_CS, 2 * (D + F) * 4);
        cs0 = bldf4<SC1>(rc, g * 16, nb * 64); cs1 = bldf4<SC1>(rc, g * 16, nb * 64 + (D + F) * 4);
    }
    if (w == 11) wait_flags(ws, WS_FH, (blockIdx.x & (REP - 1)) * 256, 192, WS_FH, 0, 0, tag);
    BAR();
    {
        const rsrc_t rh = mk_rsrc(ws + WS_HT, D * L * 2);
        const int fl = (lane >> 4) & 1;
        const int v0 = (lane >> 3) * 4096 + (((lane & 7) ^ (fl << 1)) * 16), v1 = (lane >> 3) * 4096 + (((lane & 7) ^ ((fl | 2) << 1)) * 16);
#pragma unroll
        for (int k = 0; k < 6; ++k) { const int idx = 6 * w + k; blds16<SC1>(rh, lds + idx * 1024, (k & 1) ? v1 : v0, idx * 32768 + tok0 * 2); }
    }
    VM0(); BAR();
    const int f = ((q >> 1) & 1) | ((g & 1) << 1);
    const int lo_ = (8 * g + q) * 128 + 8 * (p & 1);
    f32x4 acc[4] = {};
    LAS f32x2* st = (LAS f32x2*)(lds + KC_ST);
    LAS f32x4* xb = (LAS f32x4*)(lds + KC_XB);
    if (w < 10) {
        const LAS char* hb[4];
#pragma unroll
        for (int mt = 0; mt < 4; ++mt) hb[mt] = lds + kh * 9 * 4096 + lo_ + (((2 * mt + (p >> 1)) ^ (f << 1)) * 16);
        bf16x8 H[2][2][4];
#pragma unroll
        for (int c = 0; c < 6; ++c) {
            if (c < 5) {
#pragma unroll
                for (int i = 0; i < 2; ++i)
                    if (2 * c + i < 9) {
#pragma unroll
                        for (int mt = 0; mt < 4; ++mt) H[c & 1][i][mt] = cat8(tr_read(hb[mt] + (2 * c + i) * 4096), tr_read(hb[mt] + (2 * c + i) * 4096 + 512));
                    }
            }
            __builtin_amdgcn_sched_barrier(0);
            if (c > 0) {
#pragma unroll
                for (int i = 0; i < 2; ++i)
                    if (2 * (c - 1) + i < 9) {
#pragma unroll
                        for (int mt = 0; mt < 4; ++mt) acc[mt] = MFMA16(W[2 * (c - 1) + i], H[(c - 1) & 1][i][mt], acc[mt]);
                    }
            }
            __builtin_amdgcn_sched_barrier(0);
        }
        xb[((w * 2 + 0) * 64) + lane] = sel4(kh, acc[0], acc[2]);
        xb[((w * 2 + 1) * 64) + lane] = sel4(kh, acc[1], acc[3]);
    } else {
        __builtin_amdgcn_s_setprio(3);
        const bf16x8 ones = {0x3f80, 0x3f80, 0x3f80, 0x3f80, 0x3f80, 0x3f80, 0x3f80, 0x3f80};
        const int m0 = 2 * (w - 10);
        const LAS char* h0 = lds + lo_ + (((2 * m0 + (p >> 1)) ^ (f << 1)) * 16);
        const LAS char* h1 = lds + lo_ + (((2 * m0 + 2 + (p >> 1)) ^ (f << 1)) * 16);
        bf16x8 H[2][3][2];
#pragma unroll
        for (int c = 0; c < 7; ++c) {
            if (c < 6) {
#pragma unroll
                for (int i = 0; i < 3; ++i) {
                    H[c & 1][i][0] = cat8(tr_read(h0 + (3 * c + i) * 4096), tr_read(h0 + (3 * c + i) * 4096 + 512));
                    H[c & 1][i][1] = cat8(tr_read(h1 + (3 * c + i) * 4096), tr_read(h1 + (3 * c + i) * 4096 + 512));
                }
            }
            __builtin_amdgcn_sched_barrier(0);
            if (c > 0) {
#pragma unroll
                for (int i = 0; i < 3; ++i) {
                    const bf16x8 H0 = H[(c - 1) & 1][i][0], H1 = H[(c - 1) & 1][i][1];
                    acc[0] = MFMA16(ones, H0, acc[0]); acc[1] = MFMA16(H0, H0, acc[1]);
                    acc[2] = MFMA16(ones, H1, acc[2]); acc[3] = MFMA16(H1, H1, acc[3]);
                }
            }
            __builtin_amdgcn_sched_barrier(0);
        }
        const float sm[2] = {acc[0][0], acc[2][0]};
        if (g == q) {
            const rsrc_t r2 = mk_rsrc(ws + WS_ST2, L * 8);
#pragma unroll
            for (int t = 0; t < 2; ++t) {
                const f32x4 d = acc[2 * t + 1];
                const float s2 = p == 0 ? d[0] : p == 1 ? d[1] : p == 2 ? d[2] : d[3];
                const float mean = sm[t] * (1.0f / D), var = fmaxf(s2 * (1.0f / D) - mean * mean, 0.f);
                const float mb = rbf(mean), rs = __builtin_amdgcn_rcpf(ln_sd(var));
                const float m2 = rbf((mean - mb) * rs), rs2 = __builtin_amdgcn_rcpf(ln_sd(var * rs * rs));
                st[(m0 + t) * 16 + i16] = (f32x2){rs * rs2, (mb * rs + m2) * rs2};
                if (j == 0) bst8((u32x2){__float_as_uint(mb), __float_as_uint(rs)}, r2, (tok0 + (m0 + t) * 16 + i16) * 8, 0);
            }
        }
    }
    __builtin_amdgcn_s_setprio(0);
    LGKM0(); BAR();
    const rsrc_t rg = mk_rsrc(ws + WS_G, (L + 4) * D * 2);
    LAS char* og = lds + KC_OUT;
    if (act) {
        const f32x4 cs = cs0 + cs1;
#pragma unroll
        for (int t = 0; t < 2; ++t) {
            const f32x4 a = sel4(kh, acc[2 + t], acc[t]) + xb[((w ^ 1) * 2 + t) * 64 + lane];
            const f32x2 ab = st[(2 * kh + t) * 16 + i16];
            float o[4];
#pragma unroll
            for (int k = 0; k < 4; ++k) o[k] = gelu_tanh(ab[0] * a[k] - ab[1] * cs[k]);
            *(LAS u32x2*)(og + (kh * 32 + t * 16 + i16) * OUT_ROW + b * 32 + g * 8) = (u32x2){pk_bf16(o[0], o[1]), pk_bf16(o[2], o[3])};
        }
    }
    LGKM0(); BAR();
    {
        const int nc = 2 * nblk, row = nblk == 5 ? tid / 10 : tid >> 3, c = tid - row * nc;
        if (row < 64) bst16(*(const LAS u32x4*)(og + row * OUT_ROW + c * 16), rg, row * (D * 2) + c * 16, ((2 + tok0) * D + nbs * 16) * 2);
        if ((panel == 0 || panel == 31) && tid >= 704 && tid < 704 + 2 * nc) {
            const int z = tid - 704, zr = z >= nc ? 1 : 0;
            bst16((u32x4){0u, 0u, 0u, 0u}, rg, zr * (D * 2) + (z - zr * nc) * 16, ((panel == 0 ? 0 : L + 2) * D + nbs * 16) * 2);
        }
    }
    VM0(); BAR();
    publish(ws, WS_FG, panel * 32 + j, tag);
}

constexpr int KD_HT = 73728, KD_XB = KD_HT + 10240, KD_OUT = KD_XB + 20 * 1024;
__device__ __forceinline__ void phase_d(char* __restrict__ ws, LAS char* lds, int panel, int j, int je, unsigned tag) {
    const int tid = threadIdx.x, w = __builtin_amdgcn_readfirstlane(tid >> 6), lane = tid & 63;
    const int tok0 = panel * 64, nbs = nb0_of(j), nblk = nb0_of(j + 1) - nbs;
    const int g = lane >> 4, i16 = lane & 15, q = i16 >> 2, p = lane & 3;
    const int b = w >> 1, kh = w & 1;
    const bool act = w < 10 && b < nblk;
    const int nb = nbs + (act ? b : 0);
    bf16x8 W[9];
    if (w < 10) {
        const rsrc_t rw = mk_rsrc(ws + WS_WF + WSUB * 2, WSUB * 2);
#pragma unroll
        for (int i = 0; i < 9; ++i) W[i] = bld16<SC1>(rw, lane * 16, ((kh * 9 + i) * 36 + nb) * 1024);
    }
    if (w < 2 * nblk) {
        const rsrc_t rh = mk_rsrc(ws + WS_HT, D * L * 2);
        const int fl = ((lane >> 4) & 1) | ((w & 1) << 1);
        blds16<SC1>(rh, lds + KD_HT + w * 1024, (lane >> 3) * 4096 + (((lane & 7) ^ (fl << 1)) * 16), (nbs * 16 + w * 8) * 4096 + tok0 * 2);
    }
    if (w == 11) {
        const int p0 = panel > 0 ? panel - 1 : 0, p1 = panel < 31 ? panel + 1 : 31;
        wait_panels(ws, WS_FG, p0, WS_FG, panel, WS_FG, p1, tag);
    }
    BAR();
    f32x2 st[2];
    {
        const rsrc_t rg = mk_rsrc(ws + WS_G, (L + 4) * D * 2);
        const int c7 = (lane & 7) ^ ((lane >> 3) & 6), vb = (lane >> 3) * 1152 + c7 * 16;
#pragma unroll
        for (int k = 0; k < 6; ++k) {
            const int idx = 6 * w + k, cg = idx >> 3, c = cg * 8 + c7;
            const int o2 = (0x4031 >> (((c * 57) >> 10) * 4)) & 15;
            blds16<SC1>(rg, lds + idx * 1024, vb + o2 * 1152, (tok0 + (idx & 7) * 8) * 1152 + cg * 128);
        }
        const rsrc_t r2 = mk_rsrc(ws + WS_ST2, L * 8);
#pragma unroll
        for (int t = 0; t < 2; ++t) st[t] = __builtin_bit_cast(f32x2, bldu2<SC1>(r2, i16 * 8, (tok0 + kh * 32 + t * 16) * 8));
    }
    VM0();
    asm volatile("" : "+v"(st[0]), "+v"(st[1]));
    BAR();
    f32x4 acc[4] = {};
    LAS f32x4* xb = (LAS f32x4*)(lds + KD_XB);
    if (w < 10) {
        const int z = i16 & 6;
        const LAS char* a0 = lds + i16 * 128 + ((g ^ z) * 16), * a1 = lds + i16 * 128 + (((4 + g) ^ z) * 16);
        const LAS char* e0 = kh ? a1 + 4 * 8192 : a0;
        const LAS char* e1 = kh ? a0 + 5 * 8192 : a1;
        bf16x8 A[2][2][4];
#pragma unroll
        for (int c = 0; c < 6; ++c) {
            if (c < 5) {
#pragma unroll
                for (int ii = 0; ii < 2; ++ii) {
                    const int i = 2 * c + ii;
                    if (i < 9) {
#pragma unroll
                        for (int mt = 0; mt < 4; ++mt) A[c & 1][ii][mt] = *(const LAS bf16x8*)(((i & 1) ? e1 : e0) + (i >> 1) * 8192 + mt * 2048);
                    }
                }
            }
            __builtin_amdgcn_sched_barrier(0);
            if (c > 0) {
#pragma unroll
                for (int ii = 0; ii < 2; ++ii)
                    if (2 * (c - 1) + ii < 9) {
#pragma unroll
                        for (int mt = 0; mt < 4; ++mt) acc[mt] = MFMA16(W[2 * (c - 1) + ii], A[(c - 1) & 1][ii][mt], acc[mt]);
                    }
            }
            __builtin_amdgcn_sched_barrier(0);
        }
        xb[((w * 2 + 0) * 64) + lane] = sel4(kh, acc[0], acc[2]);
        xb[((w * 2 + 1) * 64) + lane] = sel4(kh, acc[1], acc[3]);
    }
    LGKM0(); BAR();
    LAS char* ox = lds + KD_OUT;
    if (w < 10) {
        const int bl = act ? b : 0;
        const int hrow = 16 * bl + 4 * g + q;
        const LAS char* hb = lds + KD_HT + hrow * 128 + 8 * (p & 1);
        const rsrc_t rp = mk_rsrc(ws + WS_PST, 32 * 36 * 64 * 8);
#pragma unroll
        for (int t = 0; t < 2; ++t) {
            const int mt = 2 * kh + t;
            const f32x4 a = sel4(kh, acc[2 + t], acc[t]) + xb[((w ^ 1) * 2 + t) * 64 + lane];
            const u32x2 hu = __builtin_bit_cast(u32x2, tr_read(hb + (((2 * mt + (p >> 1)) ^ (fsw(hrow) << 1)) * 16)));
            const float rs = st[t][1], nm = -st[t][0] * rs;
            u32x2 ov;
#pragma unroll
            for (int k = 0; k < 2; ++k)
                ov[k] = pk_bf16(__builtin_fmaf(bf_lo(hu[k]), rs, nm) + a[2 * k], __builtin_fmaf(bf_hi(hu[k]), rs, nm) + a[2 * k + 1]);
            float s1 = dot2bf(ov[1], 0x3f803f80u, dot2bf(ov[0], 0x3f803f80u, 0.f)), s2 = dot2bf(ov[1], ov[1], dot2bf(ov[0], ov[0], 0.f));
            s1 = rsum_g(s1); s2 = rsum_g(s2);
            if (act) {
                *(LAS u32x2*)(ox + (mt * 16 + i16) * OUT_ROW + b * 32 + g * 8) = ov;
                if (lane < 16) bst8((u32x2){__float_as_uint(s1), __float_as_uint(s2)}, rp, lane * 8, ((panel * 36 + nb) * 64 + mt * 16) * 8);
            }
        }
    }
    LGKM0(); BAR();
    {
        const int nc = 2 * nblk, row = nblk == 5 ? tid / 10 : tid >> 3, c = tid - row * nc;
        if (row < 64) {
            const int so = (tok0 * D + nbs * 16) * 2, vo = row * (D * 2) + c * 16;
            bst16(*(const LAS u32x4*)(ox + row * OUT_ROW + c * 16), mk_rsrc(ws + WS_X1, L * D * 2), vo, so);
        }
    }
    if (w == 10) wait_flags(ws, WS_FW, (2 + 2 * je) * 18, 18, WS_FW, 0, 0, tag);
    VM0(); BAR();
    publish(ws, WS_FX, panel * 32 + j, tag);
}

constexpr int BG_LDS = 131072;
__device__ __forceinline__ void phase_cd_background(const Args& a, LAS char* lds, int panel, int job, unsigned tag) {
    char* ws = a.ws;
    const int tid = threadIdx.x, w = __builtin_amdgcn_readfirstlane(tid >> 6), lane = tid & 63, tok0 = panel * 64;
    BAR(); BAR();
    {
        const rsrc_t rh = mk_rsrc(ws + WS_HT, D * L * 2);
        const int fl = (lane >> 4) & 1;
        const int v0 = (lane >> 3) * 4096 + (((lane & 7) ^ (fl << 1)) * 16), v1 = (lane >> 3) * 4096 + (((lane & 7) ^ ((fl | 2) << 1)) * 16);
#pragma unroll
        for (int k = 0; k < 6; ++k) { const int idx = 6 * w + k; blds16<SC1>(rh, lds + idx * 1024, (k & 1) ? v1 : v0, idx * 32768 + tok0 * 2); }
    }
    VM0(); BAR();
    PrepRegs P;
    prep_issue(a, job, lane, P);
    BAR(); BAR(); BAR();
    prep_finish<false, false>(a, job, lds + BG_LDS + (w - 8) * 8192, lane, tag, P);
    BAR();
    {
        const rsrc_t rg = mk_rsrc(ws + WS_G, (L + 4) * D * 2);
        const int c7 = (lane & 7) ^ ((lane >> 3) & 6), vb = (lane >> 3) * 1152 + c7 * 16;
#pragma unroll
        for (int k = 0; k < 6; ++k) {
            const int idx = 6 * w + k, cg = idx >> 3, c = cg * 8 + c7;
            const int o2 = (0x4031 >> (((c * 57) >> 10) * 4)) & 15;
            blds16<SC1>(rg, lds + idx * 1024, vb + o2 * 1152, (tok0 + (idx & 7) * 8) * 1152 + cg * 128);
        }
    }
    VM0();
    if (lane == 0) __hip_atomic_store((gu32*)(ws + WS_FW) + job, tag, RLX_AGENT);
    BAR(); BAR(); BAR(); BAR();
}

constexpr int KE_HID = 73728, KE_ST = 2 * 73728, KE_FLG = KE_ST + 1024;
constexpr int NB = 3, RING = 4;
__device__ __forceinline__ void phase_e(char* __restrict__ ws, LAS char* lds, int panel, int j, unsigned tag) {
    const int w = __builtin_amdgcn_readfirstlane(threadIdx.x >> 6), lane = threadIdx.x & 63;
    constexpr int S1 = 6, S2 = 12;
    const int tok0 = panel * 64, g = lane >> 4, i16 = lane & 15, nbs = NB * w;
    const rsrc_t rw = mk_rsrc(ws + WS_WF + (size_t)(2 + 2 * j) * WSUB * 2, 2 * WSUB * 2);
    const int wv = lane * 16, wsb = nbs * 1024;
    bf16x8 W[RING][NB];
#pragma unroll
    for (int s = 0; s < RING; ++s)
#pragma unroll
        for (int b = 0; b < NB; ++b) W[s][b] = bld16<SC1>(rw, wv + b * 1024, wsb + s * 36864);
    f32x4 acc[NB][4];
    {
        const rsrc_t rc = mk_rsrc(ws + WS_CS, 2 * (D + F) * 4);
#pragma unroll
        for (int b = 0; b < NB; ++b) {
            acc[b][0] = bldf4<SC1>(rc, g * 16 + b * 64, (D + j * D + nbs * 16) * 4);
            acc[b][1] = bldf4<SC1>(rc, g * 16 + b * 64, (D + j * D + nbs * 16 + D + F) * 4);
        }
    }
    __builtin_amdgcn_sched_barrier(0);
    unsigned fw4 = tag;
    if (w == 10 && lane < 18) fw4 = __hip_atomic_load((gu32*)(ws + WS_FW) + (3 + 2 * j) * 18 + lane, RLX_AGENT);
    if (w == 11) wait_panels(ws, WS_FX, panel, WS_FX, panel, WS_FX, panel, tag);
    BAR();
    u32x2 pv[36];
    if (w == 11) {
        const rsrc_t rp = mk_rsrc(ws + WS_PST + (size_t)panel * 36 * 512, 36 * 512);
#pragma unroll
        for (int k = 0; k < 36; ++k) pv[k] = bldu2<SC1>(rp, lane * 8, k * 512);
    }
    {
        const rsrc_t rx = mk_rsrc(ws + WS_X1 + (size_t)tok0 * D * 2, 64 * D * 2);
        const int vo = (lane >> 3) * 1152 + (((lane & 7) ^ ((lane >> 3) & 6)) * 16);
#pragma unroll
        for (int k = 0; k < 6; ++k) {
            const int idx = 6 * w + k;
            blds16<SC1>(rx, lds + idx * 1024, vo, (idx & 7) * 9216 + (idx >> 3) * 128);
        }
    }
    __builtin_amdgcn_sched_barrier(0);
    LAS f32x4* st = (LAS f32x4*)(lds + KE_ST);
    LAS unsigned* flg = (LAS unsigned*)(lds + KE_FLG);
    if (w == 11) {
        float a1[4] = {}, a2[4] = {};
#pragma unroll
        for (int k = 0; k < 36; ++k) { a1[k & 3] += __uint_as_float(pv[k][0]); a2[k & 3] += __uint_as_float(pv[k][1]); }
        const float s1 = (a1[0] + a1[1]) + (a1[2] + a1[3]), s2 = (a2[0] + a2[1]) + (a2[2] + a2[3]);
        const float mean = s1 * (1.0f / D), var = fmaxf(s2 * (1.0f / D) - mean * mean, 0.f);
        const float mb = rbf(mean), sb = ln_sd(var), al = __builtin_amdgcn_rcpf(sb);
        const float k0 = -2.0f * 0.7978845608028654f * 1.4426950408889634f;
        st[lane] = (f32x4){k0 * al, k0 * 0.044715f * al * al * al, sb, -mb};
        if (lane < 3) flg[lane] = 0u;
    }
    if (w == 10) { const bool ok4 = __all(fw4 == tag); if (lane == 0) flg[3] = ok4 ? 1u : 0u; }
    VM0(); LGKM0(); BAR();
    const int z = i16 & 6;
    const int o0 = i16 * 128 + (g ^ z) * 16, o1 = i16 * 128 + ((4 + g) ^ z) * 16;
    {
        float nm[4];
#pragma unroll
        for (int mt = 0; mt < 4; ++mt) nm[mt] = st[mt * 16 + i16][3];
#pragma unroll
        for (int b = 0; b < NB; ++b) {
            const f32x4 cs = acc[b][0] + acc[b][1];
#pragma unroll
            for (int mt = 0; mt < 4; ++mt) acc[b][mt] = cs * nm[mt];
        }
    }
#define E_SPIN(cond) do { while (cond) __builtin_amdgcn_s_sleep(1); __builtin_amdgcn_sched_barrier(0); asm volatile("" ::: "memory"); } while (0)
#define E_FLG(i) __hip_atomic_load(flg + (i), __ATOMIC_RELAXED, __HIP_MEMORY_SCOPE_WORKGROUP)
    bf16x8 A[2][4];
#pragma unroll
    for (int ph = 0; ph < 2; ++ph) {
        const LAS char* ab = lds + ph * KE_HID;
        if (ph == 1) E_SPIN(E_FLG(0) < 4u);
#pragma unroll
        for (int mt = 0; mt < 4; ++mt) A[0][mt] = *(const LAS bf16x8*)(ab + o0 + mt * 2048);
#pragma unroll
        for (int s = 0; s < 18; ++s) {
            if (ph == 1 && s + 1 == S1) E_SPIN(E_FLG(1) < 4u);
            if (ph == 1 && s + 1 == S2) E_SPIN(E_FLG(2) < 4u);
            if (ph == 0 && s == 2 && w == 8 && E_FLG(3) == 0u) {
                wait_flags(ws, WS_FW, (3 + 2 * j) * 18, 18, WS_FW, 0, 0, tag);
                if (lane == 0) __hip_atomic_store(flg + 3, 1u, __ATOMIC_RELAXED, __HIP_MEMORY_SCOPE_WORKGROUP);
            }
            if (ph == 0 && s == 18 - RING) E_SPIN(E_FLG(3) == 0u);
            if (s + 1 < 18) {
#pragma unroll
                for (int mt = 0; mt < 4; ++mt) A[(s + 1) & 1][mt] = *(const LAS bf16x8*)(ab + (((s + 1) & 1) ? o1 : o0) + mt * 2048 + ((s + 1) >> 1) * 8192);
            }
            const int t = ph * 18 + s;
#pragma unroll
            for (int b = 0; b < NB; ++b) {
#pragma unroll
                for (int mt = 0; mt < 4; ++mt) acc[b][mt] = MFMA16(W[t % RING][b], A[s & 1][mt], acc[b][mt]);
                if (t + RING < 36) W[t % RING][b] = bld16<SC1>(rw, wv + b * 1024, wsb + (t + RING) * 36864);
            }
            __builtin_amdgcn_sched_barrier(0);
        }
        if (ph == 0) {
#pragma unroll
            for (int mt = 0; mt < 4; ++mt) {
                const f32x4 k = st[mt * 16 + i16];
#pragma unroll
                for (int b = 0; b < NB; ++b) {
                    float o[4];
#pragma unroll
                    for (int e = 0; e < 4; ++e) {
                        const float a = acc[b][mt][e];
                        const float ex = __builtin_amdgcn_exp2f(a * (k[0] + k[1] * (a * a)));
                        o[e] = a * __builtin_amdgcn_rcpf(k[2] + k[2] * ex);
                    }
                    const int nb = nbs + b;
                    *(LAS u32x2*)(lds + KE_HID + (nb >> 2) * 8192 + (mt * 16 + i16) * 128 + (((2 * (nb & 3) + (g >> 1)) ^ z) * 16) + 8 * (g & 1)) =
                        (u32x2){pk_bf16(o[0], o[1]), pk_bf16(o[2], o[3])};
                    acc[b][mt] = (f32x4){0.f, 0.f, 0.f, 0.f};
                }
            }
            LGKM0();
            if (lane == 0) __hip_atomic_fetch_add(flg + (w >> 2), 1u, __ATOMIC_RELAXED, __HIP_MEMORY_SCOPE_WORKGROUP);
            if (w >= 8) __builtin_amdgcn_s_setprio(2); else if (w >= 4) __builtin_amdgcn_s_setprio(1);
        }
    }
#undef E_SPIN
#undef E_FLG
    __builtin_amdgcn_s_setprio(0);
    const rsrc_t rp = mk_rsrc(ws + WS_PART + (size_t)(panel * 8 + j) * 72 * 1024, 72 * 1024);
#pragma unroll
    for (int b = 0; b < NB; ++b)
#pragma unroll
        for (int m2 = 0; m2 < 2; ++m2) {
            const f32x4 a0 = acc[b][2 * m2], a1 = acc[b][2 * m2 + 1];
            const u32x4 v = {pk_bf16(a0[0], a0[1]), pk_bf16(a0[2], a0[3]), pk_bf16(a1[0], a1[1]), pk_bf16(a1[2], a1[3])};
            const int pr = (nbs + b) * 2 + m2;
            if (pr >= 9 * j && pr < 9 * j + 9) *(LAS u32x4*)(lds + (pr - 9 * j) * 1024 + lane * 16) = v;
            else bst16(v, rp, lane * 16, pr * 1024);
        }
    LGKM0();
    VM0(); BAR();
    publish(ws, WS_FP, panel * 32 + j, tag);
}

constexpr int KF_HT = 16384;
__device__ __forceinline__ void phase_f(char* __restrict__ ws, LAS char* lds, u16* __restrict__ out, int panel, int jj, unsigned tag) {
    const int tid = threadIdx.x, w = __builtin_amdgcn_readfirstlane(tid >> 6), lane = tid & 63;
    const int g = lane >> 4, i16 = lane & 15, q = i16 >> 2, p = lane & 3;
    const int pr = jj * 9 + (w < 9 ? w : 0), nb = pr >> 1, m2 = pr & 1, nbs = (jj * 9) >> 1;
    u32x2 xa[2];
    const int so = ((panel * 64 + m2 * 32) * D + nb * 16) * 2, vo = (i16 * D + 4 * g) * 2;
    if (w < 10) {
        const rsrc_t rh = mk_rsrc(ws + WS_HT, D * L * 2);
        const int fl = ((lane >> 4) & 1) | ((w & 1) << 1);
        blds16<SC1>(rh, lds + KF_HT + w * 1024, (lane >> 3) * 4096 + (((lane & 7) ^ (fl << 1)) * 16), (nbs * 16 + w * 8) * 4096 + panel * 128);
    }
    if (w < 9) {
        const rsrc_t r1 = mk_rsrc(ws + WS_X1, L * D * 2);
#pragma unroll
        for (int t = 0; t < 2; ++t) xa[t] = bldu2<SC1>(r1, vo, so + t * 16 * D * 2);
    }
    if (w == 11) wait_panels(ws, WS_FP, panel, WS_FP, panel, WS_FP, panel, tag);
    VM0(); BAR();
    if (w < 9) {
        const rsrc_t rp = mk_rsrc(ws + WS_PART + (size_t)(panel * 8) * 72 * 1024, 8 * 72 * 1024);
        u32x4 v[8];
#pragma unroll
        for (int k = 0; k < 7; ++k) v[k] = bldu4<SC1>(rp, lane * 16, ((k < jj ? k : k + 1) * 72 + pr) * 1024);
        v[7] = *(const LAS u32x4*)(lds + w * 1024 + lane * 16);
        const int hrow = 16 * (nb - nbs) + 4 * g + q;
        u32x2 ha[2];
#pragma unroll
        for (int t = 0; t < 2; ++t)
            ha[t] = __builtin_bit_cast(u32x2, tr_read(lds + KF_HT + hrow * 128 + (((2 * (2 * m2 + t) + (p >> 1)) ^ (fsw(hrow) << 1)) * 16) + 8 * (p & 1)));
        float s[8] = {};
#pragma unroll
        for (int k = 0; k < 8; ++k)
#pragma unroll
            for (int e = 0; e < 4; ++e) { s[2 * e] += bf_lo(v[k][e]); s[2 * e + 1] += bf_hi(v[k][e]); }
#pragma unroll
        for (int t = 0; t < 2; ++t) {
            float o[4];
#pragma unroll
            for (int k = 0; k < 4; ++k) {
                const float x1 = (k & 1) ? bf_hi(xa[t][k >> 1]) : bf_lo(xa[t][k >> 1]);
                const float h = (k & 1) ? bf_hi(ha[t][k >> 1]) : bf_lo(ha[t][k >> 1]);
                o[k] = h + (x1 + s[4 * t + k]);
            }
            __builtin_amdgcn_raw_buffer_store_b64((u32x2){pk_bf16(o[0], o[1]), pk_bf16(o[2], o[3])}, mk_rsrc(out, L * D * 2), vo, so + t * 16 * D * 2, 0);
        }
    }
}

constexpr int LDS_CTRL = 3 * CV_CH > KE_FLG + 32 ? 3 * CV_CH : KE_FLG + 32;
constexpr int LDS_BYTES = LDS_CTRL + 64;

NOPK __global__ __launch_bounds__(NTHR) void mega_kernel(Args a) {
    extern __shared__ __attribute__((aligned(16))) char smem[];
    LAS char* lds = (LAS char*)smem;
    const int tid = threadIdx.x, w = __builtin_amdgcn_readfirstlane(tid >> 6), lane = tid & 63, b = blockIdx.x;
    unsigned cpf = 0;
    if (w == 8) {
        unsigned long long pc;
        asm volatile("s_getpc_b64 %0" : "=s"(pc));
        const int line = (b >> 3) * 13 + lane;
        if (lane < 13 && line * 128 < CODE_PF) cpf = *(const GAS unsigned*)((pc & ~255ull) + (unsigned long long)line * 128);
    }
    const unsigned tagv = __hip_atomic_load((gu32*)(a.ws + WS_EPOCH), RLX_AGENT);
    const StatRow sr = row_stats_load(a, w, lane);
    __builtin_amdgcn_sched_barrier(0);
    unsigned tag;
    if (b < 192) phase_conv(a, lds, tagv, sr, tag);
    else {
        tag = __builtin_amdgcn_readfirstlane(tagv) + 1u;
        if (tid == 0) __hip_atomic_store((gu32*)(a.ws + WS_FA) + b, tag, RLX_AGENT);
        if (tid == 0) *(LAS unsigned*)(lds + LDS_FWOK) = 0u;
        row_stats(a, sr, tag, w, lane);
        LGKM0(); BAR();
        const int i = w * 64 + (b - 192);
        if (i >= 36) {
            const unsigned long long t0 = __builtin_amdgcn_s_memrealtime();
            while (__builtin_amdgcn_s_memrealtime() - t0 < PREP_DELAY) __builtin_amdgcn_s_sleep(8);
        }
        if (i < 180) {
            const int job = i < 36 ? i : (2 + 2 * ((i - 36) / 18)) * 18 + (i - 36) % 18;
            PrepRegs P;
            prep_issue(a, job, lane, P);
            prep_finish<true, true>(a, job, lds + w * 8192, lane, tag, P);
        }
        BAR();
    }
    asm volatile("" :: "v"(cpf));
    const int je = 2 * (b & 3) + ((b >> 3) & 1);
    {
        const int panel = (b & 7) * 4 + (b >> 6), j = (b >> 3) & 7;
        const int m = (w - 8) * 128 + ((b & 7) | ((b >> 4) << 3));
        if ((j & 1) && (w == 8 || w == 9) && m < 144) phase_cd_background(a, lds, panel, (3 + 2 * (m / 18)) * 18 + m % 18, tag);
        else {
            phase_c(a.ws, lds, panel, j, tag);
            phase_d(a.ws, lds, panel, j, je, tag);
        }
    }
    phase_e(a.ws, lds, ((b >> 2) & 1) * 16 + (b >> 4), je, tag);
    phase_f(a.ws, lds, a.out, ((b >> 2) & 1) * 16 + (b >> 4), je, tag);
    if (b == 0 && w == 10) {
        wait_flags(a.ws, WS_FA, 0, 256, WS_FA, 0, 0, tag);
        if (lane == 0) __hip_atomic_store((gu32*)(a.ws + WS_EPOCH), tag, RLX_AGENT);
    }
}

extern "C" void kernel_launch(void* const* d_in, const int* in_sizes, int n_in,
                              void* d_out, int out_size, void* d_ws, size_t ws_size,
                              hipStream_t stream) {
    Args a;
    a.x = (const u16*)d_in[0];
    a.are = (const u16*)d_in[1]; a.aim = (const u16*)d_in[2];
    a.bre = (const u16*)d_in[3]; a.bim = (const u16*)d_in[4];
    a.cre = (const u16*)d_in[5]; a.cim = (const u16*)d_in[6];
    a.logdt = (const u16*)d_in[7]; a.dskip = (const u16*)d_in[8];
    a.w1 = (const u16*)d_in[10]; a.w2 = (const u16*)d_in[11];
    a.w3 = (const u16*)d_in[12]; a.w4 = (const u16*)d_in[13];
    a.ws = (char*)d_ws; a.out = (u16*)d_out;
    (void)hipFuncSetAttribute((const void*)mega_kernel, hipFuncAttributeMaxDynamicSharedMemorySize, LDS_BYTES);
    mega_kernel<<<NWG, NTHR, LDS_BYTES, stream>>>(a);
}
```
